```python
import torch

def _softmax(x, axis=-1):
    return torch.softmax(x, dim=axis)
def _gelu(x):
    return torch.nn.functional.gelu(x, approximate="tanh")
def _silu(x):
    return torch.nn.functional.silu(x)
def _sigmoid(x):
    return torch.sigmoid(x)
def _layernorm(x, axis=-1, eps=1e-5):
    m = x.mean(dim=axis, keepdim=True)
    v = x.var(dim=axis, keepdim=True, unbiased=False)
    return (x - m) / torch.sqrt(v + eps)
def _scatter_add(tgt, idx, src, axis=0):
    return tgt.clone().index_add_(axis, idx, src)
def _segment_sum(src, seg, n):
    out = torch.zeros((n,) + src.shape[1:], dtype=src.dtype)
    return out.index_add_(0, seg, src)
def _pad(x, pw):
    flat = tuple(v for pair in reversed(pw) for v in pair)
    return torch.nn.functional.pad(x, flat)

kernel_name = 'interleave_layers__spectral_gfnet__dense_activation_gelu__prompt'

def setup_inputs():

    _w = (lambda a: (lambda t: t.to(torch.bfloat16) if t.is_floating_point() else t)(torch.as_tensor(a)))
    def _A_setup():
        B, S, D = 1, 2048, 512
        Df = D // 2 + 1
        return {
            "x": _w((lambda *s: torch.randn(*s, dtype=torch.bfloat16) * 0.05)(B, S, D)),
            "w_r": _w((lambda *s: torch.randn(*s, dtype=torch.bfloat16) * 0.4)(S, Df) * 0.02),
            "w_i": _w((lambda *s: torch.randn(*s, dtype=torch.bfloat16) * 0.4)(S, Df) * 0.02),
        }
    _A_in = _A_setup()

    def _B_setup():
        B, S, D, F = 1, 2048, 512, 1792
        return {
            "x": _w((lambda *s: torch.randn(*s, dtype=torch.bfloat16) * 0.05)(B, S, D)),
            "w1": _w((lambda *s: torch.randn(*s, dtype=torch.bfloat16) * 0.4)(D, F) * (D ** -0.5)),
            "b1": _w((lambda *s: torch.randn(*s, dtype=torch.bfloat16) * 12.8)(F) * 0.01),
            "w2": _w((lambda *s: torch.randn(*s, dtype=torch.bfloat16) * 0.1)(F, D) * (F ** -0.5)),
            "b2": _w((lambda *s: torch.randn(*s, dtype=torch.bfloat16) * 0.8)(D) * 0.01),
        }
    _B_in = _B_setup()

    return {
        "A_x": _A_in["x"],
        "A_w_r": _A_in["w_r"],
        "A_w_i": _A_in["w_i"],
        "B_x": _B_in["x"],
        "B_w1": _B_in["w1"],
        "B_b1": _B_in["b1"],
        "B_w2": _B_in["w2"],
        "B_b2": _B_in["b2"],
    }

def reference(A_x, A_w_r, A_w_i, B_x, B_w1, B_b1, B_w2, B_b2):
    def _A_ref(x, w_r, w_i):
        B, S, D = x.shape
        X = (lambda x, axes=(-2,-1): torch.fft.rfft2(x.float(), dim=axes))(x, axes=(-2, -1))
        W = w_r + 1j * w_i
        X = X * W
        y = (lambda x, s, axes=(-2,-1): torch.fft.irfft2(x, s=s, dim=axes).to(torch.bfloat16))(X, (S, D), axes=(-2, -1))
        return y

    def _B_ref(x, w1, b1, w2, b2):
        h = torch.einsum("bsd,df->bsf", x, w1) + b1
        h = _gelu(h)
        return torch.einsum("bsf,fd->bsd", h, w2) + b2

    x = A_x
    x = x + _A_ref(_layernorm(x, axis=-1), A_w_r, A_w_i)
    x = x + _B_ref(_layernorm(x, axis=-1), B_w1, B_b1, B_w2, B_b2)
    x = x + _A_ref(_layernorm(x, axis=-1), A_w_r, A_w_i)
    x = x + _B_ref(_layernorm(x, axis=-1), B_w1, B_b1, B_w2, B_b2)
    return x
```

```cpp
#include <hip/hip_runtime.h>
#include <hip/hip_bf16.h>
#include <stdint.h>

typedef unsigned int u32;
typedef float f2 __attribute__((ext_vector_type(2)));
typedef float f4 __attribute__((ext_vector_type(4)));
typedef u32 u2v __attribute__((ext_vector_type(2)));
typedef u32 u4v __attribute__((ext_vector_type(4)));
typedef __bf16 bf2v __attribute__((ext_vector_type(2)));
typedef __bf16 bf4v __attribute__((ext_vector_type(4)));
typedef __bf16 bf8v __attribute__((ext_vector_type(8)));
typedef short s4v __attribute__((ext_vector_type(4)));
#define LAS __attribute__((address_space(3)))
#define DI __device__ __forceinline__

constexpr int S_ = 2048, D_ = 512, F_ = 1792, DF_ = 257;
constexpr int TP = 2048 + 32;

struct cxf { float x, y; };
constexpr double kPi = 3.14159265358979323846264338327950288;
constexpr double c_sin(double x) { double x2 = x * x, t = x, s = x; for (int i = 1; i < 11; ++i) { t *= -x2 / double((2 * i) * (2 * i + 1)); s += t; } return s; }
constexpr double c_cos(double x) { double x2 = x * x, t = 1, s = 1; for (int i = 1; i < 11; ++i) { t *= -x2 / double((2 * i - 1) * (2 * i)); s += t; } return s; }
constexpr cxf tw2048(int i) {
  i &= 2047;
  int qd = i >> 9, r = i & 511;
  double c = 0, s = 0;
  if (r <= 256) { double a = 2 * kPi * r / 2048.0; c = c_cos(a); s = c_sin(a); }
  else { double a = 2 * kPi * (512 - r) / 2048.0; c = c_sin(a); s = c_cos(a); }
  double cc = 0, ss = 0;
  if (qd == 0) { cc = c; ss = s; } else if (qd == 1) { cc = -s; ss = c; } else if (qd == 2) { cc = -c; ss = -s; } else { cc = s; ss = -c; }
  return cxf{(float)cc, (float)(-ss)};
}
template <int R, int C> struct Tab { cxf v[R * C]; };
constexpr Tab<32, 8> mk_r1() { Tab<32, 8> t{}; for (int j = 0; j < 32; ++j) for (int q = 0; q < 8; ++q) t.v[j * 8 + q] = tw2048(8 * j * q); return t; }
constexpr Tab<8, 8> mk_r2() { Tab<8, 8> t{}; for (int j = 0; j < 8; ++j) for (int q = 0; q < 8; ++q) t.v[j * 8 + q] = tw2048(64 * j * q); return t; }
constexpr Tab<256, 1> mk_512() { Tab<256, 1> t{}; for (int c = 0; c < 256; ++c) t.v[c] = tw2048(4 * c); return t; }
constexpr Tab<32, 8> mk_512u() { Tab<32, 8> t{}; for (int u = 0; u < 32; ++u) for (int i = 0; i < 8; ++i) t.v[u * 8 + i] = tw2048(4 * (u + 32 * (i >> 2) + 64 * (i & 3))); return t; }
constexpr Tab<256, 8> mk_x() { Tab<256, 8> t{}; for (int s = 0; s < 256; ++s) for (int k = 0; k < 8; ++k) t.v[s * 8 + k] = tw2048(s * k); return t; }
constexpr Tab<64, 4> mk_c1() { Tab<64, 4> t{}; for (int j = 0; j < 64; ++j) for (int q = 0; q < 4; ++q) t.v[j * 4 + q] = tw2048(8 * j * q); return t; }
constexpr Tab<8, 8> mk_c2() { Tab<8, 8> t{}; for (int j = 0; j < 8; ++j) for (int q = 0; q < 8; ++q) t.v[j * 8 + q] = tw2048(32 * j * q); return t; }

__device__ const __attribute__((aligned(64))) Tab<32, 8> g_twr1 = mk_r1();
__device__ const __attribute__((aligned(64))) Tab<8, 8> g_twr2 = mk_r2();
__device__ const __attribute__((aligned(64))) Tab<256, 1> g_tw512 = mk_512();
__device__ const __attribute__((aligned(64))) Tab<32, 8> g_tw512u = mk_512u();
__device__ const __attribute__((aligned(64))) Tab<256, 8> g_twx = mk_x();
__device__ const __attribute__((aligned(64))) Tab<64, 4> g_twc1 = mk_c1();
__device__ const __attribute__((aligned(64))) Tab<8, 8> g_twc2 = mk_c2();

DI float bf_lo(u32 v) { return __uint_as_float(v << 16); }
DI float bf_hi(u32 v) { return __uint_as_float(v & 0xffff0000u); }
DI float rbf(float x) { return (float)(__bf16)x; }
DI u32 pack2(float a, float b) { bf2v v = {(__bf16)a, (__bf16)b}; return __builtin_bit_cast(u32, v); }
DI f2 cmul(f2 a, f2 b) { return f2{a.x * b.x - a.y * b.y, a.x * b.y + a.y * b.x}; }
DI f2 cmulc(f2 a, f2 b) { return f2{a.x * b.x + a.y * b.y, a.y * b.x - a.x * b.y}; }
template <bool INV> DI f2 twm(f2 a, f2 b) { return INV ? cmulc(a, b) : cmul(a, b); }
template <bool INV> DI f2 muli(f2 a) { return INV ? f2{-a.y, a.x} : f2{a.y, -a.x}; }

template <bool INV> DI void fft4(f2& b0, f2& b1, f2& b2, f2& b3) {
  f2 c0 = b0 + b2, c2 = b0 - b2, c1 = b1 + b3, c3 = muli<INV>(b1 - b3);
  b0 = c0 + c1; b2 = c0 - c1; b1 = c2 + c3; b3 = c2 - c3;
}
template <bool INV> DI void fft8(f2* v) {
  const float h = 0.70710678118654752440f;
  f2 a0 = v[0] + v[4], a4 = v[0] - v[4];
  f2 a1 = v[1] + v[5], a5 = v[1] - v[5];
  f2 a2 = v[2] + v[6], a6 = v[2] - v[6];
  f2 a3 = v[3] + v[7], a7 = v[3] - v[7];
  if (!INV) {
    a5 = f2{(a5.x + a5.y) * h, (a5.y - a5.x) * h};
    a7 = f2{(a7.y - a7.x) * h, (-a7.x - a7.y) * h};
  } else {
    a5 = f2{(a5.x - a5.y) * h, (a5.x + a5.y) * h};
    a7 = f2{(-a7.x - a7.y) * h, (a7.x - a7.y) * h};
  }
  a6 = muli<INV>(a6);
  fft4<INV>(a0, a1, a2, a3);
  fft4<INV>(a4, a5, a6, a7);
  v[0] = a0; v[2] = a1; v[4] = a2; v[6] = a3;
  v[1] = a4; v[3] = a5; v[5] = a6; v[7] = a7;
}
template <int CTRL> DI float dpp_add(float x) {
  return x + __int_as_float(__builtin_amdgcn_update_dpp(0, __float_as_int(x), CTRL, 0xf, 0xf, false));
}
DI float red32(float x) {
  x = dpp_add<0xB1>(x); x = dpp_add<0x4E>(x); x = dpp_add<0x141>(x); x = dpp_add<0x140>(x);
  auto r = __builtin_amdgcn_permlane16_swap(__float_as_uint(x), __float_as_uint(x), false, false);
  return __uint_as_float(r[0]) + __uint_as_float(r[1]);
}
DI void wbar() { __builtin_amdgcn_wave_barrier(); }
DI void layernorm16(const float* v, float* o) {
  float s = 0;
#pragma unroll
  for (int i = 0; i < 16; ++i) s += v[i];
  s = red32(s);
  float mean = s * (1.0f / 512.0f);
  float q = 0;
#pragma unroll
  for (int i = 0; i < 16; ++i) { float d = v[i] - mean; q += d * d; }
  q = red32(q);
  float m = rbf(mean), vr = rbf(q * (1.0f / 512.0f));
  float sd = rbf(sqrtf(rbf(vr + 1e-5f)));
  float ri = 1.0f / sd;
#pragma unroll
  for (int i = 0; i < 16; ++i) o[i] = rbf(rbf(v[i] - m) * ri);
}
DI f2 ldtw(const cxf* p) { return *(const f2*)p; }

constexpr int RP = 288;

DI int f_of(int kap, int g, int j) {
  int u = kap / 7, r = kap % 7;
  if (r < 6) { int ct = 2 * u + (r >= 3), p = r % 3; return 112 * ct + 32 * p + (j < 4 ? 4 * g + j : 16 + 4 * g + (j - 4)); }
  return 112 * (2 * u + (g >> 1)) + 96 + 8 * (g & 1) + j;
}
__global__ __launch_bounds__(256) void k_prep(const unsigned short* __restrict__ wr, const unsigned short* __restrict__ wi,
                                              const unsigned short* __restrict__ w2, u32* __restrict__ Wt, f4* __restrict__ PQ,
                                              unsigned short* __restrict__ W2F) {
  int b = blockIdx.x, t = threadIdx.x;
  if (b < 256) {
    int k0 = b;
    int tt = (k0 & 7) * 32 + ((k0 >> 3) & 3) * 8 + (k0 >> 5);
    u32 o[8];
#pragma unroll
    for (int i = 0; i < 8; ++i) {
      int k = k0 + 256 * i;
      o[i] = (u32)wr[k * DF_ + t] | ((u32)wi[k * DF_ + t] << 16);
    }
    u4v* dst = (u4v*)(Wt + ((size_t)t * 256 + tt) * 8);
    dst[0] = u4v{o[0], o[1], o[2], o[3]};
    dst[1] = u4v{o[4], o[5], o[6], o[7]};
    if (t < 8) {
      int k = k0 + 256 * t, km = (2048 - k) & 2047;
      auto cv = [](unsigned short x) { return __uint_as_float((u32)x << 16); };
      float a0r = cv(wr[k * DF_]), a0i = cv(wi[k * DF_]), b0r = cv(wr[km * DF_]), b0i = cv(wi[km * DF_]);
      float a1r = cv(wr[k * DF_ + 256]), a1i = cv(wi[k * DF_ + 256]), b1r = cv(wr[km * DF_ + 256]), b1i = cv(wi[km * DF_ + 256]);
      float h0r = 0.5f * (a0r + b0r), h0i = 0.5f * (a0i - b0i);
      float h1r = 0.5f * (a1r + b1r), h1i = 0.5f * (a1i - b1i);
      PQ[tt * 8 + t] = f4{0.5f * (h0r + h1r), 0.5f * (h0i + h1i), 0.5f * (h0r - h1r), 0.5f * (h0i - h1i)};
    }
  } else {
    for (int ch = (b - 256) * 256 + t; ch < 8 * 56 * 4 * 64; ch += (gridDim.x - 256) * 256) {
      int lane = ch & 63, i = (ch >> 6) & 3, kap = (ch >> 8) % 56, ct2 = (ch >> 8) / 56;
      int gp = lane >> 4, rm = lane & 15;
      int d = 64 * ct2 + 16 * (rm >> 2) + 4 * i + (rm & 3);
      u32 o[4];
#pragma unroll
      for (int j = 0; j < 4; ++j)
        o[j] = (u32)w2[f_of(kap, gp, 2 * j) * D_ + d] | ((u32)w2[f_of(kap, gp, 2 * j + 1) * D_ + d] << 16);
      ((u4v*)W2F)[ch] = u4v{o[0], o[1], o[2], o[3]};
    }
  }
}

__global__ __launch_bounds__(256) void k_r1(const u32* __restrict__ x, f2* __restrict__ T) {
  __shared__ f2 lds[8 * RP];
  const int b = blockIdx.x, s2 = 32 * (b & 7) + (b >> 3);
  const int t = threadIdx.x, s1 = t >> 5, j = t & 31;
  u32 xin[8];
  const u32* xr = x + (size_t)(256 * s1 + s2) * 256 + j;
#pragma unroll
  for (int m = 0; m < 8; ++m) xin[m] = xr[32 * m];
  f2 tw1[8], tw2[8];
#pragma unroll
  for (int q = 1; q < 8; ++q) { tw1[q] = ldtw(&g_twr1.v[j * 8 + q]); tw2[q] = ldtw(&g_twr2.v[(j & 3) * 8 + q]); }
  const f2 w512 = ldtw(&g_tw512.v[t]);
  float v[16], o[16];
#pragma unroll
  for (int m = 0; m < 8; ++m) { v[2 * m] = bf_lo(xin[m]); v[2 * m + 1] = bf_hi(xin[m]); }
  layernorm16(v, o);
  f2 z[8];
#pragma unroll
  for (int m = 0; m < 8; ++m) z[m] = f2{o[2 * m], o[2 * m + 1]};
  f2* L = lds + s1 * RP;
  fft8<false>(z);
#pragma unroll
  for (int q = 1; q < 8; ++q) z[q] = cmul(z[q], tw1[q]);
#pragma unroll
  for (int q = 0; q < 8; ++q) L[q * 36 + j] = z[q];
  wbar();
#pragma unroll
  for (int m = 0; m < 8; ++m) z[m] = L[(j >> 2) * 36 + (j & 3) + 4 * m];
  wbar();
  fft8<false>(z);
#pragma unroll
  for (int q = 1; q < 8; ++q) z[q] = cmul(z[q], tw2[q]);
#pragma unroll
  for (int q = 0; q < 8; ++q) L[(j & 3) * 68 + 8 * q + (j >> 2)] = z[q];
  wbar();
#pragma unroll
  for (int i = 0; i < 8; ++i) z[i] = L[(i & 3) * 68 + 32 * (i >> 2) + j];
  wbar();
  fft4<false>(z[0], z[1], z[2], z[3]);
  fft4<false>(z[4], z[5], z[6], z[7]);
#pragma unroll
  for (int i = 0; i < 8; ++i) L[j + 32 * (i >> 2) + 64 * (i & 3)] = z[i];
  __syncthreads();
  f2 X[8];
#pragma unroll
  for (int r = 0; r < 8; ++r) {
    f2 A = lds[r * RP + t], Bc = lds[r * RP + ((256 - t) & 255)];
    f2 E = f2{A.x + Bc.x, A.y - Bc.y};
    f2 Dm = f2{A.x - Bc.x, A.y + Bc.y};
    f2 O = f2{Dm.y, -Dm.x};
    f2 xx = E + cmul(O, w512);
    if (t == 0) xx.y = E.x - O.x;
    X[r] = xx;
  }
  fft8<false>(X);
  const cxf* twx = &g_twx.v[s2 * 8];
#pragma unroll
  for (int k = 1; k < 8; ++k) X[k] = cmul(X[k], ldtw(twx + k));
  f4* dst = (f4*)(T + (size_t)s2 * TP + t * 8);
#pragma unroll
  for (int k = 0; k < 4; ++k) dst[k] = f4{X[2 * k].x, X[2 * k].y, X[2 * k + 1].x, X[2 * k + 1].y};
}

constexpr int PK = 292, QS = 72;
DI int idx1(int k1, int q, int j) { return k1 * PK + q * QS + (j ^ ((k1 >> 2) << 2)); }
__global__ __launch_bounds__(256) void k_c(f2* __restrict__ T, const u32* __restrict__ Wt, const f4* __restrict__ PQ) {
  __shared__ f2 lds[8 * PK];
  const int b = blockIdx.x, c = 32 * (b & 7) + (b >> 3);
  const int t = threadIdx.x, kp = t & 3, j = t >> 2;
  f4 in[4];
  f4* Tp = (f4*)(T + (size_t)j * TP + c * 8 + 2 * kp);
#pragma unroll
  for (int m = 0; m < 4; ++m) in[m] = Tp[(size_t)m * 64 * TP / 2];
  const u4v* wp = (const u4v*)(Wt + ((size_t)c * 256 + t) * 8);
  u4v w0 = wp[0], w1 = wp[1];
  f2 tc1[4], tc2[8];
#pragma unroll
  for (int q = 1; q < 4; ++q) tc1[q] = ldtw(&g_twc1.v[j * 4 + q]);
#pragma unroll
  for (int q = 1; q < 8; ++q) tc2[q] = ldtw(&g_twc2.v[(t & 7) * 8 + q]);
#pragma unroll
  for (int e = 0; e < 2; ++e) {
    f2 a[4];
#pragma unroll
    for (int m = 0; m < 4; ++m) a[m] = e ? f2{in[m].z, in[m].w} : f2{in[m].x, in[m].y};
    fft4<false>(a[0], a[1], a[2], a[3]);
#pragma unroll
    for (int q = 1; q < 4; ++q) a[q] = cmul(a[q], tc1[q]);
#pragma unroll
    for (int q = 0; q < 4; ++q) lds[idx1(2 * kp + e, q, j)] = a[q];
  }
  __syncthreads();
  const int k1 = t >> 5, q = (t >> 3) & 3, jp = t & 7;
  f2 v[8];
#pragma unroll
  for (int m = 0; m < 8; ++m) v[m] = lds[idx1(k1, q, jp + 8 * m)];
  wbar();
  fft8<false>(v);
#pragma unroll
  for (int i = 1; i < 8; ++i) v[i] = cmul(v[i], tc2[i]);
  f2* L2 = lds + k1 * PK + q * QS;
#pragma unroll
  for (int i = 0; i < 8; ++i) L2[jp * 9 + i] = v[i];
  wbar();
#pragma unroll
  for (int i = 0; i < 8; ++i) v[i] = L2[i * 9 + jp];
  wbar();
  fft8<false>(v);
  const float sc = 1.0f / 2097152.0f;
  if (c != 0) {
    u32 w[8] = {w0.x, w0.y, w0.z, w0.w, w1.x, w1.y, w1.z, w1.w};
#pragma unroll
    for (int i = 0; i < 8; ++i) v[i] = cmul(v[i], f2{bf_lo(w[i]) * sc, bf_hi(w[i]) * sc});
  } else {
    __syncthreads();
    const int kb = k1 + 8 * (q + 4 * jp);
#pragma unroll
    for (int i = 0; i < 8; ++i) lds[kb + 256 * i] = v[i];
    __syncthreads();
#pragma unroll
    for (int i = 0; i < 8; ++i) {
      f2 zm = lds[(2048 - (kb + 256 * i)) & 2047];
      f4 pq = PQ[t * 8 + i];
      f2 r = cmul(v[i], f2{pq.x, pq.y}) + cmul(f2{zm.x, -zm.y}, f2{pq.z, pq.w});
      v[i] = r * sc;
    }
    __syncthreads();
  }
  fft8<true>(v);
#pragma unroll
  for (int i = 1; i < 8; ++i) v[i] = cmulc(v[i], tc2[i]);
#pragma unroll
  for (int i = 0; i < 8; ++i) L2[i * 9 + jp] = v[i];
  wbar();
#pragma unroll
  for (int i = 0; i < 8; ++i) v[i] = L2[jp * 9 + i];
  wbar();
  fft8<true>(v);
#pragma unroll
  for (int m = 0; m < 8; ++m) lds[idx1(k1, q, jp + 8 * m)] = v[m];
  __syncthreads();
  f2 o[2][4];
#pragma unroll
  for (int e = 0; e < 2; ++e) {
#pragma unroll
    for (int qq = 0; qq < 4; ++qq) o[e][qq] = lds[idx1(2 * kp + e, qq, j)];
#pragma unroll
    for (int qq = 1; qq < 4; ++qq) o[e][qq] = cmulc(o[e][qq], tc1[qq]);
    fft4<true>(o[e][0], o[e][1], o[e][2], o[e][3]);
  }
#pragma unroll
  for (int m = 0; m < 4; ++m) Tp[(size_t)m * 64 * TP / 2] = f4{o[0][m].x, o[0][m].y, o[1][m].x, o[1][m].y};
}

__global__ __launch_bounds__(256) void k_r2(const f2* __restrict__ T, const u32* xres, u32* xout, u4v* __restrict__ LNF) {
  __shared__ f2 lds[8 * RP];
  const int b = blockIdx.x, s2 = 32 * (b & 7) + (b >> 3);
  const int t = threadIdx.x, s1 = t >> 5, j = t & 31;
  f4 in[4];
  const f4* src = (const f4*)(T + (size_t)s2 * TP + t * 8);
#pragma unroll
  for (int k = 0; k < 4; ++k) in[k] = src[k];
  u32 xin[8];
  const size_t rowoff = (size_t)(256 * s1 + s2) * 256 + j;
#pragma unroll
  for (int m = 0; m < 8; ++m) xin[m] = xres[rowoff + 32 * m];
  f2 tw1[8], twu[8], tw2[2][4];
#pragma unroll
  for (int q = 1; q < 8; ++q) tw1[q] = ldtw(&g_twr1.v[j * 8 + q]);
#pragma unroll
  for (int i = 0; i < 8; ++i) twu[i] = ldtw(&g_tw512u.v[j * 8 + i]);
#pragma unroll
  for (int a = 0; a < 2; ++a)
#pragma unroll
    for (int i = 1; i < 4; ++i) tw2[a][i] = ldtw(&g_twr2.v[((j >> 3) + 4 * a) * 8 + i]);
  f2 X[8];
#pragma unroll
  for (int k = 0; k < 4; ++k) { X[2 * k] = f2{in[k].x, in[k].y}; X[2 * k + 1] = f2{in[k].z, in[k].w}; }
  const cxf* twx = &g_twx.v[s2 * 8];
#pragma unroll
  for (int k = 1; k < 8; ++k) X[k] = cmulc(X[k], ldtw(twx + k));
  fft8<true>(X);
#pragma unroll
  for (int r = 0; r < 8; ++r) lds[r * RP + t] = X[r];
  __syncthreads();
  f2* L = lds + s1 * RP;
  f2 z[8];
#pragma unroll
  for (int i = 0; i < 8; ++i) {
    int k = j + 32 * (i >> 2) + 64 * (i & 3);
    f2 A = L[k], Bc = L[(256 - k) & 255];
    f2 E = f2{A.x + Bc.x, A.y - Bc.y};
    f2 Dm = f2{A.x - Bc.x, A.y + Bc.y};
    f2 O = cmulc(Dm, twu[i]);
    f2 zz = f2{E.x - O.y, E.y + O.x};
    if (i == 0 && j == 0) zz = f2{A.x + A.y, A.x - A.y};
    z[i] = zz;
  }
  wbar();
  fft4<true>(z[0], z[1], z[2], z[3]);
  fft4<true>(z[4], z[5], z[6], z[7]);
#pragma unroll
  for (int a = 0; a < 2; ++a)
#pragma unroll
    for (int i = 1; i < 4; ++i) z[4 * a + i] = cmulc(z[4 * a + i], tw2[a][i]);
#pragma unroll
  for (int i = 0; i < 8; ++i) L[(i & 3) * 72 + 32 * (i >> 2) + j] = z[i];
  wbar();
#pragma unroll
  for (int q = 0; q < 8; ++q) z[q] = L[(j & 3) * 72 + 8 * q + (j >> 2)];
  wbar();
  fft8<true>(z);
#pragma unroll
  for (int m = 0; m < 8; ++m) L[(j >> 2) * 36 + (j & 3) + 4 * m] = z[m];
  wbar();
#pragma unroll
  for (int q = 0; q < 8; ++q) z[q] = L[q * 36 + j];
#pragma unroll
  for (int q = 1; q < 8; ++q) z[q] = cmulc(z[q], tw1[q]);
  fft8<true>(z);
  float v[16], o[16];
#pragma unroll
  for (int m = 0; m < 8; ++m) {
    v[2 * m] = rbf(bf_lo(xin[m]) + rbf(z[m].x));
    v[2 * m + 1] = rbf(bf_hi(xin[m]) + rbf(z[m].y));
    xout[rowoff + 32 * m] = pack2(v[2 * m], v[2 * m + 1]);
  }
  layernorm16(v, o);
  const int rho = 8 * s2 + s1, tt = rho >> 4, r = rho & 15;
  u4v* dst = LNF + ((size_t)(tt * 16 + (j >> 1)) * 64 + r * 4 + (j & 1) * 2);
  dst[0] = u4v{pack2(o[0], o[1]), pack2(o[2], o[3]), pack2(o[4], o[5]), pack2(o[6], o[7])};
  dst[1] = u4v{pack2(o[8], o[9]), pack2(o[10], o[11]), pack2(o[12], o[13]), pack2(o[14], o[15])};
}

DI float gelu_f(float x) {
  float u = x * x * 0.044715f + 1.0f;
  float e = __builtin_amdgcn_exp2f(x * u * (-2.0f * 0.7978845608028654f * 1.4426950408889634f));
  return x * __builtin_amdgcn_rcpf(1.0f + e);
}
DI float act1(float acc, float bias) { return gelu_f(rbf(rbf(acc) + bias)); }

__global__ __launch_bounds__(256) void k_g1(const u4v* __restrict__ LNF, const unsigned short* __restrict__ w1,
                                            const unsigned short* __restrict__ b1, u4v* __restrict__ H) {
  extern __shared__ __attribute__((aligned(16))) char smem[];
  const int b = blockIdx.x, xcd = b & 7, ii = b >> 3;
  const int rt = 2 * xcd + (ii & 1), ct = ii >> 1;
  const int t = threadIdx.x, w = t >> 6, lane = t & 63, g = lane >> 4, c = lane & 15;
#pragma unroll 4
  for (int i = 0; i < 28; ++i) {
    int n = 4 * i + w;
    int L = n * 64 + lane;
    int pos = L / 14, ch = L - pos * 14;
    int dp = (pos & ~31) + 8 * (2 * ((pos >> 4) & 1) + ((pos >> 2) & 1)) + 4 * ((pos >> 3) & 1) + (pos & 3);
    int d = 64 * ((dp >> 1) & 7) + 2 * (dp >> 4) + (dp & 1);
    const unsigned short* src = w1 + (size_t)d * F_ + ct * 112 + ch * 8;
    __builtin_amdgcn_global_load_lds((const void*)src, (LAS void*)(smem + n * 1024), 16, 0, 0);
  }
  u4v av[2][16];
  const int tt0 = 8 * rt + 2 * w;
#pragma unroll
  for (int ks = 0; ks < 16; ++ks)
#pragma unroll
    for (int h = 0; h < 2; ++h) av[h][ks] = LNF[(size_t)((tt0 + h) * 16 + ks) * 64 + c * 4 + g];
  float bias[7][4];
#pragma unroll
  for (int jt = 0; jt < 7; ++jt) {
    u2v bb = *(const u2v*)(b1 + ct * 112 + 16 * jt + 4 * g);
    bias[jt][0] = bf_lo(bb.x); bias[jt][1] = bf_hi(bb.x); bias[jt][2] = bf_lo(bb.y); bias[jt][3] = bf_hi(bb.y);
  }
  asm volatile("s_waitcnt vmcnt(0)" ::: "memory");
  __syncthreads();
  f4 acc[7][2];
#pragma unroll
  for (int jt = 0; jt < 7; ++jt) { acc[jt][0] = f4{0, 0, 0, 0}; acc[jt][1] = f4{0, 0, 0, 0}; }
  const int lbase = (16 * (g >> 1) + 4 * (g & 1) + ((lane >> 2) & 3)) * 224 + 8 * (lane & 3);
#pragma unroll
  for (int ks = 0; ks < 16; ++ks) {
#pragma unroll
    for (int jt = 0; jt < 7; ++jt) {
      s4v lo = __builtin_amdgcn_ds_read_tr16_b64_v4i16((LAS s4v*)(smem + lbase + ks * 7168 + jt * 32));
      s4v hi = __builtin_amdgcn_ds_read_tr16_b64_v4i16((LAS s4v*)(smem + lbase + ks * 7168 + 1792 + jt * 32));
      typedef short s8v __attribute__((ext_vector_type(8)));
      s8v wf = __builtin_shufflevector(lo, hi, 0, 1, 2, 3, 4, 5, 6, 7);
      bf8v wfb = __builtin_bit_cast(bf8v, wf);
      acc[jt][0] = __builtin_amdgcn_mfma_f32_16x16x32_bf16(wfb, __builtin_bit_cast(bf8v, av[0][ks]), acc[jt][0], 0, 0, 0);
      acc[jt][1] = __builtin_amdgcn_mfma_f32_16x16x32_bf16(wfb, __builtin_bit_cast(bf8v, av[1][ks]), acc[jt][1], 0, 0, 0);
    }
  }
  const int kb = 7 * (ct >> 1) + 3 * (ct & 1);
#pragma unroll
  for (int h = 0; h < 2; ++h)
#pragma unroll
    for (int p = 0; p < 3; ++p) {
      f4 a0 = acc[2 * p][h], a1 = acc[2 * p + 1][h];
      u4v o;
      o.x = pack2(act1(a0[0], bias[2 * p][0]), act1(a0[1], bias[2 * p][1]));
      o.y = pack2(act1(a0[2], bias[2 * p][2]), act1(a0[3], bias[2 * p][3]));
      o.z = pack2(act1(a1[0], bias[2 * p + 1][0]), act1(a1[1], bias[2 * p + 1][1]));
      o.w = pack2(act1(a1[2], bias[2 * p + 1][2]), act1(a1[3], bias[2 * p + 1][3]));
      H[(size_t)((tt0 + h) * 56 + kb + p) * 64 + lane] = o;
    }
  {
    f4 a0 = acc[6][0], a1 = acc[6][1];
    u32 ax = pack2(act1(a0[0], bias[6][0]), act1(a0[1], bias[6][1]));
    u32 ay = pack2(act1(a0[2], bias[6][2]), act1(a0[3], bias[6][3]));
    u32 bx = pack2(act1(a1[0], bias[6][0]), act1(a1[1], bias[6][1]));
    u32 by = pack2(act1(a1[2], bias[6][2]), act1(a1[3], bias[6][3]));
    auto rx = __builtin_amdgcn_permlane16_swap(ax, bx, false, false);
    auto ry = __builtin_amdgcn_permlane16_swap(ay, by, false, false);
    u4v o = u4v{rx[0], ry[0], rx[1], ry[1]};
    int tt = tt0 + (g & 1), gp = 2 * (ct & 1) + (g >> 1);
    H[(size_t)(tt * 56 + 7 * (ct >> 1) + 6) * 64 + gp * 16 + c] = o;
  }
}

__global__ __launch_bounds__(256) void k_g2(const u4v* __restrict__ H, const u4v* __restrict__ W2F,
                                            const unsigned short* __restrict__ b2, const unsigned short* xin, unsigned short* xout) {
  __shared__ f4 red[4 * 4 * 4 * 64];
  const int b = blockIdx.x, xcd = b & 7, ii = b >> 3;
  const int rt2 = 4 * xcd + (ii & 3), ct2 = ii >> 2;
  const int t = threadIdx.x, w = t >> 6, lane = t & 63, g = lane >> 4, c = lane & 15;
  const int rho = 64 * rt2 + 16 * w + c;
  const int srow = 256 * (rho & 7) + (rho >> 3);
  const size_t xoff = (size_t)srow * D_ + 64 * ct2 + 16 * g;
  u4v x0 = *(const u4v*)(xin + xoff), x1 = *(const u4v*)(xin + xoff + 8);
  u4v bb0 = *(const u4v*)(b2 + 64 * ct2 + 16 * g), bb1 = *(const u4v*)(b2 + 64 * ct2 + 16 * g + 8);
  f4 acc[4][4];
#pragma unroll
  for (int i = 0; i < 4; ++i)
#pragma unroll
    for (int h = 0; h < 4; ++h) acc[i][h] = f4{0, 0, 0, 0};
  const u4v* Wp = W2F + (size_t)(ct2 * 56 + 14 * w) * 256 + lane;
  const u4v* Hp = H + (size_t)(4 * rt2 * 56 + 14 * w) * 64 + lane;
#pragma unroll
  for (int kk = 0; kk < 14; ++kk) {
    u4v wf[4], hf[4];
#pragma unroll
    for (int i = 0; i < 4; ++i) wf[i] = Wp[kk * 256 + i * 64];
#pragma unroll
    for (int h = 0; h < 4; ++h) hf[h] = Hp[(size_t)h * 56 * 64 + kk * 64];
#pragma unroll
    for (int i = 0; i < 4; ++i)
#pragma unroll
      for (int h = 0; h < 4; ++h)
        acc[i][h] = __builtin_amdgcn_mfma_f32_16x16x32_bf16(__builtin_bit_cast(bf8v, wf[i]), __builtin_bit_cast(bf8v, hf[h]), acc[i][h], 0, 0, 0);
  }
#pragma unroll
  for (int h = 0; h < 4; ++h)
#pragma unroll
    for (int i = 0; i < 4; ++i) red[((w * 4 + h) * 4 + i) * 64 + lane] = acc[i][h];
  __syncthreads();
  float s[16];
#pragma unroll
  for (int i = 0; i < 4; ++i) {
    f4 a = red[((0 * 4 + w) * 4 + i) * 64 + lane] + red[((1 * 4 + w) * 4 + i) * 64 + lane];
    f4 bq = red[((2 * 4 + w) * 4 + i) * 64 + lane] + red[((3 * 4 + w) * 4 + i) * 64 + lane];
    a = a + bq;
    s[4 * i] = a[0]; s[4 * i + 1] = a[1]; s[4 * i + 2] = a[2]; s[4 * i + 3] = a[3];
  }
  u32 xs[8] = {x0.x, x0.y, x0.z, x0.w, x1.x, x1.y, x1.z, x1.w};
  u32 bs[8] = {bb0.x, bb0.y, bb0.z, bb0.w, bb1.x, bb1.y, bb1.z, bb1.w};
  u32 o[8];
#pragma unroll
  for (int i = 0; i < 8; ++i) {
    float lo = bf_lo(xs[i]) + rbf(rbf(s[2 * i]) + bf_lo(bs[i]));
    float hi = bf_hi(xs[i]) + rbf(rbf(s[2 * i + 1]) + bf_hi(bs[i]));
    o[i] = pack2(lo, hi);
  }
  *(u4v*)(xout + xoff) = u4v{o[0], o[1], o[2], o[3]};
  *(u4v*)(xout + xoff + 8) = u4v{o[4], o[5], o[6], o[7]};
}

constexpr size_t OFF_T = 0;
constexpr size_t SZ_T = (size_t)256 * TP * 8;
constexpr size_t OFF_WT = OFF_T + SZ_T;
constexpr size_t SZ_WT = (size_t)256 * 256 * 8 * 4;
constexpr size_t OFF_PQ = OFF_WT + SZ_WT;
constexpr size_t SZ_PQ = (size_t)256 * 8 * 16;
constexpr size_t OFF_W2F = OFF_PQ + SZ_PQ;
constexpr size_t SZ_W2F = (size_t)F_ * D_ * 2;
constexpr size_t OFF_XR = OFF_W2F + SZ_W2F;
constexpr size_t SZ_XR = (size_t)S_ * D_ * 2;
constexpr size_t OFF_LNF = OFF_XR + SZ_XR;
constexpr size_t SZ_LNF = (size_t)S_ * D_ * 2;
constexpr size_t OFF_H = OFF_LNF + SZ_LNF;

extern "C" void kernel_launch(void* const* d_in, const int* in_sizes, int n_in, void* d_out, int out_size, void* d_ws,
                              size_t ws_size, hipStream_t stream) {
  const unsigned short* x = (const unsigned short*)d_in[0];
  const unsigned short* wr = (const unsigned short*)d_in[1];
  const unsigned short* wi = (const unsigned short*)d_in[2];
  const unsigned short* w1 = (const unsigned short*)d_in[4];
  const unsigned short* b1 = (const unsigned short*)d_in[5];
  const unsigned short* w2 = (const unsigned short*)d_in[6];
  const unsigned short* b2 = (const unsigned short*)d_in[7];
  char* ws = (char*)d_ws;
  f2* T = (f2*)(ws + OFF_T);
  u32* Wt = (u32*)(ws + OFF_WT);
  f4* PQ = (f4*)(ws + OFF_PQ);
  unsigned short* W2F = (unsigned short*)(ws + OFF_W2F);
  unsigned short* XR = (unsigned short*)(ws + OFF_XR);
  u4v* LNF = (u4v*)(ws + OFF_LNF);
  u4v* H = (u4v*)(ws + OFF_H);
  (void)hipFuncSetAttribute((const void*)k_g1, hipFuncAttributeMaxDynamicSharedMemorySize, 114688);
  k_prep<<<256 + 448, 256, 0, stream>>>(wr, wi, w2, Wt, PQ, W2F);
  for (int layer = 0; layer < 2; ++layer) {
    const unsigned short* xi = layer ? XR : x;
    k_r1<<<256, 256, 0, stream>>>((const u32*)xi, T);
    k_c<<<256, 256, 0, stream>>>(T, Wt, PQ);
    k_r2<<<256, 256, 0, stream>>>(T, (const u32*)xi, (u32*)XR, LNF);
    k_g1<<<256, 256, 114688, stream>>>(LNF, w1, b1, H);
    k_g2<<<256, 256, 0, stream>>>(H, (const u4v*)W2F, b2, XR, layer ? (unsigned short*)d_out : XR);
  }
}
```

```cpp
#include <hip/hip_runtime.h>
#include <hip/hip_bf16.h>
#include <stdint.h>

typedef unsigned int u32;
typedef float f2 __attribute__((ext_vector_type(2)));
typedef float f4 __attribute__((ext_vector_type(4)));
typedef u32 u2v __attribute__((ext_vector_type(2)));
typedef u32 u4v __attribute__((ext_vector_type(4)));
typedef __bf16 bf2v __attribute__((ext_vector_type(2)));
typedef __bf16 bf8v __attribute__((ext_vector_type(8)));
typedef short s4v __attribute__((ext_vector_type(4)));
typedef short s8v __attribute__((ext_vector_type(8)));
#define LAS __attribute__((address_space(3)))
#define DI __device__ __forceinline__
#define RLX_AGENT __ATOMIC_RELAXED, __HIP_MEMORY_SCOPE_AGENT

#define STAMP(K, I)

constexpr int S_ = 2048, D_ = 512, F_ = 1792, DF_ = 257;
constexpr int TP = 2048 + 32;

constexpr u32 OFF_CNT = 0;
constexpr u32 OFF_T = 65536;
constexpr u32 SZ_T = 256u * TP * 8;
constexpr u32 OFF_WT = OFF_T + SZ_T;
constexpr u32 SZ_WT = 256u * 256 * 8 * 4;
constexpr u32 OFF_PQ = OFF_WT + SZ_WT;
constexpr u32 SZ_PQ = 256u * 8 * 16;
constexpr u32 OFF_W2F = OFF_PQ + SZ_PQ;
constexpr u32 SZ_W2F = (u32)F_ * D_ * 2;
constexpr u32 OFF_XR = OFF_W2F + SZ_W2F;
constexpr u32 SZ_X = (u32)S_ * D_ * 2;
constexpr u32 OFF_X1P = OFF_XR + SZ_X;
constexpr u32 OFF_LNF = OFF_X1P + SZ_X;
constexpr u32 OFF_H = OFF_LNF + SZ_X;
constexpr u32 SZ_H = (u32)S_ * F_ * 2;
constexpr u32 WS_USED = OFF_H + SZ_H;

struct cxf { float x, y; };
constexpr double kPi = 3.14159265358979323846264338327950288;
constexpr double c_sin(double x) { double x2 = x * x, t = x, s = x; for (int i = 1; i < 11; ++i) { t *= -x2 / double((2 * i) * (2 * i + 1)); s += t; } return s; }
constexpr double c_cos(double x) { double x2 = x * x, t = 1, s = 1; for (int i = 1; i < 11; ++i) { t *= -x2 / double((2 * i - 1) * (2 * i)); s += t; } return s; }
constexpr cxf tw2048(int i) {
  i &= 2047;
  int qd = i >> 9, r = i & 511;
  double c = 0, s = 0;
  if (r <= 256) { double a = 2 * kPi * r / 2048.0; c = c_cos(a); s = c_sin(a); }
  else { double a = 2 * kPi * (512 - r) / 2048.0; c = c_sin(a); s = c_cos(a); }
  double cc = 0, ss = 0;
  if (qd == 0) { cc = c; ss = s; } else if (qd == 1) { cc = -s; ss = c; } else if (qd == 2) { cc = -c; ss = -s; } else { cc = s; ss = -c; }
  return cxf{(float)cc, (float)(-ss)};
}
template <int R, int C> struct Tab { cxf v[R * C]; };
constexpr Tab<32, 8> mk_r1() { Tab<32, 8> t{}; for (int j = 0; j < 32; ++j) for (int q = 0; q < 8; ++q) t.v[j * 8 + q] = tw2048(8 * j * q); return t; }
constexpr Tab<8, 8> mk_r2() { Tab<8, 8> t{}; for (int j = 0; j < 8; ++j) for (int q = 0; q < 8; ++q) t.v[j * 8 + q] = tw2048(64 * j * q); return t; }
constexpr Tab<256, 1> mk_512() { Tab<256, 1> t{}; for (int c = 0; c < 256; ++c) t.v[c] = tw2048(4 * c + 512); return t; }
constexpr Tab<32, 8> mk_512u() { Tab<32, 8> t{}; for (int u = 0; u < 32; ++u) for (int i = 0; i < 8; ++i) t.v[u * 8 + i] = tw2048(4 * (u + 32 * (i >> 2) + 64 * (i & 3)) + 512); return t; }
constexpr Tab<256, 8> mk_x() { Tab<256, 8> t{}; for (int s = 0; s < 256; ++s) for (int k = 0; k < 8; ++k) t.v[s * 8 + k] = tw2048(s * k); return t; }
constexpr Tab<64, 4> mk_c1() { Tab<64, 4> t{}; for (int j = 0; j < 64; ++j) for (int q = 0; q < 4; ++q) t.v[j * 4 + q] = tw2048(8 * j * q); return t; }
constexpr Tab<8, 8> mk_c2() { Tab<8, 8> t{}; for (int j = 0; j < 8; ++j) for (int q = 0; q < 8; ++q) t.v[j * 8 + q] = tw2048(32 * j * q); return t; }

__device__ const __attribute__((aligned(64))) Tab<32, 8> g_twr1 = mk_r1();
__device__ const __attribute__((aligned(64))) Tab<8, 8> g_twr2 = mk_r2();
__device__ const __attribute__((aligned(64))) Tab<256, 1> g_tw512 = mk_512();
__device__ const __attribute__((aligned(64))) Tab<32, 8> g_tw512u = mk_512u();
__device__ const __attribute__((aligned(64))) Tab<256, 8> g_twx = mk_x();
__device__ const __attribute__((aligned(64))) Tab<64, 4> g_twc1 = mk_c1();
__device__ const __attribute__((aligned(64))) Tab<8, 8> g_twc2 = mk_c2();

DI float bf_lo(u32 v) { return __uint_as_float(v << 16); }
DI float bf_hi(u32 v) { return __uint_as_float(v & 0xffff0000u); }
DI f2 unpk(u32 v) { return f2{bf_lo(v), bf_hi(v)}; }
DI float rbf(float x) { return (float)(__bf16)x; }
DI u32 pack2(f2 a) { bf2v v = __builtin_convertvector(a, bf2v); return __builtin_bit_cast(u32, v); }
DI f2 rbf2(f2 a) { return unpk(pack2(a)); }
DI f2 swp(f2 a) { return __builtin_shufflevector(a, a, 1, 0); }
DI f2 fma2(f2 a, f2 b, f2 c) { return __builtin_elementwise_fma(a, b, c); }
DI f2 xx(f2 a) { return __builtin_shufflevector(a, a, 0, 0); }
DI f2 yy(f2 a) { return __builtin_shufflevector(a, a, 1, 1); }
DI f2 cmul(f2 a, f2 b) { return f2{a.x * b.x - a.y * b.y, a.x * b.y + a.y * b.x}; }
DI f2 cmulc(f2 a, f2 b) { return f2{a.x * b.x + a.y * b.y, a.y * b.x - a.x * b.y}; }
template <bool INV> DI f2 addmi(f2 a, f2 d) { return fma2(swp(d), INV ? f2{-1.f, 1.f} : f2{1.f, -1.f}, a); }
template <bool INV> DI f2 submi(f2 a, f2 d) { return fma2(swp(d), INV ? f2{1.f, -1.f} : f2{-1.f, 1.f}, a); }

template <bool INV> DI void fft4(f2& b0, f2& b1, f2& b2, f2& b3) {
  f2 c0 = b0 + b2, c2 = b0 - b2, c1 = b1 + b3, d = b1 - b3;
  b0 = c0 + c1; b2 = c0 - c1; b1 = addmi<INV>(c2, d); b3 = submi<INV>(c2, d);
}
template <bool INV> DI void fft8(f2* v) {
  const float h = 0.70710678118654752440f;
  f2 a0 = v[0] + v[4], a4 = v[0] - v[4];
  f2 a1 = v[1] + v[5], a5 = v[1] - v[5];
  f2 a2 = v[2] + v[6], a6 = v[2] - v[6];
  f2 a3 = v[3] + v[7], a7 = v[3] - v[7];
  f2 c0 = addmi<INV>(a4, a6), c2 = submi<INV>(a4, a6);
  f2 u = addmi<INV>(a5, a7), w = submi<INV>(a5, a7);
  f2 tu = addmi<INV>(u, u), tw = addmi<INV>(w, w);
  fft4<INV>(a0, a1, a2, a3);
  v[0] = a0; v[2] = a1; v[4] = a2; v[6] = a3;
  v[1] = fma2(tu, f2{h, h}, c0);
  v[5] = fma2(tu, f2{-h, -h}, c0);
  v[3] = fma2(swp(tw), INV ? f2{-h, h} : f2{h, -h}, c2);
  v[7] = fma2(swp(tw), INV ? f2{h, -h} : f2{-h, h}, c2);
}
template <int CTRL> DI float dpp_add(float x) {
  return x + __int_as_float(__builtin_amdgcn_update_dpp(0, __float_as_int(x), CTRL, 0xf, 0xf, false));
}
DI float red32(float x) {
  x = dpp_add<0xB1>(x); x = dpp_add<0x4E>(x); x = dpp_add<0x141>(x); x = dpp_add<0x140>(x);
  auto r = __builtin_amdgcn_permlane16_swap(__float_as_uint(x), __float_as_uint(x), false, false);
  return __uint_as_float(r[0]) + __uint_as_float(r[1]);
}
DI void wbar() { __builtin_amdgcn_wave_barrier(); }
DI void bar() {
  asm volatile("s_waitcnt lgkmcnt(0)" ::: "memory");
  __builtin_amdgcn_s_barrier();
  asm volatile("" ::: "memory");
}
DI void wait_vm0() { __builtin_amdgcn_s_waitcnt(0x0F70); asm volatile("" ::: "memory"); }
DI void layernorm8(const f2* v, u32* o) {
  f2 s2 = ((v[0] + v[1]) + (v[2] + v[3])) + ((v[4] + v[5]) + (v[6] + v[7]));
  float mean = red32(s2.x + s2.y) * (1.0f / 512.0f);
  f2 mm = f2{mean, mean};
  f2 q0 = f2{0, 0}, q1 = f2{0, 0};
#pragma unroll
  for (int i = 0; i < 8; i += 2) { f2 d0 = v[i] - mm, d1 = v[i + 1] - mm; q0 = fma2(d0, d0, q0); q1 = fma2(d1, d1, q1); }
  q0 = q0 + q1;
  float q = red32(q0.x + q0.y);
  float m = rbf(mean), vr = rbf(q * (1.0f / 512.0f));
  float sd = rbf(__builtin_amdgcn_sqrtf(rbf(vr + 1e-5f)));
  float ri = __builtin_amdgcn_rcpf(sd);
  f2 m2 = f2{m, m}, r2 = f2{ri, ri};
#pragma unroll
  for (int i = 0; i < 8; ++i) o[i] = pack2(rbf2(v[i] - m2) * r2);
}
DI f2 ldtw(const cxf* p) { return *(const f2*)p; }
typedef __amdgpu_buffer_rsrc_t rsrc_t;
DI rsrc_t mk_rsrc(void* p, u32 bytes) { return __builtin_amdgcn_make_buffer_rsrc(p, 0, bytes, 0x00020000); }
template <int AUX> DI u4v bld4(rsrc_t r, u32 off) { return __builtin_amdgcn_raw_buffer_load_b128(r, off, 0, AUX); }
template <int AUX> DI u32 bld1(rsrc_t r, u32 off) { return __builtin_amdgcn_raw_buffer_load_b32(r, off, 0, AUX); }
template <int AUX> DI void bst4(rsrc_t r, u32 off, u4v v) { __builtin_amdgcn_raw_buffer_store_b128(v, r, off, 0, AUX); }
DI u4v f4u(f2 a, f2 b) { return u4v{__float_as_uint(a.x), __float_as_uint(a.y), __float_as_uint(b.x), __float_as_uint(b.y)}; }

constexpr int RP = 288;

DI void signal_cnt(u32* c) { if (threadIdx.x == 0) __hip_atomic_fetch_add(c, 1u, RLX_AGENT); }
DI void wait_cnt(u32* c, u32 want) {
  if (threadIdx.x < 64) {
    for (int sp = 0; sp < (1 << 20); ++sp) {
      u32 v = __hip_atomic_load(c, RLX_AGENT);
      if (__builtin_amdgcn_readfirstlane(v) >= want) break;
      __builtin_amdgcn_s_sleep(1);
    }
  }
  bar();
}

struct R1Tw { f2 tw1[8], tw2[8], w512; };
DI void r1_load_tw(R1Tw& tw, int t) {
  const int j = t & 31;
#pragma unroll
  for (int q = 1; q < 8; ++q) { tw.tw1[q] = ldtw(&g_twr1.v[j * 8 + q]); tw.tw2[q] = ldtw(&g_twr2.v[(j & 3) * 8 + q]); }
  tw.w512 = ldtw(&g_tw512.v[t]);
}
DI void r1_body(const u32* xin, const R1Tw& tw, f2* lds, f2* T, int s2, int t) {
  const int s1 = t >> 5, j = t & 31;
  f2 z[8];
#pragma unroll
  for (int m = 0; m < 8; ++m) z[m] = unpk(xin[m]);
  u32 o[8];
  layernorm8(z, o);
#pragma unroll
  for (int m = 0; m < 8; ++m) z[m] = unpk(o[m]);
  f2* L = lds + s1 * RP;
  fft8<false>(z);
#pragma unroll
  for (int q = 1; q < 8; ++q) z[q] = cmul(z[q], tw.tw1[q]);
#pragma unroll
  for (int q = 0; q < 8; ++q) L[q * 36 + j] = z[q];
  wbar();
#pragma unroll
  for (int m = 0; m < 8; ++m) z[m] = L[(j >> 2) * 36 + (j & 3) + 4 * m];
  wbar();
  fft8<false>(z);
#pragma unroll
  for (int q = 1; q < 8; ++q) z[q] = cmul(z[q], tw.tw2[q]);
#pragma unroll
  for (int q = 0; q < 8; ++q) L[(j & 3) * 68 + 8 * q + (j >> 2)] = z[q];
  wbar();
#pragma unroll
  for (int i = 0; i < 8; ++i) z[i] = L[(i & 3) * 68 + 32 * (i >> 2) + j];
  wbar();
  fft4<false>(z[0], z[1], z[2], z[3]);
  fft4<false>(z[4], z[5], z[6], z[7]);
#pragma unroll
  for (int i = 0; i < 8; ++i) L[j + 32 * (i >> 2) + 64 * (i & 3)] = z[i];
  bar();
  f2 X[8];
  const f2 wr = f2{-tw.w512.y, tw.w512.x};
#pragma unroll
  for (int r = 0; r < 8; ++r) {
    f2 A = lds[r * RP + t], Bc = lds[r * RP + ((256 - t) & 255)];
    f2 E = fma2(Bc, f2{1.f, -1.f}, A);
    f2 Dm = fma2(Bc, f2{-1.f, 1.f}, A);
    f2 x2 = fma2(xx(Dm), tw.w512, fma2(yy(Dm), wr, E));
    if (t == 0) x2.y = E.x - Dm.y;
    X[r] = x2;
  }
  fft8<false>(X);
  const cxf* twx = &g_twx.v[s2 * 8];
#pragma unroll
  for (int k = 1; k < 8; ++k) X[k] = cmul(X[k], ldtw(twx + k));
  bar();
  u4v* L4 = (u4v*)lds + (t >> 6) * 256;
  const int l = t & 63;
#pragma unroll
  for (int k = 0; k < 4; ++k) L4[l * 4 + (k ^ ((l >> 1) & 3))] = f4u(X[2 * k], X[2 * k + 1]);
  wbar();
  u4v* dst = (u4v*)(T + (size_t)s2 * TP) + (t >> 6) * 256 + l;
#pragma unroll
  for (int k = 0; k < 4; ++k) {
    int u = l + 64 * k, c = u >> 2;
    dst[64 * k] = L4[c * 4 + ((u & 3) ^ ((c >> 1) & 3))];
  }
}

DI int f_of(int kap, int g, int j) {
  int u = kap / 7, r = kap % 7;
  if (r < 6) { int ct = 2 * u + (r >= 3), p = r % 3; return 112 * ct + 32 * p + (j < 4 ? 4 * g + j : 16 + 4 * g + (j - 4)); }
  return 112 * (2 * u + (g >> 1)) + 96 + 8 * (g & 1) + j;
}
__global__ __launch_bounds__(256) void k_first(const u32* __restrict__ x, const unsigned short* __restrict__ wr,
                                               const unsigned short* __restrict__ wi, const unsigned short* __restrict__ w2,
                                               char* __restrict__ ws) {
  __shared__ __attribute__((aligned(16))) char smem[33 * 1024];
  const int b = blockIdx.x, t = threadIdx.x;
  if (b < 256) {
    STAMP(0, 0);
    const int s2 = 32 * (b & 7) + (b >> 3);
    u32 xin[8];
    const u32* xr = x + (size_t)(256 * (t >> 5) + s2) * 256 + (t & 31);
#pragma unroll
    for (int m = 0; m < 8; ++m) xin[m] = xr[32 * m];
    R1Tw tw;
    r1_load_tw(tw, t);
    r1_body(xin, tw, (f2*)smem, (f2*)(ws + OFF_T), s2, t);
    STAMP(0, 1);
  } else if (b < 512) {
    u32* Wt = (u32*)(ws + OFF_WT);
    f4* PQ = (f4*)(ws + OFF_PQ);
    const int k0 = b - 256;
    const int tt = (k0 & 7) * 32 + ((k0 >> 3) & 3) * 8 + (k0 >> 5);
    u32 o[8];
#pragma unroll
    for (int i = 0; i < 8; ++i) {
      int k = k0 + 256 * i;
      o[i] = (u32)wr[k * DF_ + t] | ((u32)wi[k * DF_ + t] << 16);
    }
    u4v* dst = (u4v*)(Wt + ((size_t)t * 256 + tt) * 8);
    dst[0] = u4v{o[0], o[1], o[2], o[3]};
    dst[1] = u4v{o[4], o[5], o[6], o[7]};
    if (t < 8) {
      int k = k0 + 256 * t, km = (2048 - k) & 2047;
      auto cv = [](unsigned short v) { return __uint_as_float((u32)v << 16); };
      float a0r = cv(wr[k * DF_]), a0i = cv(wi[k * DF_]), b0r = cv(wr[km * DF_]), b0i = cv(wi[km * DF_]);
      float a1r = cv(wr[k * DF_ + 256]), a1i = cv(wi[k * DF_ + 256]), b1r = cv(wr[km * DF_ + 256]), b1i = cv(wi[km * DF_ + 256]);
      float h0r = 0.5f * (a0r + b0r), h0i = 0.5f * (a0i - b0i);
      float h1r = 0.5f * (a1r + b1r), h1i = 0.5f * (a1i - b1i);
      PQ[tt * 8 + t] = f4{0.5f * (h0r + h1r), 0.5f * (h0i + h1i), 0.5f * (h0r - h1r), 0.5f * (h0i - h1i)};
    }
  } else {
    const int kap = b - 512, w = t >> 6, lane = t & 63;
#pragma unroll
    for (int i = 0; i < 8; ++i) {
      int row = w * 8 + i;
      u4v v = *(const u4v*)(w2 + (size_t)f_of(kap, row >> 3, row & 7) * D_ + lane * 8);
      *(u4v*)(smem + row * 1040 + lane * 16) = v;
    }
    __syncthreads();
    const int g = lane >> 4, q = (lane >> 2) & 3, p = lane & 3;
    u4v* W2F = (u4v*)(ws + OFF_W2F);
#pragma unroll
    for (int n = 0; n < 8; ++n) {
      int ct2 = 2 * w + (n >> 2), i = n & 3;
      const char* a = smem + (g * 8 + q) * 1040 + (64 * ct2 + 16 * p + 4 * i) * 2;
      s4v lo = __builtin_amdgcn_ds_read_tr16_b64_v4i16((LAS s4v*)a);
      s4v hi = __builtin_amdgcn_ds_read_tr16_b64_v4i16((LAS s4v*)(a + 4 * 1040));
      s8v wf = __builtin_shufflevector(lo, hi, 0, 1, 2, 3, 4, 5, 6, 7);
      W2F[(size_t)((ct2 * 56 + kap) * 4 + i) * 64 + lane] = __builtin_bit_cast(u4v, wf);
    }
  }
}

constexpr int PK = 292, QS = 72;
DI int idx1(int k1, int q, int j) { return k1 * PK + q * QS + (j ^ ((k1 >> 2) << 2)); }
__global__ __launch_bounds__(256) void k_c(char* __restrict__ ws, int kid) {
  __shared__ f2 lds[8 * PK];
  STAMP(kid, 0);
  f2* T = (f2*)(ws + OFF_T);
  const u32* Wt = (const u32*)(ws + OFF_WT);
  const f4* PQ = (const f4*)(ws + OFF_PQ);
  const int b = blockIdx.x, c = 32 * (b & 7) + (b >> 3);
  const int t = threadIdx.x, kp = t & 3, j = t >> 2;
  f4 in[4];
  f4* Tp = (f4*)(T + (size_t)j * TP + c * 8 + 2 * kp);
#pragma unroll
  for (int m = 0; m < 4; ++m) in[m] = Tp[(size_t)m * 64 * TP / 2];
  const u4v* wp = (const u4v*)(Wt + ((size_t)c * 256 + t) * 8);
  u4v w0 = wp[0], w1 = wp[1];
  f2 tc1[4], tc2[8];
#pragma unroll
  for (int q = 1; q < 4; ++q) tc1[q] = ldtw(&g_twc1.v[j * 4 + q]);
#pragma unroll
  for (int q = 1; q < 8; ++q) tc2[q] = ldtw(&g_twc2.v[(t & 7) * 8 + q]);
  f4 pq[8];
  if (c == 0) {
#pragma unroll
    for (int i = 0; i < 8; ++i) pq[i] = PQ[t * 8 + i];
  }
  if (b == 1 && t < 64) ((u32*)(ws + OFF_CNT))[t * 64] = 0u;
#pragma unroll
  for (int e = 0; e < 2; ++e) {
    f2 a[4];
#pragma unroll
    for (int m = 0; m < 4; ++m) a[m] = e ? f2{in[m].z, in[m].w} : f2{in[m].x, in[m].y};
    fft4<false>(a[0], a[1], a[2], a[3]);
#pragma unroll
    for (int q = 1; q < 4; ++q) a[q] = cmul(a[q], tc1[q]);
#pragma unroll
    for (int q = 0; q < 4; ++q) lds[idx1(2 * kp + e, q, j)] = a[q];
  }
  bar();
  const int k1 = t >> 5, q = (t >> 3) & 3, jp = t & 7;
  f2 v[8];
#pragma unroll
  for (int m = 0; m < 8; ++m) v[m] = lds[idx1(k1, q, jp + 8 * m)];
  wbar();
  fft8<false>(v);
#pragma unroll
  for (int i = 1; i < 8; ++i) v[i] = cmul(v[i], tc2[i]);
  f2* L2 = lds + k1 * PK + q * QS;
#pragma unroll
  for (int i = 0; i < 8; ++i) L2[jp * 9 + i] = v[i];
  wbar();
#pragma unroll
  for (int i = 0; i < 8; ++i) v[i] = L2[i * 9 + jp];
  wbar();
  fft8<false>(v);
  const float sc = 1.0f / 2097152.0f;
  if (c != 0) {
    u32 w[8] = {w0.x, w0.y, w0.z, w0.w, w1.x, w1.y, w1.z, w1.w};
#pragma unroll
    for (int i = 0; i < 8; ++i) v[i] = cmul(v[i], unpk(w[i]) * f2{sc, sc});
  } else {
    bar();
    const int kb = k1 + 8 * (q + 4 * jp);
#pragma unroll
    for (int i = 0; i < 8; ++i) lds[kb + 256 * i + (i >> 1)] = v[i];
    bar();
#pragma unroll
    for (int i = 0; i < 8; ++i) {
      int km = (2048 - (kb + 256 * i)) & 2047;
      f2 zm = lds[km + (km >> 9)];
      f2 r = cmul(v[i], f2{pq[i].x, pq[i].y}) + cmul(f2{zm.x, -zm.y}, f2{pq[i].z, pq[i].w});
      v[i] = r * f2{sc, sc};
    }
    bar();
  }
  fft8<true>(v);
#pragma unroll
  for (int i = 1; i < 8; ++i) v[i] = cmulc(v[i], tc2[i]);
#pragma unroll
  for (int i = 0; i < 8; ++i) L2[i * 9 + jp] = v[i];
  wbar();
#pragma unroll
  for (int i = 0; i < 8; ++i) v[i] = L2[jp * 9 + i];
  wbar();
  fft8<true>(v);
#pragma unroll
  for (int m = 0; m < 8; ++m) lds[idx1(k1, q, jp + 8 * m)] = v[m];
  bar();
  f2 o[2][4];
#pragma unroll
  for (int e = 0; e < 2; ++e) {
#pragma unroll
    for (int qq = 0; qq < 4; ++qq) o[e][qq] = lds[idx1(2 * kp + e, qq, j)];
#pragma unroll
    for (int qq = 1; qq < 4; ++qq) o[e][qq] = cmulc(o[e][qq], tc1[qq]);
    fft4<true>(o[e][0], o[e][1], o[e][2], o[e][3]);
  }
#pragma unroll
  for (int m = 0; m < 4; ++m) Tp[(size_t)m * 64 * TP / 2] = f4{o[0][m].x, o[0][m].y, o[1][m].x, o[1][m].y};
  STAMP(kid, 1);
}

DI f2 gelu2(f2 x) {
  const float c0 = -2.0f * 0.7978845608028654f * 1.4426950408889634f, c1 = c0 * 0.044715f;
  f2 u = fma2(x * x, f2{c1, c1}, f2{c0, c0}) * x;
  f2 e = f2{__builtin_amdgcn_exp2f(u.x), __builtin_amdgcn_exp2f(u.y)} + f2{1.f, 1.f};
  return x * f2{__builtin_amdgcn_rcpf(e.x), __builtin_amdgcn_rcpf(e.y)};
}
DI u32 act2(float a0, float a1, u32 bias) { return pack2(gelu2(rbf2(rbf2(f2{a0, a1}) + unpk(bias)))); }

constexpr int LDS_W1 = 114688;
constexpr int LDS_M = LDS_W1 + 49152;

template <bool LAST>
__global__ __launch_bounds__(256, 1) void k_m(const u32* __restrict__ xres, const unsigned short* __restrict__ w1,
                                              const unsigned short* __restrict__ b1, const unsigned short* __restrict__ b2,
                                              char* ws, unsigned short* out, int kid) {
  extern __shared__ __attribute__((aligned(16))) char smem[];
  STAMP(kid, 0);
  const int b = blockIdx.x, xcd = b & 7, ii = b >> 3;
  const int t = threadIdx.x, w = __builtin_amdgcn_readfirstlane(t >> 6), lane = t & 63, g = lane >> 4, c = lane & 15;
  const int s2 = 32 * xcd + ii, rt = 2 * xcd + (ii >> 4), ct = ii & 15, rt2 = 4 * xcd + (ii >> 3), ct2 = ii & 7;
  const rsrc_t rs = mk_rsrc(ws, WS_USED);
  u32* cnt = (u32*)(ws + OFF_CNT);
  f2* lds = (f2*)(smem + LDS_W1);
  u4v w2f[14][4];
  {
    const int s1 = t >> 5, j = t & 31;
    f4 in[4];
    const f4* src = (const f4*)((const f2*)(ws + OFF_T) + (size_t)s2 * TP + t * 8);
#pragma unroll
    for (int k = 0; k < 4; ++k) in[k] = src[k];
    u32 xin[8];
    const size_t rowoff = (size_t)(256 * s1 + s2) * 256 + j;
#pragma unroll
    for (int m = 0; m < 8; ++m) xin[m] = xres[rowoff + 32 * m];
    f2 tw1[8], twu[8], tw2[2][4];
#pragma unroll
    for (int q = 1; q < 8; ++q) tw1[q] = ldtw(&g_twr1.v[j * 8 + q]);
#pragma unroll
    for (int i = 0; i < 8; ++i) twu[i] = ldtw(&g_tw512u.v[j * 8 + i]);
#pragma unroll
    for (int a = 0; a < 2; ++a)
#pragma unroll
      for (int i = 1; i < 4; ++i) tw2[a][i] = ldtw(&g_twr2.v[((j >> 3) + 4 * a) * 8 + i]);
    wait_vm0();
    STAMP(kid, 1);
#pragma unroll
    for (int i = 0; i < 28; ++i) {
      int n = 4 * i + w;
      int L = n * 64 + lane;
      int pos = L / 14, ch = L - pos * 14;
      int dp = (pos & ~31) + 8 * (2 * ((pos >> 4) & 1) + ((pos >> 2) & 1)) + 4 * ((pos >> 3) & 1) + (pos & 3);
      int d = 64 * ((dp >> 1) & 7) + 2 * (dp >> 4) + (dp & 1);
      const unsigned short* sp = w1 + (size_t)d * F_ + ct * 112 + ch * 8;
      __builtin_amdgcn_global_load_lds((const void*)sp, (LAS void*)(smem + n * 1024), 16, 0, 0);
    }
    f2 X[8];
#pragma unroll
    for (int k = 0; k < 4; ++k) { X[2 * k] = f2{in[k].x, in[k].y}; X[2 * k + 1] = f2{in[k].z, in[k].w}; }
    const cxf* twx = &g_twx.v[s2 * 8];
#pragma unroll
    for (int k = 1; k < 8; ++k) X[k] = cmulc(X[k], ldtw(twx + k));
    fft8<true>(X);
#pragma unroll
    for (int r = 0; r < 8; ++r) lds[r * RP + t] = X[r];
    bar();
    f2* L = lds + s1 * RP;
    f2 z[8];
#pragma unroll
    for (int i = 0; i < 8; ++i) {
      int k = j + 32 * (i >> 2) + 64 * (i & 3);
      f2 A = L[k], Bc = L[(256 - k) & 255];
      f2 E = fma2(Bc, f2{1.f, -1.f}, A);
      f2 Dm = fma2(Bc, f2{-1.f, 1.f}, A);
      f2 zz = E + cmulc(Dm, twu[i]);
      if (i == 0 && j == 0) zz = f2{A.x + A.y, A.x - A.y};
      z[i] = zz;
    }
    wbar();
    fft4<true>(z[0], z[1], z[2], z[3]);
    fft4<true>(z[4], z[5], z[6], z[7]);
#pragma unroll
    for (int a = 0; a < 2; ++a)
#pragma unroll
      for (int i = 1; i < 4; ++i) z[4 * a + i] = cmulc(z[4 * a + i], tw2[a][i]);
#pragma unroll
    for (int i = 0; i < 8; ++i) L[(i & 3) * 72 + 32 * (i >> 2) + j] = z[i];
    wbar();
#pragma unroll
    for (int q = 0; q < 8; ++q) z[q] = L[(j & 3) * 72 + 8 * q + (j >> 2)];
    wbar();
    fft8<true>(z);
#pragma unroll
    for (int m = 0; m < 8; ++m) L[(j >> 2) * 36 + (j & 3) + 4 * m] = z[m];
    wbar();
#pragma unroll
    for (int q = 0; q < 8; ++q) z[q] = L[q * 36 + j];
#pragma unroll
    for (int q = 1; q < 8; ++q) z[q] = cmulc(z[q], tw1[q]);
    fft8<true>(z);
    u32 x1[8], o[8];
#pragma unroll
    for (int m = 0; m < 8; ++m) { x1[m] = pack2(unpk(xin[m]) + rbf2(z[m])); z[m] = unpk(x1[m]); }
    const int rho = 8 * s2 + s1;
    bst4<16>(rs, OFF_X1P + rho * 1024 + j * 32, u4v{x1[0], x1[1], x1[2], x1[3]});
    bst4<16>(rs, OFF_X1P + rho * 1024 + j * 32 + 16, u4v{x1[4], x1[5], x1[6], x1[7]});
    layernorm8(z, o);
    const u32 lo = OFF_LNF + (((rho >> 4) * 16 + (j >> 1)) * 64 + (rho & 15) * 4 + (j & 1) * 2) * 16;
    bst4<16>(rs, lo, u4v{o[0], o[1], o[2], o[3]});
    bst4<16>(rs, lo + 16, u4v{o[4], o[5], o[6], o[7]});
  }
  STAMP(kid, 2);
  wait_vm0();
  bar();
  signal_cnt(cnt + rt * 64);
  STAMP(kid, 3);
  const u32 w2off = OFF_W2F + ((ct2 * 56 + 14 * w) * 256 + lane) * 16;
#pragma unroll
  for (int kk = 0; kk < 7; ++kk)
#pragma unroll
    for (int i = 0; i < 4; ++i) w2f[kk][i] = bld4<0>(rs, w2off + (kk * 256 + i * 64) * 16);
  wait_cnt(cnt + rt * 64, 16);
  STAMP(kid, 4);
  {
    u4v av[2][16];
    const int tt0 = 8 * rt + 2 * w;
#pragma unroll
    for (int ks = 0; ks < 16; ++ks)
#pragma unroll
      for (int h = 0; h < 2; ++h) av[h][ks] = bld4<16>(rs, OFF_LNF + (((tt0 + h) * 16 + ks) * 64 + c * 4 + g) * 16);
    u2v bias[7];
#pragma unroll
    for (int jt = 0; jt < 7; ++jt) bias[jt] = *(const u2v*)(b1 + ct * 112 + 16 * jt + 4 * g);
#pragma unroll
    for (int kk = 7; kk < 14; ++kk)
#pragma unroll
      for (int i = 0; i < 4; ++i) w2f[kk][i] = bld4<0>(rs, w2off + (kk * 256 + i * 64) * 16);
    f4 acc[7][2];
#pragma unroll
    for (int jt = 0; jt < 7; ++jt) { acc[jt][0] = f4{0, 0, 0, 0}; acc[jt][1] = f4{0, 0, 0, 0}; }
    const char* lb = smem + (16 * (g >> 1) + 4 * (g & 1) + ((lane >> 2) & 3)) * 224 + 8 * (lane & 3);
    s4v wl[2][7], wh[2][7];
#pragma unroll
    for (int jt = 0; jt < 7; ++jt) {
      wl[0][jt] = __builtin_amdgcn_ds_read_tr16_b64_v4i16((LAS s4v*)(lb + jt * 32));
      wh[0][jt] = __builtin_amdgcn_ds_read_tr16_b64_v4i16((LAS s4v*)(lb + 1792 + jt * 32));
    }
#pragma unroll
    for (int ks = 0; ks < 16; ++ks) {
      const int cu = ks & 1, nx = cu ^ 1;
      if (ks < 15) {
#pragma unroll
        for (int jt = 0; jt < 7; ++jt) {
          wl[nx][jt] = __builtin_amdgcn_ds_read_tr16_b64_v4i16((LAS s4v*)(lb + (ks + 1) * 7168 + jt * 32));
          wh[nx][jt] = __builtin_amdgcn_ds_read_tr16_b64_v4i16((LAS s4v*)(lb + (ks + 1) * 7168 + 1792 + jt * 32));
        }
      }
#pragma unroll
      for (int jt = 0; jt < 7; ++jt) {
        bf8v wfb = __builtin_bit_cast(bf8v, __builtin_shufflevector(wl[cu][jt], wh[cu][jt], 0, 1, 2, 3, 4, 5, 6, 7));
        acc[jt][0] = __builtin_amdgcn_mfma_f32_16x16x32_bf16(wfb, __builtin_bit_cast(bf8v, av[0][ks]), acc[jt][0], 0, 0, 0);
        acc[jt][1] = __builtin_amdgcn_mfma_f32_16x16x32_bf16(wfb, __builtin_bit_cast(bf8v, av[1][ks]), acc[jt][1], 0, 0, 0);
      }
#pragma unroll
      for (int i = 0; i < 14; ++i) {
        __builtin_amdgcn_sched_group_barrier(0x100, 1, 0);
        __builtin_amdgcn_sched_group_barrier(0x008, 1, 0);
      }
    }
    STAMP(kid, 5);
    const int kb = 7 * (ct >> 1) + 3 * (ct & 1);
#pragma unroll
    for (int h = 0; h < 2; ++h)
#pragma unroll
      for (int p = 0; p < 3; ++p) {
        f4 a0 = acc[2 * p][h], a1 = acc[2 * p + 1][h];
        u4v o;
        o.x = act2(a0[0], a0[1], bias[2 * p].x);
        o.y = act2(a0[2], a0[3], bias[2 * p].y);
        o.z = act2(a1[0], a1[1], bias[2 * p + 1].x);
        o.w = act2(a1[2], a1[3], bias[2 * p + 1].y);
        bst4<16>(rs, OFF_H + (((tt0 + h) * 56 + kb + p) * 64 + lane) * 16, o);
      }
    {
      f4 a0 = acc[6][0], a1 = acc[6][1];
      u32 ax = act2(a0[0], a0[1], bias[6].x), ay = act2(a0[2], a0[3], bias[6].y);
      u32 bx = act2(a1[0], a1[1], bias[6].x), by = act2(a1[2], a1[3], bias[6].y);
      auto rx = __builtin_amdgcn_permlane16_swap(ax, bx, false, false);
      auto ry = __builtin_amdgcn_permlane16_swap(ay, by, false, false);
      u4v o = u4v{rx[0], ry[0], rx[1], ry[1]};
      int tt = tt0 + (g & 1), gp = 2 * (ct & 1) + (g >> 1);
      bst4<16>(rs, OFF_H + ((tt * 56 + 7 * (ct >> 1) + 6) * 64 + gp * 16 + c) * 16, o);
    }
  }
  STAMP(kid, 6);
  wait_vm0();
  bar();
  signal_cnt(cnt + (16 + rt) * 64);
  STAMP(kid, 7);
  const int rho2 = 64 * rt2 + 16 * w + c;
  u32 x1v[8];
#pragma unroll
  for (int i = 0; i < 8; ++i) x1v[i] = bld1<16>(rs, OFF_X1P + rho2 * 1024 + (8 * g + i) * 32 + ct2 * 4);
  u4v bb0 = *(const u4v*)(b2 + 64 * ct2 + 16 * g), bb1 = *(const u4v*)(b2 + 64 * ct2 + 16 * g + 8);
  wait_cnt(cnt + (16 + rt) * 64, 16);
  STAMP(kid, 8);
  {
    f4 acc[4][4];
#pragma unroll
    for (int i = 0; i < 4; ++i)
#pragma unroll
      for (int h = 0; h < 4; ++h) acc[i][h] = f4{0, 0, 0, 0};
    u32 hoff[4];
#pragma unroll
    for (int h = 0; h < 4; ++h) hoff[h] = OFF_H + (((4 * rt2 + ((w + h) & 3)) * 56 + 14 * w) * 64 + lane) * 16;
    constexpr int PD = 8;
    u4v hf[14][4];
#pragma unroll
    for (int kk = 0; kk < PD; ++kk)
#pragma unroll
      for (int h = 0; h < 4; ++h) hf[kk][h] = bld4<16>(rs, hoff[h] + kk * 1024);
    __builtin_amdgcn_sched_barrier(0);
#pragma unroll
    for (int kk = 0; kk < 14; ++kk) {
#pragma unroll
      for (int i = 0; i < 4; ++i)
#pragma unroll
        for (int h = 0; h < 4; ++h)
          acc[i][h] = __builtin_amdgcn_mfma_f32_16x16x32_bf16(__builtin_bit_cast(bf8v, w2f[kk][i]), __builtin_bit_cast(bf8v, hf[kk][h]), acc[i][h], 0, 0, 0);
      if (kk + PD < 14) {
#pragma unroll
        for (int h = 0; h < 4; ++h) hf[kk + PD][h] = bld4<16>(rs, hoff[h] + (kk + PD) * 1024);
      }
      __builtin_amdgcn_sched_barrier(0);
    }
    STAMP(kid, 9);
    f4* red = (f4*)(smem + LDS_W1);
#pragma unroll
    for (int h = 1; h < 4; ++h)
#pragma unroll
      for (int i = 0; i < 4; ++i) red[((((w + h) & 3) * 3 + (h - 1)) * 4 + i) * 64 + lane] = acc[i][h];
    bar();
    f2 s[8];
#pragma unroll
    for (int i = 0; i < 4; ++i) {
      f4 a = (acc[i][0] + red[((w * 3 + 0) * 4 + i) * 64 + lane]) + (red[((w * 3 + 1) * 4 + i) * 64 + lane] + red[((w * 3 + 2) * 4 + i) * 64 + lane]);
      s[2 * i] = f2{a[0], a[1]}; s[2 * i + 1] = f2{a[2], a[3]};
    }
    u32 bs[8] = {bb0.x, bb0.y, bb0.z, bb0.w, bb1.x, bb1.y, bb1.z, bb1.w};
    u32 o[8];
#pragma unroll
    for (int i = 0; i < 8; ++i) o[i] = pack2(unpk(x1v[i]) + rbf2(rbf2(s[i]) + unpk(bs[i])));
    const int srow = 256 * (rho2 & 7) + (rho2 >> 3);
    const u32 xo = (srow * D_ + 64 * ct2 + 16 * g) * 2;
    if (LAST) {
      *(u4v*)((char*)out + xo) = u4v{o[0], o[1], o[2], o[3]};
      *(u4v*)((char*)out + xo + 16) = u4v{o[4], o[5], o[6], o[7]};
    } else {
      bst4<16>(rs, OFF_XR + xo, u4v{o[0], o[1], o[2], o[3]});
      bst4<16>(rs, OFF_XR + xo + 16, u4v{o[4], o[5], o[6], o[7]});
    }
  }
  STAMP(kid, 10);
  if (!LAST) {
    wait_vm0();
    bar();
    signal_cnt(cnt + (32 + rt2) * 64);
    R1Tw tw;
    r1_load_tw(tw, t);
    wait_cnt(cnt + (32 + rt2) * 64, 8);
    STAMP(kid, 11);
    u32 xin[8];
    const u32 ro = OFF_XR + ((256 * (t >> 5) + s2) * 256 + (t & 31)) * 4;
#pragma unroll
    for (int m = 0; m < 8; ++m) xin[m] = bld1<16>(rs, ro + 128 * m);
    r1_body(xin, tw, lds, (f2*)(ws + OFF_T), s2, t);
    STAMP(kid, 12);
  }
}

extern "C" void kernel_launch(void* const* d_in, const int* in_sizes, int n_in, void* d_out, int out_size, void* d_ws,
                              size_t ws_size, hipStream_t stream) {
  const unsigned short* x = (const unsigned short*)d_in[0];
  const unsigned short* wr = (const unsigned short*)d_in[1];
  const unsigned short* wi = (const unsigned short*)d_in[2];
  const unsigned short* w1 = (const unsigned short*)d_in[4];
  const unsigned short* b1 = (const unsigned short*)d_in[5];
  const unsigned short* w2 = (const unsigned short*)d_in[6];
  const unsigned short* b2 = (const unsigned short*)d_in[7];
  char* ws = (char*)d_ws;
  (void)hipFuncSetAttribute((const void*)k_m<false>, hipFuncAttributeMaxDynamicSharedMemorySize, LDS_M);
  (void)hipFuncSetAttribute((const void*)k_m<true>, hipFuncAttributeMaxDynamicSharedMemorySize, LDS_M);
  k_first<<<512 + 56, 256, 0, stream>>>((const u32*)x, wr, wi, w2, ws);
  k_c<<<256, 256, 0, stream>>>(ws, 1);
  k_m<false><<<256, 256, LDS_M, stream>>>((const u32*)x, w1, b1, b2, ws, (unsigned short*)d_out, 2);
  k_c<<<256, 256, 0, stream>>>(ws, 3);
  k_m<true><<<256, 256, LDS_M, stream>>>((const u32*)(ws + OFF_XR), w1, b1, b2, ws, (unsigned short*)d_out, 4);
}
```

```cpp
#include <hip/hip_runtime.h>
#include <hip/hip_bf16.h>
#include <stdint.h>

typedef unsigned int u32;
typedef float f2 __attribute__((ext_vector_type(2)));
typedef float f4 __attribute__((ext_vector_type(4)));
typedef float f16v __attribute__((ext_vector_type(16)));
typedef u32 u2v __attribute__((ext_vector_type(2)));
typedef u32 u4v __attribute__((ext_vector_type(4)));
typedef __bf16 bf2v __attribute__((ext_vector_type(2)));
typedef __bf16 bf8v __attribute__((ext_vector_type(8)));
typedef short s4v __attribute__((ext_vector_type(4)));
typedef short s8v __attribute__((ext_vector_type(8)));
#define LAS __attribute__((address_space(3)))
#define DI __device__ __forceinline__
#define RLX_AGENT __ATOMIC_RELAXED, __HIP_MEMORY_SCOPE_AGENT

#define STAMP(K, I)

constexpr int S_ = 2048, D_ = 512, F_ = 1792, DF_ = 257;
constexpr u32 TPB = 16384 + 256;

constexpr u32 OFF_CNT = 0;
constexpr u32 OFF_T = 65536;
constexpr u32 SZ_T = 256u * TPB;
constexpr u32 OFF_WT = OFF_T + SZ_T;
constexpr u32 SZ_WT = 256u * 256 * 8 * 4;
constexpr u32 OFF_PQ = OFF_WT + SZ_WT;
constexpr u32 SZ_PQ = 256u * 8 * 16;
constexpr u32 OFF_W2F = OFF_PQ + SZ_PQ;
constexpr u32 SZ_W2F = (u32)F_ * D_ * 2;
constexpr u32 OFF_W1F = OFF_W2F + SZ_W2F;
constexpr u32 OFF_XR = OFF_W1F + SZ_W2F;
constexpr u32 SZ_X = (u32)S_ * D_ * 2;
constexpr u32 OFF_X1P = OFF_XR + SZ_X;
constexpr u32 OFF_LNF = OFF_X1P + SZ_X;
constexpr u32 OFF_H = OFF_LNF + SZ_X;
constexpr u32 SZ_H = (u32)S_ * F_ * 2;
constexpr u32 WS_USED = OFF_H + SZ_H;

struct cxf { float x, y; };
constexpr double kPi = 3.14159265358979323846264338327950288;
constexpr double c_sin(double x) { double x2 = x * x, t = x, s = x; for (int i = 1; i < 11; ++i) { t *= -x2 / double((2 * i) * (2 * i + 1)); s += t; } return s; }
constexpr double c_cos(double x) { double x2 = x * x, t = 1, s = 1; for (int i = 1; i < 11; ++i) { t *= -x2 / double((2 * i - 1) * (2 * i)); s += t; } return s; }
constexpr cxf tw2048(int i) {
  i &= 2047;
  int qd = i >> 9, r = i & 511;
  double c = 0, s = 0;
  if (r <= 256) { double a = 2 * kPi * r / 2048.0; c = c_cos(a); s = c_sin(a); }
  else { double a = 2 * kPi * (512 - r) / 2048.0; c = c_sin(a); s = c_cos(a); }
  double cc = 0, ss = 0;
  if (qd == 0) { cc = c; ss = s; } else if (qd == 1) { cc = -s; ss = c; } else if (qd == 2) { cc = -c; ss = -s; } else { cc = s; ss = -c; }
  return cxf{(float)cc, (float)(-ss)};
}
template <int R, int C> struct Tab { cxf v[R * C]; };
constexpr Tab<32, 8> mk_r1() { Tab<32, 8> t{}; for (int j = 0; j < 32; ++j) for (int q = 0; q < 8; ++q) t.v[j * 8 + q] = tw2048(8 * j * q); return t; }
constexpr Tab<8, 8> mk_r2() { Tab<8, 8> t{}; for (int j = 0; j < 8; ++j) for (int q = 0; q < 8; ++q) t.v[j * 8 + q] = tw2048(64 * j * q); return t; }
constexpr Tab<256, 1> mk_512() { Tab<256, 1> t{}; for (int c = 0; c < 256; ++c) t.v[c] = tw2048(4 * c + 512); return t; }
constexpr Tab<32, 8> mk_512u() { Tab<32, 8> t{}; for (int u = 0; u < 32; ++u) for (int i = 0; i < 8; ++i) t.v[u * 8 + i] = tw2048(4 * (u + 32 * (i >> 2) + 64 * (i & 3)) + 512); return t; }
constexpr Tab<32, 22> mk_r2all() {
  Tab<32, 22> t{};
  for (int j = 0; j < 32; ++j) {
    for (int i = 0; i < 8; ++i) t.v[j * 22 + i] = tw2048(4 * (j + 32 * (i >> 2) + 64 * (i & 3)) + 512);
    for (int a = 0; a < 2; ++a) for (int i = 1; i < 4; ++i) t.v[j * 22 + 8 + 3 * a + (i - 1)] = tw2048(64 * ((j >> 3) + 4 * a) * i);
    for (int q = 1; q < 8; ++q) t.v[j * 22 + 13 + q] = tw2048(8 * j * q);
    t.v[j * 22 + 21] = cxf{0.f, 0.f};
  }
  return t;
}
constexpr Tab<256, 8> mk_x() { Tab<256, 8> t{}; for (int s = 0; s < 256; ++s) for (int k = 0; k < 8; ++k) t.v[s * 8 + k] = tw2048(s * k); return t; }
constexpr Tab<64, 4> mk_c1() { Tab<64, 4> t{}; for (int j = 0; j < 64; ++j) for (int q = 0; q < 4; ++q) t.v[j * 4 + q] = tw2048(8 * j * q); return t; }
constexpr double kSc = 6.9053396600248785e-04;
constexpr cxf tw2048s(int i) {
  i &= 2047;
  int qd = i >> 9, r = i & 511;
  double c = 0, s = 0;
  if (r <= 256) { double a = 2 * kPi * r / 2048.0; c = c_cos(a); s = c_sin(a); }
  else { double a = 2 * kPi * (512 - r) / 2048.0; c = c_sin(a); s = c_cos(a); }
  double cc = 0, ss = 0;
  if (qd == 0) { cc = c; ss = s; } else if (qd == 1) { cc = -s; ss = c; } else if (qd == 2) { cc = -c; ss = -s; } else { cc = s; ss = -c; }
  return cxf{(float)(cc * kSc), (float)(-ss * kSc)};
}
constexpr Tab<8, 8> mk_c2() { Tab<8, 8> t{}; for (int j = 0; j < 8; ++j) for (int q = 0; q < 8; ++q) t.v[j * 8 + q] = tw2048s(32 * j * q); return t; }

__device__ const __attribute__((aligned(64))) Tab<32, 8> g_twr1 = mk_r1();
__device__ const __attribute__((aligned(64))) Tab<8, 8> g_twr2 = mk_r2();
__device__ const __attribute__((aligned(64))) Tab<256, 1> g_tw512 = mk_512();
__device__ const __attribute__((aligned(64))) Tab<32, 22> g_r2all = mk_r2all();
__device__ const __attribute__((aligned(64))) Tab<256, 8> g_twx = mk_x();
__device__ const __attribute__((aligned(64))) Tab<64, 4> g_twc1 = mk_c1();
__device__ const __attribute__((aligned(64))) Tab<8, 8> g_twc2 = mk_c2();

DI float bf_lo(u32 v) { return __uint_as_float(v << 16); }
DI float bf_hi(u32 v) { return __uint_as_float(v & 0xffff0000u); }
DI f2 unpk(u32 v) { return f2{bf_lo(v), bf_hi(v)}; }
DI float rbf(float x) { return (float)(__bf16)x; }
DI u32 pack2(f2 a) { bf2v v = __builtin_convertvector(a, bf2v); return __builtin_bit_cast(u32, v); }
DI f2 rbf2(f2 a) { return unpk(pack2(a)); }
DI f2 swp(f2 a) { return __builtin_shufflevector(a, a, 1, 0); }
DI f2 fma2(f2 a, f2 b, f2 c) { return __builtin_elementwise_fma(a, b, c); }
DI f2 xx(f2 a) { return __builtin_shufflevector(a, a, 0, 0); }
DI f2 yy(f2 a) { return __builtin_shufflevector(a, a, 1, 1); }
DI f2 cmul(f2 a, f2 b) {
  f2 t, r;
  asm("v_pk_mul_f32 %0, %1, %2 op_sel:[1,1] op_sel_hi:[1,0]" : "=v"(t) : "v"(a), "v"(b));
  asm("v_pk_fma_f32 %0, %1, %2, %3 op_sel:[0,0,0] op_sel_hi:[0,1,1] neg_lo:[0,0,1]" : "=v"(r) : "v"(a), "v"(b), "v"(t));
  return r;
}
DI f2 cmulc(f2 a, f2 b) {
  f2 t, r;
  asm("v_pk_mul_f32 %0, %1, %2 op_sel:[1,1] op_sel_hi:[1,0]" : "=v"(t) : "v"(a), "v"(b));
  asm("v_pk_fma_f32 %0, %1, %2, %3 op_sel:[0,0,0] op_sel_hi:[0,1,1] neg_hi:[0,1,0]" : "=v"(r) : "v"(a), "v"(b), "v"(t));
  return r;
}
DI f2 cmul_s(f2 a, f2 b) {
  f2 t, r;
  asm("v_pk_mul_f32 %0, %1, %2 op_sel:[1,1] op_sel_hi:[1,0]" : "=v"(t) : "v"(a), "s"(b));
  asm("v_pk_fma_f32 %0, %1, %2, %3 op_sel:[0,0,0] op_sel_hi:[0,1,1] neg_lo:[0,0,1]" : "=v"(r) : "v"(a), "s"(b), "v"(t));
  return r;
}
DI f2 cmulc_s(f2 a, f2 b) {
  f2 t, r;
  asm("v_pk_mul_f32 %0, %1, %2 op_sel:[1,1] op_sel_hi:[1,0]" : "=v"(t) : "v"(a), "s"(b));
  asm("v_pk_fma_f32 %0, %1, %2, %3 op_sel:[0,0,0] op_sel_hi:[0,1,1] neg_hi:[0,1,0]" : "=v"(r) : "v"(a), "s"(b), "v"(t));
  return r;
}
DI f2 cmul_add(f2 a, f2 b, f2 e) {
  f2 t, r;
  asm("v_pk_fma_f32 %0, %1, %2, %3 op_sel:[1,1,0] op_sel_hi:[1,0,1] neg_lo:[0,1,0]" : "=v"(t) : "v"(a), "v"(b), "v"(e));
  asm("v_pk_fma_f32 %0, %1, %2, %3 op_sel:[0,0,0] op_sel_hi:[0,1,1]" : "=v"(r) : "v"(a), "v"(b), "v"(t));
  return r;
}
DI f2 cmulc_add(f2 a, f2 b, f2 e) {
  f2 t, r;
  asm("v_pk_fma_f32 %0, %1, %2, %3 op_sel:[1,1,0] op_sel_hi:[1,0,1]" : "=v"(t) : "v"(a), "v"(b), "v"(e));
  asm("v_pk_fma_f32 %0, %1, %2, %3 op_sel:[0,0,0] op_sel_hi:[0,1,1] neg_hi:[0,1,0]" : "=v"(r) : "v"(a), "v"(b), "v"(t));
  return r;
}
DI f2 ccmul_add(f2 a, f2 b, f2 e) {
  f2 t, r;
  asm("v_pk_fma_f32 %0, %1, %2, %3 op_sel:[1,1,0] op_sel_hi:[1,0,1] neg_hi:[0,1,0]" : "=v"(t) : "v"(a), "v"(b), "v"(e));
  asm("v_pk_fma_f32 %0, %1, %2, %3 op_sel:[0,0,0] op_sel_hi:[0,1,1]" : "=v"(r) : "v"(a), "v"(b), "v"(t));
  return r;
}
template <bool INV> DI f2 addmi(f2 a, f2 d) { return fma2(swp(d), INV ? f2{-1.f, 1.f} : f2{1.f, -1.f}, a); }
template <bool INV> DI f2 submi(f2 a, f2 d) { return fma2(swp(d), INV ? f2{1.f, -1.f} : f2{-1.f, 1.f}, a); }

template <bool INV> DI void fft4(f2& b0, f2& b1, f2& b2, f2& b3) {
  f2 c0 = b0 + b2, c2 = b0 - b2, c1 = b1 + b3, d = b1 - b3;
  b0 = c0 + c1; b2 = c0 - c1; b1 = addmi<INV>(c2, d); b3 = submi<INV>(c2, d);
}
template <bool INV> DI void fft8(f2* v) {
  const float h = 0.70710678118654752440f;
  f2 a0 = v[0] + v[4], a4 = v[0] - v[4];
  f2 a1 = v[1] + v[5], a5 = v[1] - v[5];
  f2 a2 = v[2] + v[6], a6 = v[2] - v[6];
  f2 a3 = v[3] + v[7], a7 = v[3] - v[7];
  f2 c0 = addmi<INV>(a4, a6), c2 = submi<INV>(a4, a6);
  f2 u = addmi<INV>(a5, a7), w = submi<INV>(a5, a7);
  f2 tu = addmi<INV>(u, u), tw = addmi<INV>(w, w);
  fft4<INV>(a0, a1, a2, a3);
  v[0] = a0; v[2] = a1; v[4] = a2; v[6] = a3;
  v[1] = fma2(tu, f2{h, h}, c0);
  v[5] = fma2(tu, f2{-h, -h}, c0);
  v[3] = fma2(swp(tw), INV ? f2{-h, h} : f2{h, -h}, c2);
  v[7] = fma2(swp(tw), INV ? f2{h, -h} : f2{-h, h}, c2);
}
template <int CTRL> DI float dpp_add(float x) {
  return x + __int_as_float(__builtin_amdgcn_update_dpp(0, __float_as_int(x), CTRL, 0xf, 0xf, false));
}
DI float red32(float x) {
  x = dpp_add<0xB1>(x); x = dpp_add<0x4E>(x); x = dpp_add<0x141>(x); x = dpp_add<0x140>(x);
  auto r = __builtin_amdgcn_permlane16_swap(__float_as_uint(x), __float_as_uint(x), false, false);
  return __uint_as_float(r[0]) + __uint_as_float(r[1]);
}
DI void wbar() { __builtin_amdgcn_wave_barrier(); }
DI void bar() {
  asm volatile("s_waitcnt lgkmcnt(0)" ::: "memory");
  __builtin_amdgcn_s_barrier();
  asm volatile("" ::: "memory");
}
DI void wait_vm0() { __builtin_amdgcn_s_waitcnt(0x0F70); asm volatile("" ::: "memory"); }
DI void layernorm8(const f2* v, u32* o) {
  f2 s2 = ((v[0] + v[1]) + (v[2] + v[3])) + ((v[4] + v[5]) + (v[6] + v[7]));
  float mean = red32(s2.x + s2.y) * (1.0f / 512.0f);
  f2 mm = f2{mean, mean};
  f2 q0 = f2{0, 0}, q1 = f2{0, 0};
#pragma unroll
  for (int i = 0; i < 8; i += 2) { f2 d0 = v[i] - mm, d1 = v[i + 1] - mm; q0 = fma2(d0, d0, q0); q1 = fma2(d1, d1, q1); }
  q0 = q0 + q1;
  float q = red32(q0.x + q0.y);
  float m = rbf(mean), vr = rbf(q * (1.0f / 512.0f));
  float sd = rbf(__builtin_amdgcn_sqrtf(rbf(vr + 1e-5f)));
  float ri = __builtin_amdgcn_rcpf(sd);
  f2 m2 = f2{m, m}, r2 = f2{ri, ri};
#pragma unroll
  for (int i = 0; i < 8; ++i) o[i] = pack2(rbf2(v[i] - m2) * r2);
}
DI f2 ldtw(const cxf* p) { return *(const f2*)p; }
typedef __amdgpu_buffer_rsrc_t rsrc_t;
DI rsrc_t mk_rsrc(void* p, u32 bytes) { return __builtin_amdgcn_make_buffer_rsrc(p, 0, bytes, 0x00020000); }
template <int AUX> DI u4v bld4(rsrc_t r, u32 off) { return __builtin_amdgcn_raw_buffer_load_b128(r, off, 0, AUX); }
template <int AUX> DI u32 bld1(rsrc_t r, u32 off) { return __builtin_amdgcn_raw_buffer_load_b32(r, off, 0, AUX); }
template <int AUX> DI void bst4(rsrc_t r, u32 off, u4v v) { __builtin_amdgcn_raw_buffer_store_b128(v, r, off, 0, AUX); }
DI u4v f4u(f2 a, f2 b) { return u4v{__float_as_uint(a.x), __float_as_uint(a.y), __float_as_uint(b.x), __float_as_uint(b.y)}; }

constexpr int RP = 288;

DI void signal_cnt(u32* c) { if (threadIdx.x == 0) __hip_atomic_fetch_add(c, 1u, RLX_AGENT); }
DI void wait_cnt(u32* c, u32 want) {
  if (threadIdx.x < 64) {
    for (int sp = 0; sp < (1 << 20); ++sp) {
      u32 v = __hip_atomic_load(c, RLX_AGENT);
      if (__builtin_amdgcn_readfirstlane(v) >= want) break;
      __builtin_amdgcn_s_sleep(1);
    }
  }
  bar();
}

struct R1Tw { f2 tw1[8], tw2[8], w512; };
DI void r1_load_tw(R1Tw& tw, int t) {
  const int j = t & 31;
#pragma unroll
  for (int q = 1; q < 8; ++q) { tw.tw1[q] = ldtw(&g_twr1.v[j * 8 + q]); tw.tw2[q] = ldtw(&g_twr2.v[(j & 3) * 8 + q]); }
  tw.w512 = ldtw(&g_tw512.v[t]);
}
DI void r1_body(const u32* xin, const R1Tw& tw, const f16v& tx, f2* lds, char* ws, int s2, int t) {
  const int s1 = t >> 5, j = t & 31;
  f2 z[8];
#pragma unroll
  for (int m = 0; m < 8; ++m) z[m] = unpk(xin[m]);
  u32 o[8];
  layernorm8(z, o);
#pragma unroll
  for (int m = 0; m < 8; ++m) z[m] = unpk(o[m]);
  f2* L = lds + s1 * RP;
  fft8<false>(z);
#pragma unroll
  for (int q = 1; q < 8; ++q) z[q] = cmul(z[q], tw.tw1[q]);
#pragma unroll
  for (int q = 0; q < 8; ++q) L[q * 36 + j] = z[q];
  wbar();
#pragma unroll
  for (int m = 0; m < 8; ++m) z[m] = L[(j >> 2) * 36 + (j & 3) + 4 * m];
  wbar();
  fft8<false>(z);
#pragma unroll
  for (int q = 1; q < 8; ++q) z[q] = cmul(z[q], tw.tw2[q]);
#pragma unroll
  for (int q = 0; q < 8; ++q) L[(j & 3) * 68 + 8 * q + (j >> 2)] = z[q];
  wbar();
#pragma unroll
  for (int i = 0; i < 8; ++i) z[i] = L[(i & 3) * 68 + 32 * (i >> 2) + j];
  wbar();
  fft4<false>(z[0], z[1], z[2], z[3]);
  fft4<false>(z[4], z[5], z[6], z[7]);
#pragma unroll
  for (int i = 0; i < 8; ++i) L[j + 32 * (i >> 2) + 64 * (i & 3)] = z[i];
  bar();
  f2 X[8];
#pragma unroll
  for (int r = 0; r < 8; ++r) {
    f2 A = lds[r * RP + t], Bc = lds[r * RP + ((256 - t) & 255)];
    f2 E = fma2(Bc, f2{1.f, -1.f}, A);
    f2 Dm = fma2(Bc, f2{-1.f, 1.f}, A);
    f2 x2 = cmul_add(Dm, tw.w512, E);
    if (t == 0) x2.y = E.x - Dm.y;
    X[r] = x2;
  }
  fft8<false>(X);
#pragma unroll
  for (int k = 1; k < 8; ++k) X[k] = cmul_s(X[k], f2{tx[2 * k], tx[2 * k + 1]});
  u4v* dst = (u4v*)(ws + OFF_T + (size_t)s2 * TPB) + t;
  dst[0] = f4u(X[0], X[4]);
  dst[256] = f4u(X[1], X[7]);
  dst[512] = f4u(X[2], X[6]);
  dst[768] = f4u(X[3], X[5]);
}

DI int f_of(int kap, int g, int j) {
  int u = kap / 7, r = kap % 7;
  if (r < 6) { int ct = 2 * u + (r >= 3), p = r % 3; return 112 * ct + 32 * p + (j < 4 ? 4 * g + j : 16 + 4 * g + (j - 4)); }
  return 112 * (2 * u + (g >> 1)) + 96 + 8 * (g & 1) + j;
}
constexpr int WPIT = 528;
__global__ __launch_bounds__(256) void k_first(const u32* __restrict__ x, const unsigned short* __restrict__ wr,
                                               const unsigned short* __restrict__ wi, const unsigned short* __restrict__ w1,
                                               const unsigned short* __restrict__ w2, char* __restrict__ ws) {
  __shared__ __attribute__((aligned(16))) char smem[8 * RP * 8 + 32 * WPIT];
  const int b = blockIdx.x, t = threadIdx.x, w = __builtin_amdgcn_readfirstlane(t >> 6), lane = t & 63;
  STAMP(0, 0);
  const int s2 = 32 * (b & 7) + (b >> 3);
  u32 xin[8];
  const u32* xr = x + (size_t)(256 * (t >> 5) + s2) * 256 + (t & 31);
#pragma unroll
  for (int m = 0; m < 8; ++m) xin[m] = xr[32 * m];
  R1Tw tw;
  r1_load_tw(tw, t);
  const f16v tx = *(const f16v*)&g_twx.v[s2 * 8];
  unsigned short fr[8], fi[8];
#pragma unroll
  for (int i = 0; i < 8; ++i) { fr[i] = wr[(b + 256 * i) * DF_ + t]; fi[i] = wi[(b + 256 * i) * DF_ + t]; }
  u4v wu[4];
  const int isw2 = b >= 112;
  const int ua = isw2 ? (b - 112) >> 1 : b / 7, ub = isw2 ? (b - 112) & 1 : b % 7;
  if (b < 224) {
#pragma unroll
    for (int g = 0; g < 4; ++g) {
      const int i = t >> 5;
      const unsigned short* p;
      if (isw2) p = w2 + (size_t)f_of(ua, g, i) * D_;
      else p = w1 + (size_t)(256 * (g & 1) + 64 * (i >> 1) + 4 * ua + 2 * (g >> 1) + (i & 1)) * F_;
      wu[g] = *(const u4v*)(p + 256 * ub + (t & 31) * 8);
    }
  } else {
#pragma unroll
    for (int g = 0; g < 4; ++g) asm volatile("" : "=v"(wu[g]));
  }
  __builtin_amdgcn_sched_barrier(0);
  r1_body(xin, tw, tx, (f2*)smem, ws, s2, t);
  STAMP(0, 1);
  {
    const int k1 = b & 7, kp = k1 <= 4 ? (k1 & 3) : 8 - k1, e = k1 >= 4;
    const int tl = e * 32 + ((b >> 3) & 3) * 8 + (b >> 5);
    u32 o[8];
#pragma unroll
    for (int i = 0; i < 8; ++i) o[i] = (u32)fr[i] | ((u32)fi[i] << 16);
    u4v* dst = (u4v*)(ws + OFF_WT) + ((size_t)(kp * 64 + (t >> 2)) * 256 + (t & 3) * 64 + tl) * 2;
    dst[0] = u4v{o[0], o[1], o[2], o[3]};
    dst[1] = u4v{o[4], o[5], o[6], o[7]};
    if (t < 8) {
      int k = b + 256 * t, km = (2048 - k) & 2047;
      auto cv = [](unsigned short v) { return __uint_as_float((u32)v << 16); };
      float a0r = cv(wr[k * DF_]), a0i = cv(wi[k * DF_]), b0r = cv(wr[km * DF_]), b0i = cv(wi[km * DF_]);
      float a1r = cv(wr[k * DF_ + 256]), a1i = cv(wi[k * DF_ + 256]), b1r = cv(wr[km * DF_ + 256]), b1i = cv(wi[km * DF_ + 256]);
      float h0r = 0.5f * (a0r + b0r), h0i = 0.5f * (a0i - b0i);
      float h1r = 0.5f * (a1r + b1r), h1i = 0.5f * (a1i - b1i);
      ((f4*)(ws + OFF_PQ))[(kp * 64 + tl) * 8 + t] = f4{0.5f * (h0r + h1r), 0.5f * (h0i + h1i), 0.5f * (h0r - h1r), 0.5f * (h0i - h1i)};
    }
  }
  if (b < 224) {
    char* im = smem + 8 * RP * 8;
#pragma unroll
    for (int g = 0; g < 4; ++g) *(u4v*)(im + (8 * g + (t >> 5)) * WPIT + (t & 31) * 16) = wu[g];
    bar();
    const int g = lane >> 4, q = (lane >> 2) & 3, p = lane & 3;
    const char* a0 = im + (8 * g + q) * WPIT + (isw2 ? 32 * p : 8 * p) + 128 * w;
#pragma unroll
    for (int n = 0; n < 4; ++n) {
      const char* a = a0 + (isw2 ? 8 * n : 32 * n);
      s4v lo = __builtin_amdgcn_ds_read_tr16_b64_v4i16((LAS s4v*)a);
      s4v hi = __builtin_amdgcn_ds_read_tr16_b64_v4i16((LAS s4v*)(a + 4 * WPIT));
      s8v wf = __builtin_shufflevector(lo, hi, 0, 1, 2, 3, 4, 5, 6, 7);
      u32 off;
      if (isw2) off = OFF_W2F + (((4 * ub + w) * 56 + ua) * 4 + n) * 1024;
      else { int ft = 16 * ub + 4 * w + n; off = OFF_W1F + (((ft / 7) * 16 + ua) * 7 + ft % 7) * 1024; }
      *(u4v*)(ws + off + lane * 16) = __builtin_bit_cast(u4v, wf);
    }
  }
  STAMP(0, 2);
}

constexpr int PK = 292, QS = 72;
DI int idx1(int k1, int q, int j) { return k1 * PK + q * QS + (j ^ ((k1 >> 2) << 2)); }
template <int AUX> DI u4v bld4s(rsrc_t r, u32 off, u32 soff) { return __builtin_amdgcn_raw_buffer_load_b128(r, off, soff, AUX); }
template <int AUX> DI void bst4s(rsrc_t r, u32 off, u32 soff, u4v v) { __builtin_amdgcn_raw_buffer_store_b128(v, r, off + soff, 0, AUX); }
__global__ __launch_bounds__(256) void k_c(char* __restrict__ ws, int kid) {
  __shared__ f2 lds[8 * PK];
  STAMP(kid, 0);
  const rsrc_t rs = mk_rsrc(ws, WS_USED);
  const int b = blockIdx.x, qd = 8 * (b & 7) + ((b >> 3) & 7), kp = b >> 6;
  const int t = threadIdx.x, cc = t & 3, j = t >> 2;
  u4v in[4];
  const u32 tv = j * TPB + cc * 16, ts = OFF_T + kp * 4096 + qd * 64;
#pragma unroll
  for (int m = 0; m < 4; ++m) in[m] = bld4s<0>(rs, tv, ts + m * 64 * TPB);
  const u32 wsof = OFF_WT + (kp * 64 + qd) * 8192;
  u4v w0 = bld4s<0>(rs, t * 32, wsof), w1 = bld4s<0>(rs, t * 32 + 16, wsof);
  f2 tc1[4], tc2[8];
#pragma unroll
  for (int q = 1; q < 4; ++q) tc1[q] = ldtw(&g_twc1.v[j * 4 + q]);
#pragma unroll
  for (int q = 1; q < 8; ++q) tc2[q] = ldtw(&g_twc2.v[(t & 7) * 8 + q]);
  const bool special = qd == 0 && __builtin_amdgcn_readfirstlane(t >> 6) == 0;
  u4v pq[8];
  if (special) {
#pragma unroll
    for (int i = 0; i < 8; ++i) pq[i] = bld4s<0>(rs, t * 128 + i * 16, OFF_PQ + kp * 8192);
  } else {
#pragma unroll
    for (int i = 0; i < 8; ++i) asm volatile("" : "=v"(pq[i]));
  }
  if (b == 1 && t < 64) ((u32*)(ws + OFF_CNT))[t * 64] = 0u;
#pragma unroll
  for (int e = 0; e < 2; ++e) {
    f2 a[4];
#pragma unroll
    for (int m = 0; m < 4; ++m) a[m] = e ? f2{__uint_as_float(in[m].z), __uint_as_float(in[m].w)} : f2{__uint_as_float(in[m].x), __uint_as_float(in[m].y)};
    fft4<false>(a[0], a[1], a[2], a[3]);
#pragma unroll
    for (int q = 1; q < 4; ++q) a[q] = cmul(a[q], tc1[q]);
#pragma unroll
    for (int q = 0; q < 4; ++q) lds[idx1(2 * cc + e, q, j)] = a[q];
  }
  bar();
  const int k1 = t >> 5, q = (t >> 3) & 3, jp = t & 7;
  const float sc = (float)kSc;
  f2 v[8];
#pragma unroll
  for (int m = 0; m < 8; ++m) v[m] = lds[idx1(k1, q, jp + 8 * m)];
  wbar();
  fft8<false>(v);
  v[0] = v[0] * f2{sc, sc};
#pragma unroll
  for (int i = 1; i < 8; ++i) v[i] = cmul(v[i], tc2[i]);
  f2* L2 = lds + k1 * PK + q * QS;
#pragma unroll
  for (int i = 0; i < 8; ++i) L2[jp * 9 + i] = v[i];
  wbar();
#pragma unroll
  for (int i = 0; i < 8; ++i) v[i] = L2[i * 9 + jp];
  wbar();
  fft8<false>(v);
  if (!special) {
    u32 w[8] = {w0.x, w0.y, w0.z, w0.w, w1.x, w1.y, w1.z, w1.w};
#pragma unroll
    for (int i = 0; i < 8; ++i) v[i] = cmul(v[i], unpk(w[i]));
  } else {
#pragma unroll
    for (int i = 0; i < 8; ++i) lds[k1 * PK + i * 32 + (t & 31)] = v[i];
    wbar();
    const bool z0 = kp == 0 && k1 == 0;
    const int u = q + 4 * jp, up = z0 ? (32 - u) & 31 : 31 - u;
    const int zi = (z0 && u == 0) ? 8 : 7;
    const f2* src = lds + (kp == 0 ? k1 : k1 ^ 1) * PK + (up & 3) * 8 + (up >> 2);
#pragma unroll
    for (int i = 0; i < 8; ++i) {
      f2 zm = src[((zi - i) & 7) * 32];
      f2 P = f2{__uint_as_float(pq[i].x), __uint_as_float(pq[i].y)}, Q = f2{__uint_as_float(pq[i].z), __uint_as_float(pq[i].w)};
      v[i] = ccmul_add(zm, Q, cmul(v[i], P));
    }
    wbar();
  }
  fft8<true>(v);
  v[0] = v[0] * f2{sc, sc};
#pragma unroll
  for (int i = 1; i < 8; ++i) v[i] = cmulc(v[i], tc2[i]);
#pragma unroll
  for (int i = 0; i < 8; ++i) L2[i * 9 + jp] = v[i];
  wbar();
#pragma unroll
  for (int i = 0; i < 8; ++i) v[i] = L2[jp * 9 + i];
  wbar();
  fft8<true>(v);
#pragma unroll
  for (int m = 0; m < 8; ++m) lds[idx1(k1, q, jp + 8 * m)] = v[m];
  bar();
  f2 o[2][4];
#pragma unroll
  for (int e = 0; e < 2; ++e) {
#pragma unroll
    for (int qq = 0; qq < 4; ++qq) o[e][qq] = lds[idx1(2 * cc + e, qq, j)];
#pragma unroll
    for (int qq = 1; qq < 4; ++qq) o[e][qq] = cmulc(o[e][qq], tc1[qq]);
    fft4<true>(o[e][0], o[e][1], o[e][2], o[e][3]);
  }
#pragma unroll
  for (int m = 0; m < 4; ++m) bst4s<0>(rs, tv, ts + m * 64 * TPB, f4u(o[0][m], o[1][m]));
  STAMP(kid, 1);
}

#define SB() __builtin_amdgcn_sched_barrier(0)
template <int N> DI void actN(const f2* a, const f2* bias, u32* o) {
  const float c0 = -2.0f * 0.7978845608028654f * 1.4426950408889634f, c1 = c0 * 0.044715f;
  f2 x[N], u[N];
#pragma unroll
  for (int i = 0; i < N; ++i) x[i] = rbf2(a[i]);
  SB();
#pragma unroll
  for (int i = 0; i < N; ++i) x[i] = x[i] + bias[i];
  SB();
#pragma unroll
  for (int i = 0; i < N; ++i) x[i] = rbf2(x[i]);
  SB();
#pragma unroll
  for (int i = 0; i < N; ++i) u[i] = x[i] * x[i];
  SB();
#pragma unroll
  for (int i = 0; i < N; ++i) u[i] = fma2(u[i], f2{c1, c1}, f2{c0, c0});
  SB();
#pragma unroll
  for (int i = 0; i < N; ++i) u[i] = u[i] * x[i];
  SB();
#pragma unroll
  for (int i = 0; i < N; ++i) u[i] = f2{__builtin_amdgcn_exp2f(u[i].x), __builtin_amdgcn_exp2f(u[i].y)};
  SB();
#pragma unroll
  for (int i = 0; i < N; ++i) u[i] = u[i] + f2{1.f, 1.f};
  SB();
#pragma unroll
  for (int i = 0; i < N; ++i) u[i] = f2{__builtin_amdgcn_rcpf(u[i].x), __builtin_amdgcn_rcpf(u[i].y)};
  SB();
#pragma unroll
  for (int i = 0; i < N; ++i) u[i] = u[i] * x[i];
  SB();
#pragma unroll
  for (int i = 0; i < N; ++i) o[i] = pack2(u[i]);
  SB();
}

#define ALD4(dst, voff, sbase, imm) asm volatile("global_load_dwordx4 %0, %1, %2 offset:" #imm : "=v"(dst) : "v"(voff), "s"(sbase))
#define ALD1(dst, voff, sbase, imm) asm volatile("global_load_dword %0, %1, %2 offset:" #imm : "=v"(dst) : "v"(voff), "s"(sbase))
constexpr int LDS_W1 = 114688;
constexpr int LDS_M = LDS_W1 + 49152;
constexpr int W2E = 8;

__global__ __launch_bounds__(256, 1) void k_m(const u32* __restrict__ xres, const unsigned short* __restrict__ b1,
                                              const unsigned short* __restrict__ b2, char* ws, unsigned short* out, int last, int kid) {
  extern __shared__ __attribute__((aligned(16))) char smem[];
  STAMP(kid, 0);
  const int b = blockIdx.x, xcd = b & 7, ii = b >> 3;
  const int t = threadIdx.x, w = __builtin_amdgcn_readfirstlane(t >> 6), lane = t & 63, g = lane >> 4, c = lane & 15;
  const int s2 = 32 * xcd + ii, rt = 2 * xcd + (ii >> 4), ct = ii & 15, rt2 = 4 * xcd + (ii >> 3), ct2 = ii & 7;
  const rsrc_t rs = mk_rsrc(ws, WS_USED);
  u32* cnt = (u32*)(ws + OFF_CNT);
  f2* lds = (f2*)(smem + LDS_W1);
  u4v w2f[14][4];
  f16v tx;
  const u32 l16 = lane * 16;
  const u32 w2s = OFF_W2F + (ct2 * 56 + 14 * w) * 4096;
#define W2LD(kk) do { _Pragma("unroll") for (int i_ = 0; i_ < 4; ++i_) w2f[kk][i_] = bld4s<0>(rs, l16 + i_ * 1024, w2s + (kk) * 4096); } while (0)
  {
    const int s1 = t >> 5, j = t & 31;
    u4v in[4], tq[11];
    u32 xin[8];
    {
      const char* tb = ws + OFF_T + (size_t)s2 * TPB;
      const u32 t16 = t * 16;
      ALD4(in[0], t16, tb, 0);
      ALD4(in[1], t16, tb + 4096, 0);
      ALD4(in[2], t16, tb + 8192, 0);
      ALD4(in[3], t16, tb + 12288, 0);
      const u32 jo = j * 176;
      const void* tp = &g_r2all;
      ALD4(tq[0], jo, tp, 0); ALD4(tq[1], jo, tp, 16); ALD4(tq[2], jo, tp, 32); ALD4(tq[3], jo, tp, 48);
      ALD4(tq[4], jo, tp, 64); ALD4(tq[5], jo, tp, 80); ALD4(tq[6], jo, tp, 96); ALD4(tq[7], jo, tp, 112);
      ALD4(tq[8], jo, tp, 128); ALD4(tq[9], jo, tp, 144); ALD4(tq[10], jo, tp, 160);
      const u32 xo = s1 * (256 * 1024) + j * 4;
      const u32* xb = xres + s2 * 256;
      ALD1(xin[0], xo, xb, 0); ALD1(xin[1], xo, xb, 128); ALD1(xin[2], xo, xb, 256); ALD1(xin[3], xo, xb, 384);
      ALD1(xin[4], xo, xb, 512); ALD1(xin[5], xo, xb, 640); ALD1(xin[6], xo, xb, 768); ALD1(xin[7], xo, xb, 896);
    }
    SB();
    tx = *(const f16v*)&g_twx.v[s2 * 8];
    SB();
    {
      const char* gsrc = ws + OFF_W1F + (size_t)ct * LDS_W1 + w * 28672 + l16;
#pragma unroll
      for (int i = 0; i < 7; ++i) {
        __builtin_amdgcn_global_load_lds((const void*)(gsrc + i * 4096), (LAS void*)(smem + w * 28672 + i * 4096), 16, 0, 0);
        __builtin_amdgcn_global_load_lds((const void*)(gsrc + i * 4096), (LAS void*)(smem + w * 28672 + i * 4096), 16, 1024, 0);
        __builtin_amdgcn_global_load_lds((const void*)(gsrc + i * 4096), (LAS void*)(smem + w * 28672 + i * 4096), 16, 2048, 0);
        __builtin_amdgcn_global_load_lds((const void*)(gsrc + i * 4096), (LAS void*)(smem + w * 28672 + i * 4096), 16, 3072, 0);
      }
    }
    SB();
    asm volatile("s_waitcnt vmcnt(47)" : "+v"(in[0]), "+v"(in[1]), "+v"(in[2]), "+v"(in[3]));
    STAMP(kid, 1);
    f2 X[8];
    {
      auto lo = [](u4v v) { return f2{__uint_as_float(v.x), __uint_as_float(v.y)}; };
      auto hi = [](u4v v) { return f2{__uint_as_float(v.z), __uint_as_float(v.w)}; };
      X[0] = lo(in[0]); X[4] = hi(in[0]); X[1] = lo(in[1]); X[7] = hi(in[1]);
      X[2] = lo(in[2]); X[6] = hi(in[2]); X[3] = lo(in[3]); X[5] = hi(in[3]);
    }
#pragma unroll
    for (int k = 1; k < 8; ++k) X[k] = cmulc_s(X[k], f2{tx[2 * k], tx[2 * k + 1]});
    fft8<true>(X);
#pragma unroll
    for (int r = 0; r < 8; ++r) lds[r * RP + t] = X[r];
    SB();
    W2LD(0); W2LD(1);
    SB();
    asm volatile("s_waitcnt vmcnt(36)" : "+v"(tq[0]), "+v"(tq[1]), "+v"(tq[2]), "+v"(tq[3]), "+v"(tq[4]), "+v"(tq[5]), "+v"(tq[6]), "+v"(tq[7]),
                 "+v"(tq[8]), "+v"(tq[9]), "+v"(tq[10]), "+v"(xin[0]), "+v"(xin[1]), "+v"(xin[2]), "+v"(xin[3]), "+v"(xin[4]), "+v"(xin[5]),
                 "+v"(xin[6]), "+v"(xin[7]));
    f2 tw1[8], twu[8], tw2[2][4];
    {
      f2 tv[22];
#pragma unroll
      for (int i = 0; i < 11; ++i) {
        tv[2 * i] = f2{__uint_as_float(tq[i].x), __uint_as_float(tq[i].y)};
        tv[2 * i + 1] = f2{__uint_as_float(tq[i].z), __uint_as_float(tq[i].w)};
      }
#pragma unroll
      for (int i = 0; i < 8; ++i) twu[i] = tv[i];
#pragma unroll
      for (int a = 0; a < 2; ++a)
#pragma unroll
        for (int i = 1; i < 4; ++i) tw2[a][i] = tv[8 + 3 * a + (i - 1)];
#pragma unroll
      for (int q = 1; q < 8; ++q) tw1[q] = tv[13 + q];
    }
    bar();
    f2* L = lds + s1 * RP;
    f2 z[8];
#pragma unroll
    for (int i = 0; i < 8; ++i) {
      int k = j + 32 * (i >> 2) + 64 * (i & 3);
      f2 A = L[k], Bc = L[(256 - k) & 255];
      f2 E = fma2(Bc, f2{1.f, -1.f}, A);
      f2 Dm = fma2(Bc, f2{-1.f, 1.f}, A);
      f2 zz = cmulc_add(Dm, twu[i], E);
      if (i == 0 && j == 0) zz = f2{A.x + A.y, A.x - A.y};
      z[i] = zz;
    }
    wbar();
    fft4<true>(z[0], z[1], z[2], z[3]);
    fft4<true>(z[4], z[5], z[6], z[7]);
#pragma unroll
    for (int a = 0; a < 2; ++a)
#pragma unroll
      for (int i = 1; i < 4; ++i) z[4 * a + i] = cmulc(z[4 * a + i], tw2[a][i]);
#pragma unroll
    for (int i = 0; i < 8; ++i) L[(i & 3) * 72 + 32 * (i >> 2) + j] = z[i];
    wbar();
#pragma unroll
    for (int q = 0; q < 8; ++q) z[q] = L[(j & 3) * 72 + 8 * q + (j >> 2)];
    SB();
    W2LD(2); W2LD(3);
    SB();
    fft8<true>(z);
#pragma unroll
    for (int m = 0; m < 8; ++m) L[(j >> 2) * 36 + (j & 3) + 4 * m] = z[m];
    wbar();
#pragma unroll
    for (int q = 0; q < 8; ++q) z[q] = L[q * 36 + j];
    SB();
    W2LD(4); W2LD(5);
    SB();
#pragma unroll
    for (int q = 1; q < 8; ++q) z[q] = cmulc(z[q], tw1[q]);
    fft8<true>(z);
    u32 x1[8], o[8];
#pragma unroll
    for (int m = 0; m < 8; ++m) { x1[m] = pack2(unpk(xin[m]) + rbf2(z[m])); z[m] = unpk(x1[m]); }
    const int rho = 8 * s2 + s1;
    SB();
    W2LD(6); W2LD(7);
    SB();
    layernorm8(z, o);
    const u32 lo = OFF_LNF + (((rho >> 4) * 16 + (j >> 1)) * 64 + (rho & 15) * 4 + (j & 1) * 2) * 16;
    bst4<16>(rs, lo, u4v{o[0], o[1], o[2], o[3]});
    bst4<16>(rs, lo + 16, u4v{o[4], o[5], o[6], o[7]});
    bst4<16>(rs, OFF_X1P + rho * 1024 + j * 32, u4v{x1[0], x1[1], x1[2], x1[3]});
    bst4<16>(rs, OFF_X1P + rho * 1024 + j * 32 + 16, u4v{x1[4], x1[5], x1[6], x1[7]});
  }
  STAMP(kid, 2);
  wait_vm0();
  bar();
  signal_cnt(cnt + rt * 64);
  STAMP(kid, 3);
  u2v bias[7];
#pragma unroll
  for (int jt = 0; jt < 7; ++jt) bias[jt] = *(const u2v*)(b1 + ct * 112 + 16 * jt + 4 * g);
  wait_cnt(cnt + rt * 64, 16);
  STAMP(kid, 4);
  {
    u4v av[2][16];
    const int tt0 = 8 * rt + 2 * w;
    const u32 aoff = (c * 4 + g) * 16;
#pragma unroll
    for (int ks = 0; ks < 16; ++ks)
#pragma unroll
      for (int h = 0; h < 2; ++h) av[h][ks] = bld4s<16>(rs, aoff + (ks & 3) * 1024, OFF_LNF + ((tt0 + h) * 16 + (ks & ~3)) * 1024);
    SB();
    f4 acc[7][2];
#pragma unroll
    for (int jt = 0; jt < 7; ++jt) { acc[jt][0] = f4{0, 0, 0, 0}; acc[jt][1] = f4{0, 0, 0, 0}; }
    const char* lb = smem + l16;
    u4v wf[2][7];
#pragma unroll
    for (int jt = 0; jt < 7; ++jt) wf[0][jt] = *(const u4v*)(lb + jt * 1024);
    SB();
#pragma unroll
    for (int ks = 0; ks < 16; ++ks) {
      const int cu = ks & 1, nx = cu ^ 1;
      if (ks < 15) {
#pragma unroll
        for (int jt = 0; jt < 7; ++jt) wf[nx][jt] = *(const u4v*)(lb + ((ks + 1) * 7 + jt) * 1024);
      }
#pragma unroll
      for (int jt = 0; jt < 7; ++jt) {
        bf8v wfb = __builtin_bit_cast(bf8v, wf[cu][jt]);
        acc[jt][0] = __builtin_amdgcn_mfma_f32_16x16x32_bf16(wfb, __builtin_bit_cast(bf8v, av[0][ks]), acc[jt][0], 0, 0, 0);
        acc[jt][1] = __builtin_amdgcn_mfma_f32_16x16x32_bf16(wfb, __builtin_bit_cast(bf8v, av[1][ks]), acc[jt][1], 0, 0, 0);
      }
      if (ks < 15) {
#pragma unroll
        for (int i = 0; i < 7; ++i) {
          __builtin_amdgcn_sched_group_barrier(0x008, 1, 0);
          __builtin_amdgcn_sched_group_barrier(0x100, 1, 0);
          __builtin_amdgcn_sched_group_barrier(0x008, 1, 0);
        }
      }
      SB();
    }
    STAMP(kid, 5);
#pragma unroll
    for (int kk = W2E; kk < 14; ++kk) W2LD(kk);
    SB();
    const int kb = 7 * (ct >> 1) + 3 * (ct & 1);
#pragma unroll
    for (int p = 0; p < 3; ++p) {
      f2 a[8], bs[8];
      u32 o[8];
#pragma unroll
      for (int h = 0; h < 2; ++h) {
        f4 a0 = acc[2 * p][h], a1 = acc[2 * p + 1][h];
        a[4 * h] = f2{a0[0], a0[1]}; a[4 * h + 1] = f2{a0[2], a0[3]}; a[4 * h + 2] = f2{a1[0], a1[1]}; a[4 * h + 3] = f2{a1[2], a1[3]};
        bs[4 * h] = unpk(bias[2 * p].x); bs[4 * h + 1] = unpk(bias[2 * p].y); bs[4 * h + 2] = unpk(bias[2 * p + 1].x); bs[4 * h + 3] = unpk(bias[2 * p + 1].y);
      }
      actN<8>(a, bs, o);
#pragma unroll
      for (int h = 0; h < 2; ++h)
        bst4s<16>(rs, l16 + p * 1024, OFF_H + ((tt0 + h) * 56 + kb) * 1024, u4v{o[4 * h], o[4 * h + 1], o[4 * h + 2], o[4 * h + 3]});
    }
    {
      f4 a0 = acc[6][0], a1 = acc[6][1];
      f2 a[4] = {f2{a0[0], a0[1]}, f2{a0[2], a0[3]}, f2{a1[0], a1[1]}, f2{a1[2], a1[3]}};
      f2 bs[4] = {unpk(bias[6].x), unpk(bias[6].y), unpk(bias[6].x), unpk(bias[6].y)};
      u32 o4[4];
      actN<4>(a, bs, o4);
      auto rx = __builtin_amdgcn_permlane16_swap(o4[0], o4[2], false, false);
      auto ry = __builtin_amdgcn_permlane16_swap(o4[1], o4[3], false, false);
      u4v o = u4v{rx[0], ry[0], rx[1], ry[1]};
      int tt = tt0 + (g & 1), gp = 2 * (ct & 1) + (g >> 1);
      bst4<16>(rs, OFF_H + ((tt * 56 + 7 * (ct >> 1) + 6) * 64 + gp * 16 + c) * 16, o);
    }
  }
  STAMP(kid, 6);
  wait_vm0();
  bar();
  signal_cnt(cnt + (16 + rt) * 64);
  STAMP(kid, 7);
  wait_cnt(cnt + (16 + rt) * 64, 16);
  STAMP(kid, 8);
  {
    f4 acc[4][4];
#pragma unroll
    for (int i = 0; i < 4; ++i)
#pragma unroll
      for (int h = 0; h < 4; ++h) acc[i][h] = f4{0, 0, 0, 0};
    u32 hoff[4];
#pragma unroll
    for (int h = 0; h < 4; ++h) hoff[h] = OFF_H + ((4 * rt2 + ((w + h) & 3)) * 56 + 14 * w) * 1024;
    constexpr int PD = 8;
    u4v hf[14][4];
#pragma unroll
    for (int kk = 0; kk < PD; ++kk)
#pragma unroll
      for (int h = 0; h < 4; ++h) hf[kk][h] = bld4s<16>(rs, l16 + (kk & 3) * 1024, hoff[h] + (kk & ~3) * 1024);
    const int rho2 = 64 * rt2 + 16 * w + c;
    u32 x1v[8];
#pragma unroll
    for (int i = 0; i < 8; ++i) x1v[i] = bld1<16>(rs, OFF_X1P + rho2 * 1024 + (8 * g + i) * 32 + ct2 * 4);
    u4v bb0 = *(const u4v*)(b2 + 64 * ct2 + 16 * g), bb1 = *(const u4v*)(b2 + 64 * ct2 + 16 * g + 8);
    SB();
#pragma unroll
    for (int kk = 0; kk < 14; ++kk) {
#pragma unroll
      for (int h = 0; h < 4; ++h)
#pragma unroll
        for (int i = 0; i < 4; ++i)
          acc[i][h] = __builtin_amdgcn_mfma_f32_16x16x32_bf16(__builtin_bit_cast(bf8v, w2f[kk][i]), __builtin_bit_cast(bf8v, hf[kk][h]), acc[i][h], 0, 0, 0);
      if (kk + PD < 14) {
#pragma unroll
        for (int h = 0; h < 4; ++h) hf[kk + PD][h] = bld4s<16>(rs, l16 + ((kk + PD) & 3) * 1024, hoff[h] + ((kk + PD) & ~3) * 1024);
      }
      SB();
    }
    STAMP(kid, 9);
    f4* red = (f4*)(smem + LDS_W1);
#pragma unroll
    for (int h = 1; h < 4; ++h)
#pragma unroll
      for (int i = 0; i < 4; ++i) red[((((w + h) & 3) * 3 + (h - 1)) * 4 + i) * 64 + lane] = acc[i][h];
    bar();
    f2 s[8];
#pragma unroll
    for (int i = 0; i < 4; ++i) {
      f4 a = (acc[i][0] + red[((w * 3 + 0) * 4 + i) * 64 + lane]) + (red[((w * 3 + 1) * 4 + i) * 64 + lane] + red[((w * 3 + 2) * 4 + i) * 64 + lane]);
      s[2 * i] = f2{a[0], a[1]}; s[2 * i + 1] = f2{a[2], a[3]};
    }
    u32 bs[8] = {bb0.x, bb0.y, bb0.z, bb0.w, bb1.x, bb1.y, bb1.z, bb1.w};
    u32 o[8];
#pragma unroll
    for (int i = 0; i < 8; ++i) o[i] = pack2(unpk(x1v[i]) + rbf2(rbf2(s[i]) + unpk(bs[i])));
    const int srow = 256 * (rho2 & 7) + (rho2 >> 3);
    const u32 xo = (srow * D_ + 64 * ct2 + 16 * g) * 2;
    if (last) {
      *(u4v*)((char*)out + xo) = u4v{o[0], o[1], o[2], o[3]};
      *(u4v*)((char*)out + xo + 16) = u4v{o[4], o[5], o[6], o[7]};
      STAMP(kid, 10);
      return;
    }
    bst4<16>(rs, OFF_XR + xo, u4v{o[0], o[1], o[2], o[3]});
    bst4<16>(rs, OFF_XR + xo + 16, u4v{o[4], o[5], o[6], o[7]});
  }
  STAMP(kid, 10);
  {
    wait_vm0();
    bar();
    signal_cnt(cnt + (32 + rt2) * 64);
    R1Tw tw;
    r1_load_tw(tw, t);
    wait_cnt(cnt + (32 + rt2) * 64, 8);
    STAMP(kid, 11);
    u32 xin[8];
    const u32 ro = OFF_XR + ((256 * (t >> 5) + s2) * 256 + (t & 31)) * 4;
#pragma unroll
    for (int m = 0; m < 8; ++m) xin[m] = bld1<16>(rs, ro + 128 * m);
    r1_body(xin, tw, tx, lds, ws, s2, t);
    STAMP(kid, 12);
  }
}

extern "C" void kernel_launch(void* const* d_in, const int* in_sizes, int n_in, void* d_out, int out_size, void* d_ws,
                              size_t ws_size, hipStream_t stream) {
  const unsigned short* x = (const unsigned short*)d_in[0];
  const unsigned short* wr = (const unsigned short*)d_in[1];
  const unsigned short* wi = (const unsigned short*)d_in[2];
  const unsigned short* w1 = (const unsigned short*)d_in[4];
  const unsigned short* b1 = (const unsigned short*)d_in[5];
  const unsigned short* w2 = (const unsigned short*)d_in[6];
  const unsigned short* b2 = (const unsigned short*)d_in[7];
  char* ws = (char*)d_ws;
  (void)hipFuncSetAttribute((const void*)k_m, hipFuncAttributeMaxDynamicSharedMemorySize, LDS_M);
  k_first<<<256, 256, 0, stream>>>((const u32*)x, wr, wi, w1, w2, ws);
  k_c<<<256, 256, 0, stream>>>(ws, 1);
  k_m<<<256, 256, LDS_M, stream>>>((const u32*)x, b1, b2, ws, (unsigned short*)d_out, 0, 2);
  k_c<<<256, 256, 0, stream>>>(ws, 3);
  k_m<<<256, 256, LDS_M, stream>>>((const u32*)(ws + OFF_XR), b1, b2, ws, (unsigned short*)d_out, 1, 4);
}
```

```cpp
#include <hip/hip_runtime.h>
#include <hip/hip_bf16.h>
#include <stdint.h>

typedef unsigned int u32;
typedef float f2 __attribute__((ext_vector_type(2)));
typedef float f4 __attribute__((ext_vector_type(4)));
typedef float f16v __attribute__((ext_vector_type(16)));
typedef u32 u2v __attribute__((ext_vector_type(2)));
typedef u32 u4v __attribute__((ext_vector_type(4)));
typedef __bf16 bf2v __attribute__((ext_vector_type(2)));
typedef __bf16 bf8v __attribute__((ext_vector_type(8)));
typedef short s4v __attribute__((ext_vector_type(4)));
typedef short s8v __attribute__((ext_vector_type(8)));
#define LAS __attribute__((address_space(3)))
#define DI __device__ __forceinline__
#define RLX_AGENT __ATOMIC_RELAXED, __HIP_MEMORY_SCOPE_AGENT

#define STAMP(K, I)

constexpr int S_ = 2048, D_ = 512, F_ = 1792, DF_ = 257;
constexpr u32 TPB = 16384 + 256;

constexpr u32 OFF_CNT = 0;
constexpr u32 OFF_T = 65536;
constexpr u32 SZ_T = 256u * TPB;
constexpr u32 OFF_WT = OFF_T + SZ_T;
constexpr u32 SZ_WT = 256u * 256 * 8 * 4;
constexpr u32 OFF_PQ = OFF_WT + SZ_WT;
constexpr u32 SZ_PQ = 256u * 8 * 16;
constexpr u32 OFF_W2F = OFF_PQ + SZ_PQ;
constexpr u32 SZ_W2F = (u32)F_ * D_ * 2;
constexpr u32 OFF_W1F = OFF_W2F + SZ_W2F;
constexpr u32 OFF_XR = OFF_W1F + SZ_W2F;
constexpr u32 SZ_X = (u32)S_ * D_ * 2;
constexpr u32 OFF_X1P = OFF_XR + SZ_X;
constexpr u32 OFF_LNF = OFF_X1P + SZ_X;
constexpr u32 OFF_H = OFF_LNF + SZ_X;
constexpr u32 SZ_H = (u32)S_ * F_ * 2;
constexpr u32 WS_USED = OFF_H + SZ_H;

struct cxf { float x, y; };
constexpr double kPi = 3.14159265358979323846264338327950288;
constexpr double c_sin(double x) { double x2 = x * x, t = x, s = x; for (int i = 1; i < 11; ++i) { t *= -x2 / double((2 * i) * (2 * i + 1)); s += t; } return s; }
constexpr double c_cos(double x) { double x2 = x * x, t = 1, s = 1; for (int i = 1; i < 11; ++i) { t *= -x2 / double((2 * i - 1) * (2 * i)); s += t; } return s; }
constexpr cxf tw2048(int i) {
  i &= 2047;
  int qd = i >> 9, r = i & 511;
  double c = 0, s = 0;
  if (r <= 256) { double a = 2 * kPi * r / 2048.0; c = c_cos(a); s = c_sin(a); }
  else { double a = 2 * kPi * (512 - r) / 2048.0; c = c_sin(a); s = c_cos(a); }
  double cc = 0, ss = 0;
  if (qd == 0) { cc = c; ss = s; } else if (qd == 1) { cc = -s; ss = c; } else if (qd == 2) { cc = -c; ss = -s; } else { cc = s; ss = -c; }
  return cxf{(float)cc, (float)(-ss)};
}
template <int R, int C> struct Tab { cxf v[R * C]; };
constexpr Tab<32, 8> mk_r1() { Tab<32, 8> t{}; for (int j = 0; j < 32; ++j) for (int q = 0; q < 8; ++q) t.v[j * 8 + q] = tw2048(8 * j * q); return t; }
constexpr Tab<8, 8> mk_r2() { Tab<8, 8> t{}; for (int j = 0; j < 8; ++j) for (int q = 0; q < 8; ++q) t.v[j * 8 + q] = tw2048(64 * j * q); return t; }
constexpr Tab<256, 1> mk_512() { Tab<256, 1> t{}; for (int c = 0; c < 256; ++c) t.v[c] = tw2048(4 * c + 512); return t; }
constexpr Tab<32, 8> mk_512u() { Tab<32, 8> t{}; for (int u = 0; u < 32; ++u) for (int i = 0; i < 8; ++i) t.v[u * 8 + i] = tw2048(4 * (u + 32 * (i >> 2) + 64 * (i & 3)) + 512); return t; }
constexpr Tab<32, 22> mk_r2all() {
  Tab<32, 22> t{};
  for (int j = 0; j < 32; ++j) {
    for (int i = 0; i < 8; ++i) t.v[j * 22 + i] = tw2048(4 * (j + 32 * (i >> 2) + 64 * (i & 3)) + 512);
    for (int a = 0; a < 2; ++a) for (int i = 1; i < 4; ++i) t.v[j * 22 + 8 + 3 * a + (i - 1)] = tw2048(64 * ((j >> 3) + 4 * a) * i);
    for (int q = 1; q < 8; ++q) t.v[j * 22 + 13 + q] = tw2048(8 * j * q);
    t.v[j * 22 + 21] = cxf{0.f, 0.f};
  }
  return t;
}
constexpr Tab<256, 8> mk_x() { Tab<256, 8> t{}; for (int s = 0; s < 256; ++s) for (int k = 0; k < 8; ++k) t.v[s * 8 + k] = tw2048(s * k); return t; }
constexpr Tab<64, 4> mk_c1() { Tab<64, 4> t{}; for (int j = 0; j < 64; ++j) for (int q = 0; q < 4; ++q) t.v[j * 4 + q] = tw2048(8 * j * q); return t; }
constexpr double kSc = 6.9053396600248785e-04;
constexpr cxf tw2048s(int i) {
  i &= 2047;
  int qd = i >> 9, r = i & 511;
  double c = 0, s = 0;
  if (r <= 256) { double a = 2 * kPi * r / 2048.0; c = c_cos(a); s = c_sin(a); }
  else { double a = 2 * kPi * (512 - r) / 2048.0; c = c_sin(a); s = c_cos(a); }
  double cc = 0, ss = 0;
  if (qd == 0) { cc = c; ss = s; } else if (qd == 1) { cc = -s; ss = c; } else if (qd == 2) { cc = -c; ss = -s; } else { cc = s; ss = -c; }
  return cxf{(float)(cc * kSc), (float)(-ss * kSc)};
}
constexpr Tab<8, 8> mk_c2() { Tab<8, 8> t{}; for (int j = 0; j < 8; ++j) for (int q = 0; q < 8; ++q) t.v[j * 8 + q] = tw2048s(32 * j * q); return t; }

__device__ const __attribute__((aligned(64))) Tab<32, 8> g_twr1 = mk_r1();
__device__ const __attribute__((aligned(64))) Tab<8, 8> g_twr2 = mk_r2();
__device__ const __attribute__((aligned(64))) Tab<256, 1> g_tw512 = mk_512();
__device__ const __attribute__((aligned(64))) Tab<32, 22> g_r2all = mk_r2all();
__device__ const __attribute__((aligned(64))) Tab<256, 8> g_twx = mk_x();
__device__ const __attribute__((aligned(64))) Tab<64, 4> g_twc1 = mk_c1();
__device__ const __attribute__((aligned(64))) Tab<8, 8> g_twc2 = mk_c2();

DI float bf_lo(u32 v) { return __uint_as_float(v << 16); }
DI float bf_hi(u32 v) { return __uint_as_float(v & 0xffff0000u); }
DI f2 unpk(u32 v) { return f2{bf_lo(v), bf_hi(v)}; }
DI float rbf(float x) { return (float)(__bf16)x; }
DI u32 pack2(f2 a) { bf2v v = __builtin_convertvector(a, bf2v); return __builtin_bit_cast(u32, v); }
DI f2 rbf2(f2 a) { return unpk(pack2(a)); }
DI f2 swp(f2 a) { return __builtin_shufflevector(a, a, 1, 0); }
DI f2 fma2(f2 a, f2 b, f2 c) { return __builtin_elementwise_fma(a, b, c); }
DI f2 xx(f2 a) { return __builtin_shufflevector(a, a, 0, 0); }
DI f2 yy(f2 a) { return __builtin_shufflevector(a, a, 1, 1); }
DI f2 cmul(f2 a, f2 b) {
  f2 t, r;
  asm("v_pk_mul_f32 %0, %1, %2 op_sel:[1,1] op_sel_hi:[1,0]" : "=v"(t) : "v"(a), "v"(b));
  asm("v_pk_fma_f32 %0, %1, %2, %3 op_sel:[0,0,0] op_sel_hi:[0,1,1] neg_lo:[0,0,1]" : "=v"(r) : "v"(a), "v"(b), "v"(t));
  return r;
}
DI f2 cmulc(f2 a, f2 b) {
  f2 t, r;
  asm("v_pk_mul_f32 %0, %1, %2 op_sel:[1,1] op_sel_hi:[1,0]" : "=v"(t) : "v"(a), "v"(b));
  asm("v_pk_fma_f32 %0, %1, %2, %3 op_sel:[0,0,0] op_sel_hi:[0,1,1] neg_hi:[0,1,0]" : "=v"(r) : "v"(a), "v"(b), "v"(t));
  return r;
}
DI f2 cmul_s(f2 a, f2 b) {
  f2 t, r;
  asm("v_pk_mul_f32 %0, %1, %2 op_sel:[1,1] op_sel_hi:[1,0]" : "=v"(t) : "v"(a), "s"(b));
  asm("v_pk_fma_f32 %0, %1, %2, %3 op_sel:[0,0,0] op_sel_hi:[0,1,1] neg_lo:[0,0,1]" : "=v"(r) : "v"(a), "s"(b), "v"(t));
  return r;
}
DI f2 cmulc_s(f2 a, f2 b) {
  f2 t, r;
  asm("v_pk_mul_f32 %0, %1, %2 op_sel:[1,1] op_sel_hi:[1,0]" : "=v"(t) : "v"(a), "s"(b));
  asm("v_pk_fma_f32 %0, %1, %2, %3 op_sel:[0,0,0] op_sel_hi:[0,1,1] neg_hi:[0,1,0]" : "=v"(r) : "v"(a), "s"(b), "v"(t));
  return r;
}
DI f2 cmul_add(f2 a, f2 b, f2 e) {
  f2 t, r;
  asm("v_pk_fma_f32 %0, %1, %2, %3 op_sel:[1,1,0] op_sel_hi:[1,0,1] neg_lo:[0,1,0]" : "=v"(t) : "v"(a), "v"(b), "v"(e));
  asm("v_pk_fma_f32 %0, %1, %2, %3 op_sel:[0,0,0] op_sel_hi:[0,1,1]" : "=v"(r) : "v"(a), "v"(b), "v"(t));
  return r;
}
DI f2 cmulc_add(f2 a, f2 b, f2 e) {
  f2 t, r;
  asm("v_pk_fma_f32 %0, %1, %2, %3 op_sel:[1,1,0] op_sel_hi:[1,0,1]" : "=v"(t) : "v"(a), "v"(b), "v"(e));
  asm("v_pk_fma_f32 %0, %1, %2, %3 op_sel:[0,0,0] op_sel_hi:[0,1,1] neg_hi:[0,1,0]" : "=v"(r) : "v"(a), "v"(b), "v"(t));
  return r;
}
DI f2 ccmul_add(f2 a, f2 b, f2 e) {
  f2 t, r;
  asm("v_pk_fma_f32 %0, %1, %2, %3 op_sel:[1,1,0] op_sel_hi:[1,0,1] neg_hi:[0,1,0]" : "=v"(t) : "v"(a), "v"(b), "v"(e));
  asm("v_pk_fma_f32 %0, %1, %2, %3 op_sel:[0,0,0] op_sel_hi:[0,1,1]" : "=v"(r) : "v"(a), "v"(b), "v"(t));
  return r;
}
template <bool INV> DI f2 addmi(f2 a, f2 d) { return fma2(swp(d), INV ? f2{-1.f, 1.f} : f2{1.f, -1.f}, a); }
template <bool INV> DI f2 submi(f2 a, f2 d) { return fma2(swp(d), INV ? f2{1.f, -1.f} : f2{-1.f, 1.f}, a); }

template <bool INV> DI void fft4(f2& b0, f2& b1, f2& b2, f2& b3) {
  f2 c0 = b0 + b2, c2 = b0 - b2, c1 = b1 + b3, d = b1 - b3;
  b0 = c0 + c1; b2 = c0 - c1; b1 = addmi<INV>(c2, d); b3 = submi<INV>(c2, d);
}
template <bool INV> DI void fft8(f2* v) {
  const float h = 0.70710678118654752440f;
  f2 a0 = v[0] + v[4], a4 = v[0] - v[4];
  f2 a1 = v[1] + v[5], a5 = v[1] - v[5];
  f2 a2 = v[2] + v[6], a6 = v[2] - v[6];
  f2 a3 = v[3] + v[7], a7 = v[3] - v[7];
  f2 c0 = addmi<INV>(a4, a6), c2 = submi<INV>(a4, a6);
  f2 u = addmi<INV>(a5, a7), w = submi<INV>(a5, a7);
  f2 tu = addmi<INV>(u, u), tw = addmi<INV>(w, w);
  fft4<INV>(a0, a1, a2, a3);
  v[0] = a0; v[2] = a1; v[4] = a2; v[6] = a3;
  v[1] = fma2(tu, f2{h, h}, c0);
  v[5] = fma2(tu, f2{-h, -h}, c0);
  v[3] = fma2(swp(tw), INV ? f2{-h, h} : f2{h, -h}, c2);
  v[7] = fma2(swp(tw), INV ? f2{h, -h} : f2{-h, h}, c2);
}
template <int CTRL> DI float dpp_add(float x) {
  return x + __int_as_float(__builtin_amdgcn_update_dpp(0, __float_as_int(x), CTRL, 0xf, 0xf, false));
}
DI float red32(float x) {
  x = dpp_add<0xB1>(x); x = dpp_add<0x4E>(x); x = dpp_add<0x141>(x); x = dpp_add<0x140>(x);
  auto r = __builtin_amdgcn_permlane16_swap(__float_as_uint(x), __float_as_uint(x), false, false);
  return __uint_as_float(r[0]) + __uint_as_float(r[1]);
}
DI void wbar() { __builtin_amdgcn_wave_barrier(); }
DI void bar() {
  asm volatile("s_waitcnt lgkmcnt(0)" ::: "memory");
  __builtin_amdgcn_s_barrier();
  asm volatile("" ::: "memory");
}
DI void wait_vm0() { __builtin_amdgcn_s_waitcnt(0x0F70); asm volatile("" ::: "memory"); }
DI void layernorm8(const f2* v, u32* o) {
  f2 s2 = ((v[0] + v[1]) + (v[2] + v[3])) + ((v[4] + v[5]) + (v[6] + v[7]));
  float mean = red32(s2.x + s2.y) * (1.0f / 512.0f);
  f2 mm = f2{mean, mean};
  f2 q0 = f2{0, 0}, q1 = f2{0, 0};
#pragma unroll
  for (int i = 0; i < 8; i += 2) { f2 d0 = v[i] - mm, d1 = v[i + 1] - mm; q0 = fma2(d0, d0, q0); q1 = fma2(d1, d1, q1); }
  q0 = q0 + q1;
  float q = red32(q0.x + q0.y);
  float m = rbf(mean), vr = rbf(q * (1.0f / 512.0f));
  float sd = rbf(__builtin_amdgcn_sqrtf(rbf(vr + 1e-5f)));
  float ri = __builtin_amdgcn_rcpf(sd);
  f2 m2 = f2{m, m}, r2 = f2{ri, ri};
#pragma unroll
  for (int i = 0; i < 8; ++i) o[i] = pack2(rbf2(v[i] - m2) * r2);
}
DI f2 ldtw(const cxf* p) { return *(const f2*)p; }
typedef __amdgpu_buffer_rsrc_t rsrc_t;
DI rsrc_t mk_rsrc(void* p, u32 bytes) { return __builtin_amdgcn_make_buffer_rsrc(p, 0, bytes, 0x00020000); }
template <int AUX> DI u4v bld4(rsrc_t r, u32 off) { return __builtin_amdgcn_raw_buffer_load_b128(r, off, 0, AUX); }
template <int AUX> DI u32 bld1(rsrc_t r, u32 off) { return __builtin_amdgcn_raw_buffer_load_b32(r, off, 0, AUX); }
template <int AUX> DI void bst4(rsrc_t r, u32 off, u4v v) { __builtin_amdgcn_raw_buffer_store_b128(v, r, off, 0, AUX); }
DI u4v f4u(f2 a, f2 b) { return u4v{__float_as_uint(a.x), __float_as_uint(a.y), __float_as_uint(b.x), __float_as_uint(b.y)}; }

constexpr int RP = 288;

DI void signal_cnt(u32* c) { if (threadIdx.x == 0) __hip_atomic_fetch_add(c, 1u, RLX_AGENT); }
DI void wait_cnt(u32* c, u32 want) {
  if (threadIdx.x < 64) {
    for (int sp = 0; sp < (1 << 20); ++sp) {
      u32 v = __hip_atomic_load(c, RLX_AGENT);
      if (__builtin_amdgcn_readfirstlane(v) >= want) break;
      __builtin_amdgcn_s_sleep(1);
    }
  }
  bar();
}

struct R1Tw { f2 tw1[8], tw2[8], w512; };
DI void r1_load_tw(R1Tw& tw, int t) {
  const int j = t & 31;
#pragma unroll
  for (int q = 1; q < 8; ++q) { tw.tw1[q] = ldtw(&g_twr1.v[j * 8 + q]); tw.tw2[q] = ldtw(&g_twr2.v[(j & 3) * 8 + q]); }
  tw.w512 = ldtw(&g_tw512.v[t]);
}
DI void r1_body(const u32* xin, const R1Tw& tw, const f16v& tx, f2* lds, char* ws, int s2, int t) {
  const int s1 = t >> 5, j = t & 31;
  f2 z[8];
#pragma unroll
  for (int m = 0; m < 8; ++m) z[m] = unpk(xin[m]);
  u32 o[8];
  layernorm8(z, o);
#pragma unroll
  for (int m = 0; m < 8; ++m) z[m] = unpk(o[m]);
  f2* L = lds + s1 * RP;
  fft8<false>(z);
#pragma unroll
  for (int q = 1; q < 8; ++q) z[q] = cmul(z[q], tw.tw1[q]);
#pragma unroll
  for (int q = 0; q < 8; ++q) L[q * 36 + j] = z[q];
  wbar();
#pragma unroll
  for (int m = 0; m < 8; ++m) z[m] = L[(j >> 2) * 36 + (j & 3) + 4 * m];
  wbar();
  fft8<false>(z);
#pragma unroll
  for (int q = 1; q < 8; ++q) z[q] = cmul(z[q], tw.tw2[q]);
#pragma unroll
  for (int q = 0; q < 8; ++q) L[(j & 3) * 68 + 8 * q + (j >> 2)] = z[q];
  wbar();
#pragma unroll
  for (int i = 0; i < 8; ++i) z[i] = L[(i & 3) * 68 + 32 * (i >> 2) + j];
  wbar();
  fft4<false>(z[0], z[1], z[2], z[3]);
  fft4<false>(z[4], z[5], z[6], z[7]);
#pragma unroll
  for (int i = 0; i < 8; ++i) L[j + 32 * (i >> 2) + 64 * (i & 3)] = z[i];
  bar();
  f2 X[8];
#pragma unroll
  for (int r = 0; r < 8; ++r) {
    f2 A = lds[r * RP + t], Bc = lds[r * RP + ((256 - t) & 255)];
    f2 E = fma2(Bc, f2{1.f, -1.f}, A);
    f2 Dm = fma2(Bc, f2{-1.f, 1.f}, A);
    f2 x2 = cmul_add(Dm, tw.w512, E);
    if (t == 0) x2.y = E.x - Dm.y;
    X[r] = x2;
  }
  fft8<false>(X);
#pragma unroll
  for (int k = 1; k < 8; ++k) X[k] = cmul_s(X[k], f2{tx[2 * k], tx[2 * k + 1]});
  u4v* dst = (u4v*)(ws + OFF_T + (size_t)s2 * TPB) + t;
  dst[0] = f4u(X[0], X[4]);
  dst[256] = f4u(X[1], X[7]);
  dst[512] = f4u(X[2], X[6]);
  dst[768] = f4u(X[3], X[5]);
}

DI int f_of(int kap, int g, int j) {
  int u = kap / 7, r = kap % 7;
  if (r < 6) { int ct = 2 * u + (r >= 3), p = r % 3; return 112 * ct + 32 * p + (j < 4 ? 4 * g + j : 16 + 4 * g + (j - 4)); }
  return 112 * (2 * u + (g >> 1)) + 96 + 8 * (g & 1) + j;
}
constexpr int WPIT = 528;
__global__ __launch_bounds__(256) void k_first(const u32* __restrict__ x, const unsigned short* __restrict__ wr,
                                               const unsigned short* __restrict__ wi, const unsigned short* __restrict__ w1,
                                               const unsigned short* __restrict__ w2, char* __restrict__ ws) {
  __shared__ __attribute__((aligned(16))) char smem[8 * RP * 8 + 32 * WPIT];
  const int b = blockIdx.x, t = threadIdx.x, w = __builtin_amdgcn_readfirstlane(t >> 6), lane = t & 63;
  STAMP(0, 0);
  const int s2 = 32 * (b & 7) + (b >> 3);
  u32 xin[8];
  const u32* xr = x + (size_t)(256 * (t >> 5) + s2) * 256 + (t & 31);
#pragma unroll
  for (int m = 0; m < 8; ++m) xin[m] = xr[32 * m];
  R1Tw tw;
  r1_load_tw(tw, t);
  const f16v tx = *(const f16v*)&g_twx.v[s2 * 8];
  unsigned short fr[8], fi[8];
#pragma unroll
  for (int i = 0; i < 8; ++i) { fr[i] = wr[(b + 256 * i) * DF_ + t]; fi[i] = wi[(b + 256 * i) * DF_ + t]; }
  u4v wu[4];
  const int isw2 = b >= 112;
  const int ua = isw2 ? (b - 112) >> 1 : b / 7, ub = isw2 ? (b - 112) & 1 : b % 7;
  if (b < 224) {
#pragma unroll
    for (int g = 0; g < 4; ++g) {
      const int i = t >> 5;
      const unsigned short* p;
      if (isw2) p = w2 + (size_t)f_of(ua, g, i) * D_;
      else p = w1 + (size_t)(256 * (g & 1) + 64 * (i >> 1) + 4 * ua + 2 * (g >> 1) + (i & 1)) * F_;
      wu[g] = *(const u4v*)(p + 256 * ub + (t & 31) * 8);
    }
  } else {
#pragma unroll
    for (int g = 0; g < 4; ++g) asm volatile("" : "=v"(wu[g]));
  }
  unsigned short pv[8];
  {
    const int k = b + 256 * (t & 7), km = (2048 - k) & 2047;
    pv[0] = wr[k * DF_]; pv[1] = wi[k * DF_]; pv[2] = wr[km * DF_]; pv[3] = wi[km * DF_];
    pv[4] = wr[k * DF_ + 256]; pv[5] = wi[k * DF_ + 256]; pv[6] = wr[km * DF_ + 256]; pv[7] = wi[km * DF_ + 256];
  }
  __builtin_amdgcn_sched_barrier(0);
  if (b < 224) {
    char* im = smem + 8 * RP * 8;
#pragma unroll
    for (int g = 0; g < 4; ++g) *(u4v*)(im + (8 * g + (t >> 5)) * WPIT + (t & 31) * 16) = wu[g];
    bar();
    const int g = lane >> 4, q = (lane >> 2) & 3, p = lane & 3;
    const char* a0 = im + (8 * g + q) * WPIT + (isw2 ? 32 * p : 8 * p) + 128 * w;
#pragma unroll
    for (int n = 0; n < 4; ++n) {
      const char* a = a0 + (isw2 ? 8 * n : 32 * n);
      s4v lo = __builtin_amdgcn_ds_read_tr16_b64_v4i16((LAS s4v*)a);
      s4v hi = __builtin_amdgcn_ds_read_tr16_b64_v4i16((LAS s4v*)(a + 4 * WPIT));
      s8v wf = __builtin_shufflevector(lo, hi, 0, 1, 2, 3, 4, 5, 6, 7);
      u32 off;
      if (isw2) off = OFF_W2F + (((4 * ub + w) * 56 + ua) * 4 + n) * 1024;
      else { int ft = 16 * ub + 4 * w + n; off = OFF_W1F + (((ft / 7) * 16 + ua) * 7 + ft % 7) * 1024; }
      *(u4v*)(ws + off + lane * 16) = __builtin_bit_cast(u4v, wf);
    }
  }
  {
    const int k1 = b & 7, kp = k1 <= 4 ? (k1 & 3) : 8 - k1, e = k1 >= 4;
    const int tl = e * 32 + ((b >> 3) & 3) * 8 + (b >> 5);
    u32 o[8];
#pragma unroll
    for (int i = 0; i < 8; ++i) o[i] = (u32)fr[i] | ((u32)fi[i] << 16);
    u4v* dst = (u4v*)(ws + OFF_WT) + ((size_t)(kp * 64 + (t >> 2)) * 256 + (t & 3) * 64 + tl) * 2;
    dst[0] = u4v{o[0], o[1], o[2], o[3]};
    dst[1] = u4v{o[4], o[5], o[6], o[7]};
    auto cv = [](unsigned short v) { return __uint_as_float((u32)v << 16); };
    float h0r = 0.5f * (cv(pv[0]) + cv(pv[2])), h0i = 0.5f * (cv(pv[1]) - cv(pv[3]));
    float h1r = 0.5f * (cv(pv[4]) + cv(pv[6])), h1i = 0.5f * (cv(pv[5]) - cv(pv[7]));
    if (t < 8) ((f4*)(ws + OFF_PQ))[(kp * 64 + tl) * 8 + t] = f4{0.5f * (h0r + h1r), 0.5f * (h0i + h1i), 0.5f * (h0r - h1r), 0.5f * (h0i - h1i)};
  }
  STAMP(0, 1);
  __builtin_amdgcn_sched_barrier(0);
  r1_body(xin, tw, tx, (f2*)smem, ws, s2, t);
  STAMP(0, 2);
}

constexpr int PK = 292, QS = 72;
DI int idx1(int k1, int q, int j) { return k1 * PK + q * QS + (j ^ ((k1 >> 2) << 2)); }
template <int AUX> DI u4v bld4s(rsrc_t r, u32 off, u32 soff) { return __builtin_amdgcn_raw_buffer_load_b128(r, off, soff, AUX); }
template <int AUX> DI void bst4s(rsrc_t r, u32 off, u32 soff, u4v v) { __builtin_amdgcn_raw_buffer_store_b128(v, r, off + soff, 0, AUX); }
__global__ __launch_bounds__(256) void k_c(char* __restrict__ ws, int kid) {
  __shared__ f2 lds[8 * PK];
  __shared__ __attribute__((aligned(16))) char sink[4096];
  STAMP(kid, 0);
  const rsrc_t rs = mk_rsrc(ws, WS_USED);
  const int b = blockIdx.x, qd = 8 * (b & 7) + ((b >> 3) & 7), kp = b >> 6;
  const int t = threadIdx.x, cc = t & 3, j = t >> 2;
  u4v in[4];
  const u32 tv = j * TPB + cc * 16, ts = OFF_T + kp * 4096 + qd * 64;
#pragma unroll
  for (int m = 0; m < 4; ++m) in[m] = bld4s<0>(rs, tv, ts + m * 64 * TPB);
  const u32 wsof = OFF_WT + (kp * 64 + qd) * 8192;
  u4v w0 = bld4s<0>(rs, t * 32, wsof), w1 = bld4s<0>(rs, t * 32 + 16, wsof);
  f2 tc1[4], tc2[8];
#pragma unroll
  for (int q = 1; q < 4; ++q) tc1[q] = ldtw(&g_twc1.v[j * 4 + q]);
#pragma unroll
  for (int q = 1; q < 8; ++q) tc2[q] = ldtw(&g_twc2.v[(t & 7) * 8 + q]);
  const bool special = qd == 0 && __builtin_amdgcn_readfirstlane(t >> 6) == 0;
  u4v pq[8];
  if (special) {
#pragma unroll
    for (int i = 0; i < 8; ++i) pq[i] = bld4s<0>(rs, t * 128 + i * 16, OFF_PQ + kp * 8192);
  } else {
#pragma unroll
    for (int i = 0; i < 8; ++i) asm volatile("" : "=v"(pq[i]));
  }
  if (b == 1 && t < 64) ((u32*)(ws + OFF_CNT))[t * 64] = 0u;
#pragma unroll
  for (int e = 0; e < 2; ++e) {
    f2 a[4];
#pragma unroll
    for (int m = 0; m < 4; ++m) a[m] = e ? f2{__uint_as_float(in[m].z), __uint_as_float(in[m].w)} : f2{__uint_as_float(in[m].x), __uint_as_float(in[m].y)};
    fft4<false>(a[0], a[1], a[2], a[3]);
#pragma unroll
    for (int q = 1; q < 4; ++q) a[q] = cmul(a[q], tc1[q]);
#pragma unroll
    for (int q = 0; q < 4; ++q) lds[idx1(2 * cc + e, q, j)] = a[q];
  }
  {
    asm volatile("" :: "v"(w0), "v"(w1), "v"(tc2[1]), "v"(tc2[2]), "v"(tc2[3]), "v"(tc2[4]), "v"(tc2[5]), "v"(tc2[6]), "v"(tc2[7]),
                 "v"(pq[0]), "v"(pq[1]), "v"(pq[2]), "v"(pq[3]), "v"(pq[4]), "v"(pq[5]), "v"(pq[6]), "v"(pq[7]));
    __builtin_amdgcn_sched_barrier(0);
    const int wv = __builtin_amdgcn_readfirstlane(t >> 6);
    const u32 ps = OFF_W1F + (b >> 3) * 57344 + wv * 14336;
    __builtin_amdgcn_sched_barrier(0);
  }
  bar();
  const int k1 = t >> 5, q = (t >> 3) & 3, jp = t & 7;
  const float sc = (float)kSc;
  f2 v[8];
#pragma unroll
  for (int m = 0; m < 8; ++m) v[m] = lds[idx1(k1, q, jp + 8 * m)];
  wbar();
  fft8<false>(v);
  v[0] = v[0] * f2{sc, sc};
#pragma unroll
  for (int i = 1; i < 8; ++i) v[i] = cmul(v[i], tc2[i]);
  f2* L2 = lds + k1 * PK + q * QS;
#pragma unroll
  for (int i = 0; i < 8; ++i) L2[jp * 9 + i] = v[i];
  wbar();
#pragma unroll
  for (int i = 0; i < 8; ++i) v[i] = L2[i * 9 + jp];
  wbar();
  fft8<false>(v);
  if (!special) {
    u32 w[8] = {w0.x, w0.y, w0.z, w0.w, w1.x, w1.y, w1.z, w1.w};
#pragma unroll
    for (int i = 0; i < 8; ++i) v[i] = cmul(v[i], unpk(w[i]));
  } else {
#pragma unroll
    for (int i = 0; i < 8; ++i) lds[k1 * PK + i * 32 + (t & 31)] = v[i];
    wbar();
    const bool z0 = kp == 0 && k1 == 0;
    const int u = q + 4 * jp, up = z0 ? (32 - u) & 31 : 31 - u;
    const int zi = (z0 && u == 0) ? 8 : 7;
    const f2* src = lds + (kp == 0 ? k1 : k1 ^ 1) * PK + (up & 3) * 8 + (up >> 2);
#pragma unroll
    for (int i = 0; i < 8; ++i) {
      f2 zm = src[((zi - i) & 7) * 32];
      f2 P = f2{__uint_as_float(pq[i].x), __uint_as_float(pq[i].y)}, Q = f2{__uint_as_float(pq[i].z), __uint_as_float(pq[i].w)};
      v[i] = ccmul_add(zm, Q, cmul(v[i], P));
    }
    wbar();
  }
  fft8<true>(v);
  v[0] = v[0] * f2{sc, sc};
#pragma unroll
  for (int i = 1; i < 8; ++i) v[i] = cmulc(v[i], tc2[i]);
#pragma unroll
  for (int i = 0; i < 8; ++i) L2[i * 9 + jp] = v[i];
  wbar();
#pragma unroll
  for (int i = 0; i < 8; ++i) v[i] = L2[jp * 9 + i];
  wbar();
  fft8<true>(v);
#pragma unroll
  for (int m = 0; m < 8; ++m) lds[idx1(k1, q, jp + 8 * m)] = v[m];
  bar();
  f2 o[2][4];
#pragma unroll
  for (int e = 0; e < 2; ++e) {
#pragma unroll
    for (int qq = 0; qq < 4; ++qq) o[e][qq] = lds[idx1(2 * cc + e, qq, j)];
#pragma unroll
    for (int qq = 1; qq < 4; ++qq) o[e][qq] = cmulc(o[e][qq], tc1[qq]);
    fft4<true>(o[e][0], o[e][1], o[e][2], o[e][3]);
  }
#pragma unroll
  for (int m = 0; m < 4; ++m) bst4s<0>(rs, tv, ts + m * 64 * TPB, f4u(o[0][m], o[1][m]));
  STAMP(kid, 1);
}

#define SB() __builtin_amdgcn_sched_barrier(0)
template <int N> DI void actN(const f2* a, const f2* bias, u32* o) {
  const float c0 = -2.0f * 0.7978845608028654f * 1.4426950408889634f, c1 = c0 * 0.044715f;
  f2 x[N], u[N];
#pragma unroll
  for (int i = 0; i < N; ++i) x[i] = rbf2(a[i]);
  SB();
#pragma unroll
  for (int i = 0; i < N; ++i) x[i] = x[i] + bias[i];
  SB();
#pragma unroll
  for (int i = 0; i < N; ++i) x[i] = rbf2(x[i]);
  SB();
#pragma unroll
  for (int i = 0; i < N; ++i) u[i] = x[i] * x[i];
  SB();
#pragma unroll
  for (int i = 0; i < N; ++i) u[i] = fma2(u[i], f2{c1, c1}, f2{c0, c0});
  SB();
#pragma unroll
  for (int i = 0; i < N; ++i) u[i] = u[i] * x[i];
  SB();
#pragma unroll
  for (int i = 0; i < N; ++i) u[i] = f2{__builtin_amdgcn_exp2f(u[i].x), __builtin_amdgcn_exp2f(u[i].y)};
  SB();
#pragma unroll
  for (int i = 0; i < N; ++i) u[i] = u[i] + f2{1.f, 1.f};
  SB();
#pragma unroll
  for (int i = 0; i < N; ++i) u[i] = f2{__builtin_amdgcn_rcpf(u[i].x), __builtin_amdgcn_rcpf(u[i].y)};
  SB();
#pragma unroll
  for (int i = 0; i < N; ++i) u[i] = u[i] * x[i];
  SB();
#pragma unroll
  for (int i = 0; i < N; ++i) o[i] = pack2(u[i]);
  SB();
}

#define ALD4(dst, voff, sbase, imm) asm volatile("global_load_dwordx4 %0, %1, %2 offset:" #imm : "=v"(dst) : "v"(voff), "s"(sbase))
#define ALD1(dst, voff, sbase, imm) asm volatile("global_load_dword %0, %1, %2 offset:" #imm : "=v"(dst) : "v"(voff), "s"(sbase))
#define DBG_HOFF OFF_H
#define DBG_LOFF OFF_LNF
#define HFLD(d, e) d = e
#define PROBE_RUN(I)
#define AUXL 16
constexpr int LDS_W1 = 114688;
constexpr int LDS_M = LDS_W1 + 49152;
constexpr int W2E = 7;
constexpr int PA = 6;

__global__ __launch_bounds__(256, 1) void k_m(const u32* __restrict__ xres, const unsigned short* __restrict__ b1,
                                              const unsigned short* __restrict__ b2, char* ws, unsigned short* out, int last, int kid) {
  extern __shared__ __attribute__((aligned(16))) char smem[];
  STAMP(kid, 0);
  const int b = blockIdx.x, xcd = b & 7, ii = b >> 3;
  const int t = threadIdx.x, w = __builtin_amdgcn_readfirstlane(t >> 6), lane = t & 63, g = lane >> 4, c = lane & 15;
  const int s2 = 32 * xcd + ii, rt = 2 * xcd + (ii >> 4), ct = ii & 15, rt2 = 4 * xcd + (ii >> 3), ct2 = ii & 7;
  const rsrc_t rs = mk_rsrc(ws, WS_USED);
  u32* cnt = (u32*)(ws + OFF_CNT);
  f2* lds = (f2*)(smem + LDS_W1);
  u4v w2f[14][4];
  f16v tx;
  const u32 l16 = lane * 16;
  const u32 w2s = OFF_W2F + (ct2 * 56 + 14 * w) * 4096;
  const int rot2 = (ct2 * 7) >> 2;
#define KROT(kk) (((kk) + rot2) >= 14 ? (kk) + rot2 - 14 : (kk) + rot2)
#define W2LD(kk) do { const u32 so_ = w2s + KROT(kk) * 4096; _Pragma("unroll") for (int i_ = 0; i_ < 4; ++i_) w2f[kk][i_] = bld4s<0>(rs, l16 + i_ * 1024, so_); } while (0)
  PROBE_RUN(0);
  {
    const int s1 = t >> 5, j = t & 31;
    u4v in[4], tq[11];
    u32 xin[8];
    {
      const char* tb = ws + OFF_T + (size_t)s2 * TPB;
      const u32 t16 = t * 16;
      ALD4(in[0], t16, tb, 0);
      ALD4(in[1], t16, tb + 4096, 0);
      ALD4(in[2], t16, tb + 8192, 0);
      ALD4(in[3], t16, tb + 12288, 0);
      const u32 jo = j * 176;
      const void* tp = &g_r2all;
      ALD4(tq[0], jo, tp, 0); ALD4(tq[1], jo, tp, 16); ALD4(tq[2], jo, tp, 32); ALD4(tq[3], jo, tp, 48);
      ALD4(tq[4], jo, tp, 64); ALD4(tq[5], jo, tp, 80); ALD4(tq[6], jo, tp, 96); ALD4(tq[7], jo, tp, 112);
      ALD4(tq[8], jo, tp, 128); ALD4(tq[9], jo, tp, 144); ALD4(tq[10], jo, tp, 160);
      const u32 xo = s1 * (256 * 1024) + j * 4;
      const u32* xb = xres + s2 * 256;
      ALD1(xin[0], xo, xb, 0); ALD1(xin[1], xo, xb, 128); ALD1(xin[2], xo, xb, 256); ALD1(xin[3], xo, xb, 384);
      ALD1(xin[4], xo, xb, 512); ALD1(xin[5], xo, xb, 640); ALD1(xin[6], xo, xb, 768); ALD1(xin[7], xo, xb, 896);
    }
    SB();
    tx = *(const f16v*)&g_twx.v[s2 * 8];
    SB();
    const char* gsrc = ws + OFF_W1F + (size_t)ct * LDS_W1 + l16;
#define W1DMA(pa, pb) do { SB(); _Pragma("unroll") for (int p_ = (pa); p_ < (pb); ++p_) { \
      const int sl_ = p_ / 7, pc_ = p_ % 7; \
      const char* gp_ = gsrc + ((4 * w + sl_ + ct) & 15) * 7168 + (pc_ & 4) * 1024; \
      char* lp_ = smem + (4 * w + sl_) * 7168 + (pc_ & 4) * 1024; \
      if ((pc_ & 3) == 0) __builtin_amdgcn_global_load_lds((const void*)gp_, (LAS void*)lp_, 16, 0, 0); \
      if ((pc_ & 3) == 1) __builtin_amdgcn_global_load_lds((const void*)gp_, (LAS void*)lp_, 16, 1024, 0); \
      if ((pc_ & 3) == 2) __builtin_amdgcn_global_load_lds((const void*)gp_, (LAS void*)lp_, 16, 2048, 0); \
      if ((pc_ & 3) == 3) __builtin_amdgcn_global_load_lds((const void*)gp_, (LAS void*)lp_, 16, 3072, 0); } SB(); } while (0)
#define L2PF(voff, soff) __builtin_amdgcn_raw_ptr_buffer_load_lds(rs, (LAS void*)(smem + LDS_W1 + 32768 + w * 1024), 16, voff, soff, 0, 0)
    const u32 p2s = OFF_W2F + ii * 57344 + w * 14336;
#define W2PF(pa, pb) do { SB(); _Pragma("unroll") for (int p_ = (pa); p_ < (pb); ++p_) L2PF(l16, p2s + p_ * 1024); SB(); } while (0)
    W1DMA(0, 4);
    asm volatile("s_waitcnt vmcnt(23)" : "+v"(in[0]), "+v"(in[1]), "+v"(in[2]), "+v"(in[3]));
    STAMP(kid, 1);
    f2 X[8];
    {
      auto lo = [](u4v v) { return f2{__uint_as_float(v.x), __uint_as_float(v.y)}; };
      auto hi = [](u4v v) { return f2{__uint_as_float(v.z), __uint_as_float(v.w)}; };
      X[0] = lo(in[0]); X[4] = hi(in[0]); X[1] = lo(in[1]); X[7] = hi(in[1]);
      X[2] = lo(in[2]); X[6] = hi(in[2]); X[3] = lo(in[3]); X[5] = hi(in[3]);
    }
#pragma unroll
    for (int k = 1; k < 8; ++k) X[k] = cmulc_s(X[k], f2{tx[2 * k], tx[2 * k + 1]});
    fft8<true>(X);
#pragma unroll
    for (int r = 0; r < 8; ++r) lds[r * RP + t] = X[r];
    W1DMA(4, 8);
    asm volatile("s_waitcnt vmcnt(8)" : "+v"(tq[0]), "+v"(tq[1]), "+v"(tq[2]), "+v"(tq[3]), "+v"(tq[4]), "+v"(tq[5]), "+v"(tq[6]), "+v"(tq[7]),
                 "+v"(tq[8]), "+v"(tq[9]), "+v"(tq[10]), "+v"(xin[0]), "+v"(xin[1]), "+v"(xin[2]), "+v"(xin[3]), "+v"(xin[4]), "+v"(xin[5]),
                 "+v"(xin[6]), "+v"(xin[7]));
    f2 tw1[8], twu[8], tw2[2][4];
    {
      f2 tv[22];
#pragma unroll
      for (int i = 0; i < 11; ++i) {
        tv[2 * i] = f2{__uint_as_float(tq[i].x), __uint_as_float(tq[i].y)};
        tv[2 * i + 1] = f2{__uint_as_float(tq[i].z), __uint_as_float(tq[i].w)};
      }
#pragma unroll
      for (int i = 0; i < 8; ++i) twu[i] = tv[i];
#pragma unroll
      for (int a = 0; a < 2; ++a)
#pragma unroll
        for (int i = 1; i < 4; ++i) tw2[a][i] = tv[8 + 3 * a + (i - 1)];
#pragma unroll
      for (int q = 1; q < 8; ++q) tw1[q] = tv[13 + q];
    }
    bar();
    f2* L = lds + s1 * RP;
    f2 z[8];
#pragma unroll
    for (int i = 0; i < 8; ++i) {
      int k = j + 32 * (i >> 2) + 64 * (i & 3);
      f2 A = L[k], Bc = L[(256 - k) & 255];
      f2 E = fma2(Bc, f2{1.f, -1.f}, A);
      f2 Dm = fma2(Bc, f2{-1.f, 1.f}, A);
      f2 zz = cmulc_add(Dm, twu[i], E);
      if (i == 0 && j == 0) zz = f2{A.x + A.y, A.x - A.y};
      z[i] = zz;
    }
    wbar();
    W1DMA(8, 12);
    fft4<true>(z[0], z[1], z[2], z[3]);
    fft4<true>(z[4], z[5], z[6], z[7]);
#pragma unroll
    for (int a = 0; a < 2; ++a)
#pragma unroll
      for (int i = 1; i < 4; ++i) z[4 * a + i] = cmulc(z[4 * a + i], tw2[a][i]);
#pragma unroll
    for (int i = 0; i < 8; ++i) L[(i & 3) * 72 + 32 * (i >> 2) + j] = z[i];
    wbar();
#pragma unroll
    for (int q = 0; q < 8; ++q) z[q] = L[(j & 3) * 72 + 8 * q + (j >> 2)];
    W1DMA(12, 16);
    fft8<true>(z);
#pragma unroll
    for (int m = 0; m < 8; ++m) L[(j >> 2) * 36 + (j & 3) + 4 * m] = z[m];
    wbar();
#pragma unroll
    for (int q = 0; q < 8; ++q) z[q] = L[q * 36 + j];
    W1DMA(16, 22);
#pragma unroll
    for (int q = 1; q < 8; ++q) z[q] = cmulc(z[q], tw1[q]);
    fft8<true>(z);
    W1DMA(22, 28);
    u32 x1[8], o[8];
#pragma unroll
    for (int m = 0; m < 8; ++m) { x1[m] = pack2(unpk(xin[m]) + rbf2(z[m])); z[m] = unpk(x1[m]); }
    const int rho = 8 * s2 + s1;
    u32* Lw = (u32*)L;
#pragma unroll
    for (int m = 0; m < 8; ++m) Lw[32 * m + j] = x1[m];
    wbar();
    u4v xa = *(const u4v*)(Lw + 4 * j), xb = *(const u4v*)(Lw + 128 + 4 * j);
    layernorm8(z, o);
    const u32 lo = OFF_LNF + (((rho >> 4) * 16 + (j >> 1)) * 64 + (rho & 15) * 4 + (j & 1) * 2) * 16;
    bst4<16>(rs, lo, u4v{o[0], o[1], o[2], o[3]});
    bst4<16>(rs, lo + 16, u4v{o[4], o[5], o[6], o[7]});
    bst4<16>(rs, OFF_X1P + rho * 1024 + j * 16, xa);
    bst4<16>(rs, OFF_X1P + rho * 1024 + j * 16 + 512, xb);
  }
  STAMP(kid, 2);
  wait_vm0();
  bar();
  signal_cnt(cnt + rt * 64);
  STAMP(kid, 3);
  if (w != 0) {
    const u32 lv = (lane >> 5) * 1024 + (lane & 31) * 16;
    const u32 ls = OFF_LNF + (s2 >> 1) * 16384 + (s2 & 1) * 512;
    const u32 xs = OFF_X1P + s2 * 8192;
    if (w == 1) { L2PF(lv, ls); L2PF(lv, ls + 2048); L2PF(lv, ls + 4096); L2PF(l16, xs); L2PF(l16, xs + 1024); }
    if (w == 2) { L2PF(lv, ls + 6144); L2PF(lv, ls + 8192); L2PF(lv, ls + 10240); L2PF(l16, xs + 2048); L2PF(l16, xs + 3072); L2PF(l16, xs + 4096); }
    if (w == 3) { L2PF(lv, ls + 12288); L2PF(lv, ls + 14336); L2PF(l16, xs + 5120); L2PF(l16, xs + 6144); L2PF(l16, xs + 7168); }
  }
  u2v bias[7];
#pragma unroll
  for (int jt = 0; jt < 7; ++jt) bias[jt] = *(const u2v*)(b1 + ct * 112 + 16 * jt + 4 * g);
  wait_cnt(cnt + rt * 64, 16);
  STAMP(kid, 4);
  PROBE_RUN(1);
  {
    u4v av[2][16];
    const int tt0 = 8 * rt + 2 * w;
    const u32 aoff = (c * 4 + g) * 16;
#define AVLD(ks) do { _Pragma("unroll") for (int h_ = 0; h_ < 2; ++h_) av[h_][ks] = bld4s<AUXL>(rs, aoff + (((ks) + ct) & 15) * 1024, DBG_LOFF + (tt0 + h_) * 16384); } while (0)
#pragma unroll
    for (int ks = 0; ks < PA; ++ks) AVLD(ks);
    SB();
    f4 acc[7][2];
#pragma unroll
    for (int jt = 0; jt < 7; ++jt) { acc[jt][0] = f4{0, 0, 0, 0}; acc[jt][1] = f4{0, 0, 0, 0}; }
    const char* lb = smem + l16;
    u4v wf[2][7];
#pragma unroll
    for (int jt = 0; jt < 7; ++jt) wf[0][jt] = *(const u4v*)(lb + jt * 1024);
    SB();
#pragma unroll
    for (int ks = 0; ks < 16; ++ks) {
      const int cu = ks & 1, nx = cu ^ 1;
      if (ks < 15) {
#pragma unroll
        for (int jt = 0; jt < 7; ++jt) wf[nx][jt] = *(const u4v*)(lb + ((ks + 1) * 7 + jt) * 1024);
      }
      if (ks + PA < 16) AVLD(ks + PA);
      if (ks >= 16 - W2E) W2LD(ks - (16 - W2E));
#pragma unroll
      for (int jt = 0; jt < 7; ++jt) {
        bf8v wfb = __builtin_bit_cast(bf8v, wf[cu][jt]);
        acc[jt][0] = __builtin_amdgcn_mfma_f32_16x16x32_bf16(wfb, __builtin_bit_cast(bf8v, av[0][ks]), acc[jt][0], 0, 0, 0);
        acc[jt][1] = __builtin_amdgcn_mfma_f32_16x16x32_bf16(wfb, __builtin_bit_cast(bf8v, av[1][ks]), acc[jt][1], 0, 0, 0);
      }
#pragma unroll
      for (int i = 0; i < 7; ++i) {
        __builtin_amdgcn_sched_group_barrier(0x008, 1, 0);
        if (ks < 15) __builtin_amdgcn_sched_group_barrier(0x100, 1, 0);
        __builtin_amdgcn_sched_group_barrier(0x008, 1, 0);
        if (ks + PA < 16 && (i == 1 || i == 4)) __builtin_amdgcn_sched_group_barrier(0x020, 1, 0);
        if (ks >= 16 - W2E && (i == 0 || i == 2 || i == 3 || i == 5)) __builtin_amdgcn_sched_group_barrier(0x020, 1, 0);
      }
      SB();
    }
    STAMP(kid, 5);
    const int kb = 7 * (ct >> 1) + 3 * (ct & 1);
#pragma unroll
    for (int p = 0; p < 3; ++p) {
      f2 a[8], bs[8];
      u32 o[8];
#pragma unroll
      for (int h = 0; h < 2; ++h) {
        f4 a0 = acc[2 * p][h], a1 = acc[2 * p + 1][h];
        a[4 * h] = f2{a0[0], a0[1]}; a[4 * h + 1] = f2{a0[2], a0[3]}; a[4 * h + 2] = f2{a1[0], a1[1]}; a[4 * h + 3] = f2{a1[2], a1[3]};
        bs[4 * h] = unpk(bias[2 * p].x); bs[4 * h + 1] = unpk(bias[2 * p].y); bs[4 * h + 2] = unpk(bias[2 * p + 1].x); bs[4 * h + 3] = unpk(bias[2 * p + 1].y);
      }
      SB();
      W2LD(W2E + 2 * p); W2LD(W2E + 2 * p + 1);
      SB();
      actN<8>(a, bs, o);
#pragma unroll
      for (int h = 0; h < 2; ++h)
        bst4s<16>(rs, l16 + p * 1024, OFF_H + ((tt0 + h) * 56 + kb) * 1024, u4v{o[4 * h], o[4 * h + 1], o[4 * h + 2], o[4 * h + 3]});
    }
    {
      f4 a0 = acc[6][0], a1 = acc[6][1];
      f2 a[4] = {f2{a0[0], a0[1]}, f2{a0[2], a0[3]}, f2{a1[0], a1[1]}, f2{a1[2], a1[3]}};
      f2 bs[4] = {unpk(bias[6].x), unpk(bias[6].y), unpk(bias[6].x), unpk(bias[6].y)};
      u32 o4[4];
      SB();
      W2LD(13);
      SB();
      actN<4>(a, bs, o4);
      auto rx = __builtin_amdgcn_permlane16_swap(o4[0], o4[2], false, false);
      auto ry = __builtin_amdgcn_permlane16_swap(o4[1], o4[3], false, false);
      u4v o = u4v{rx[0], ry[0], rx[1], ry[1]};
      int tt = tt0 + (g & 1), gp = 2 * (ct & 1) + (g >> 1);
      bst4<16>(rs, OFF_H + ((tt * 56 + 7 * (ct >> 1) + 6) * 64 + gp * 16 + c) * 16, o);
    }
  }
  STAMP(kid, 6);
  wait_vm0();
  bar();
  signal_cnt(cnt + (16 + rt) * 64);
  STAMP(kid, 7);
  if (w != 0) {
    const u32 hs = OFF_H + (8 * rt * 56 + 7 * (ct >> 1) + 3 * (ct & 1) + (w - 1)) * 1024;
#pragma unroll
    for (int i = 0; i < 8; ++i) L2PF(l16, hs + i * 57344);
    const u32 ov = (g & 1) * 57344 + ((g >> 1) * 16 + c) * 16, os = OFF_H + (8 * rt * 56 + 7 * (ct >> 1) + 6) * 1024 + (ct & 1) * 512;
    L2PF(ov, os + (w - 1) * 114688);
    if (w == 1) L2PF(ov, os + 3 * 114688);
  }
  wait_cnt(cnt + (16 + rt) * 64, 16);
  STAMP(kid, 8);
  PROBE_RUN(2);
  R1Tw tw;
  {
    f4 acc[4][4];
#pragma unroll
    for (int i = 0; i < 4; ++i)
#pragma unroll
      for (int h = 0; h < 4; ++h) acc[i][h] = f4{0, 0, 0, 0};
    u32 hoff[4];
#pragma unroll
    for (int h = 0; h < 4; ++h) hoff[h] = DBG_HOFF + ((4 * rt2 + ((w + h) & 3)) * 56 + 14 * w) * 1024;
    constexpr int PD = 4;
    u4v hf[14][4];
#pragma unroll
    for (int kk = 0; kk < PD; ++kk)
#pragma unroll
      for (int h = 0; h < 4; ++h) HFLD(hf[kk][h], bld4s<AUXL>(rs, l16 + KROT(kk) * 1024, hoff[h]));
    const int rho2 = 64 * rt2 + 16 * w + c;
    const u4v x1a = bld4<AUXL>(rs, OFF_X1P + rho2 * 1024 + ct2 * 128 + g * 32), x1b = bld4<AUXL>(rs, OFF_X1P + rho2 * 1024 + ct2 * 128 + g * 32 + 16);
    const u32 x1v[8] = {x1a.x, x1a.y, x1a.z, x1a.w, x1b.x, x1b.y, x1b.z, x1b.w};
    u4v bb0 = *(const u4v*)(b2 + 64 * ct2 + 16 * g), bb1 = *(const u4v*)(b2 + 64 * ct2 + 16 * g + 8);
    SB();
    STAMP(kid, 13);
    SB();
#pragma unroll
    for (int kk = 0; kk < 14; ++kk) {
#pragma unroll
      for (int h = 0; h < 4; ++h)
#pragma unroll
        for (int i = 0; i < 4; ++i)
          acc[i][h] = __builtin_amdgcn_mfma_f32_16x16x32_bf16(__builtin_bit_cast(bf8v, w2f[kk][i]), __builtin_bit_cast(bf8v, hf[kk][h]), acc[i][h], 0, 0, 0);
      if (kk + PD < 14) {
#pragma unroll
        for (int h = 0; h < 4; ++h) HFLD(hf[kk + PD][h], bld4s<AUXL>(rs, l16 + KROT(kk + PD) * 1024, hoff[h]));
      }
      SB();
      if (kk == 3) { STAMP(kid, 14); SB(); }
      if (kk == 8) { STAMP(kid, 15); SB(); }
    }
    STAMP(kid, 9);
    r1_load_tw(tw, t);
    SB();
    f4* red = (f4*)(smem + LDS_W1);
#pragma unroll
    for (int h = 1; h < 4; ++h)
#pragma unroll
      for (int i = 0; i < 4; ++i) red[((((w + h) & 3) * 3 + (h - 1)) * 4 + i) * 64 + lane] = acc[i][h];
    bar();
    f2 s[8];
#pragma unroll
    for (int i = 0; i < 4; ++i) {
      f4 a = (acc[i][0] + red[((w * 3 + 0) * 4 + i) * 64 + lane]) + (red[((w * 3 + 1) * 4 + i) * 64 + lane] + red[((w * 3 + 2) * 4 + i) * 64 + lane]);
      s[2 * i] = f2{a[0], a[1]}; s[2 * i + 1] = f2{a[2], a[3]};
    }
    u32 bs[8] = {bb0.x, bb0.y, bb0.z, bb0.w, bb1.x, bb1.y, bb1.z, bb1.w};
    u32 o[8];
#pragma unroll
    for (int i = 0; i < 8; ++i) o[i] = pack2(unpk(x1v[i]) + rbf2(rbf2(s[i]) + unpk(bs[i])));
    const int srow = 256 * (rho2 & 7) + (rho2 >> 3);
    const u32 xo = (srow * D_ + 64 * ct2 + 16 * g) * 2;
    if (last) {
      *(u4v*)((char*)out + xo) = u4v{o[0], o[1], o[2], o[3]};
      *(u4v*)((char*)out + xo + 16) = u4v{o[4], o[5], o[6], o[7]};
      STAMP(kid, 10);
      return;
    }
    bst4<16>(rs, OFF_XR + xo, u4v{o[0], o[1], o[2], o[3]});
    bst4<16>(rs, OFF_XR + xo + 16, u4v{o[4], o[5], o[6], o[7]});
  }
  STAMP(kid, 10);
  {
    wait_vm0();
    bar();
    signal_cnt(cnt + (32 + rt2) * 64);
    wait_cnt(cnt + (32 + rt2) * 64, 8);
    STAMP(kid, 11);
    u32 xin[8];
    const u32 ro = OFF_XR + ((256 * (t >> 5) + s2) * 256 + (t & 31)) * 4;
#pragma unroll
    for (int m = 0; m < 8; ++m) xin[m] = bld1<AUXL>(rs, ro + 128 * m);
    r1_body(xin, tw, tx, lds, ws, s2, t);
    STAMP(kid, 12);
  }
}

extern "C" void kernel_launch(void* const* d_in, const int* in_sizes, int n_in, void* d_out, int out_size, void* d_ws,
                              size_t ws_size, hipStream_t stream) {
  const unsigned short* x = (const unsigned short*)d_in[0];
  const unsigned short* wr = (const unsigned short*)d_in[1];
  const unsigned short* wi = (const unsigned short*)d_in[2];
  const unsigned short* w1 = (const unsigned short*)d_in[4];
  const unsigned short* b1 = (const unsigned short*)d_in[5];
  const unsigned short* w2 = (const unsigned short*)d_in[6];
  const unsigned short* b2 = (const unsigned short*)d_in[7];
  char* ws = (char*)d_ws;
  (void)hipFuncSetAttribute((const void*)k_m, hipFuncAttributeMaxDynamicSharedMemorySize, LDS_M);
  k_first<<<256, 256, 0, stream>>>((const u32*)x, wr, wi, w1, w2, ws);
  k_c<<<256, 256, 0, stream>>>(ws, 1);
  k_m<<<256, 256, LDS_M, stream>>>((const u32*)x, b1, b2, ws, (unsigned short*)d_out, 0, 2);
  k_c<<<256, 256, 0, stream>>>(ws, 3);
  k_m<<<256, 256, LDS_M, stream>>>((const u32*)(ws + OFF_XR), b1, b2, ws, (unsigned short*)d_out, 1, 4);
}
```

```cpp
#include <hip/hip_runtime.h>
#include <hip/hip_bf16.h>
#include <stdint.h>

typedef unsigned int u32;
typedef float f2 __attribute__((ext_vector_type(2)));
typedef float f4 __attribute__((ext_vector_type(4)));
typedef float f16v __attribute__((ext_vector_type(16)));
typedef u32 u2v __attribute__((ext_vector_type(2)));
typedef u32 u4v __attribute__((ext_vector_type(4)));
typedef __bf16 bf2v __attribute__((ext_vector_type(2)));
typedef __bf16 bf8v __attribute__((ext_vector_type(8)));
typedef short s4v __attribute__((ext_vector_type(4)));
typedef short s8v __attribute__((ext_vector_type(8)));
#define LAS __attribute__((address_space(3)))
#define DI __device__ __forceinline__
#define RLX_AGENT __ATOMIC_RELAXED, __HIP_MEMORY_SCOPE_AGENT

#define STAMP(K, I)

constexpr int S_ = 2048, D_ = 512, F_ = 1792, DF_ = 257;
constexpr u32 TPB = 16384 + 256;

constexpr u32 OFF_CNT = 0;
constexpr u32 OFF_T = 65536;
constexpr u32 SZ_T = 256u * TPB;
constexpr u32 OFF_WT = OFF_T + SZ_T;
constexpr u32 SZ_WT = 256u * 256 * 8 * 4;
constexpr u32 OFF_PQ = OFF_WT + SZ_WT;
constexpr u32 SZ_PQ = 256u * 8 * 16;
constexpr u32 OFF_W2F = OFF_PQ + SZ_PQ;
constexpr u32 SZ_W2F = (u32)F_ * D_ * 2;
constexpr u32 OFF_W1F = OFF_W2F + SZ_W2F;
constexpr u32 OFF_XR = OFF_W1F + SZ_W2F;
constexpr u32 SZ_X = (u32)S_ * D_ * 2;
constexpr u32 OFF_X1P = OFF_XR + SZ_X;
constexpr u32 OFF_LNF = OFF_X1P + SZ_X;
constexpr u32 OFF_H = OFF_LNF + SZ_X;
constexpr u32 SZ_H = (u32)S_ * F_ * 2;
constexpr u32 OFF_LNF2 = OFF_H + SZ_H;
constexpr u32 OFF_H2 = OFF_LNF2 + SZ_X;
constexpr u32 WS_USED = OFF_H2 + SZ_H;

struct cxf { float x, y; };
constexpr double kPi = 3.14159265358979323846264338327950288;
constexpr double c_sin(double x) { double x2 = x * x, t = x, s = x; for (int i = 1; i < 11; ++i) { t *= -x2 / double((2 * i) * (2 * i + 1)); s += t; } return s; }
constexpr double c_cos(double x) { double x2 = x * x, t = 1, s = 1; for (int i = 1; i < 11; ++i) { t *= -x2 / double((2 * i - 1) * (2 * i)); s += t; } return s; }
constexpr cxf tw2048(int i) {
  i &= 2047;
  int qd = i >> 9, r = i & 511;
  double c = 0, s = 0;
  if (r <= 256) { double a = 2 * kPi * r / 2048.0; c = c_cos(a); s = c_sin(a); }
  else { double a = 2 * kPi * (512 - r) / 2048.0; c = c_sin(a); s = c_cos(a); }
  double cc = 0, ss = 0;
  if (qd == 0) { cc = c; ss = s; } else if (qd == 1) { cc = -s; ss = c; } else if (qd == 2) { cc = -c; ss = -s; } else { cc = s; ss = -c; }
  return cxf{(float)cc, (float)(-ss)};
}
template <int R, int C> struct Tab { cxf v[R * C]; };
constexpr Tab<32, 8> mk_r1() { Tab<32, 8> t{}; for (int j = 0; j < 32; ++j) for (int q = 0; q < 8; ++q) t.v[j * 8 + q] = tw2048(8 * j * q); return t; }
constexpr Tab<8, 8> mk_r2() { Tab<8, 8> t{}; for (int j = 0; j < 8; ++j) for (int q = 0; q < 8; ++q) t.v[j * 8 + q] = tw2048(64 * j * q); return t; }
constexpr Tab<256, 1> mk_512() { Tab<256, 1> t{}; for (int c = 0; c < 256; ++c) t.v[c] = tw2048(4 * c + 512); return t; }
constexpr Tab<32, 8> mk_512u() { Tab<32, 8> t{}; for (int u = 0; u < 32; ++u) for (int i = 0; i < 8; ++i) t.v[u * 8 + i] = tw2048(4 * (u + 32 * (i >> 2) + 64 * (i & 3)) + 512); return t; }
constexpr Tab<32, 22> mk_r2all() {
  Tab<32, 22> t{};
  for (int j = 0; j < 32; ++j) {
    cxf v[22] = {};
    for (int i = 0; i < 8; ++i) v[i] = tw2048(4 * (j + 32 * (i >> 2) + 64 * (i & 3)) + 512);
    for (int a = 0; a < 2; ++a) for (int i = 1; i < 4; ++i) v[8 + 3 * a + (i - 1)] = tw2048(64 * ((j >> 3) + 4 * a) * i);
    for (int q = 1; q < 8; ++q) v[13 + q] = tw2048(8 * j * q);
    for (int i = 0; i < 22; ++i) t.v[((i >> 1) * 32 + j) * 2 + (i & 1)] = v[i];
  }
  return t;
}
constexpr Tab<32, 14> mk_r1all() {
  Tab<32, 14> t{};
  for (int j = 0; j < 32; ++j)
    for (int i = 0; i < 14; ++i) t.v[((i >> 1) * 32 + j) * 2 + (i & 1)] = i < 7 ? tw2048(8 * j * (i + 1)) : tw2048(64 * (j & 3) * (i - 6));
  return t;
}
constexpr Tab<256, 8> mk_x() { Tab<256, 8> t{}; for (int s = 0; s < 256; ++s) for (int k = 0; k < 8; ++k) t.v[s * 8 + k] = tw2048(s * k); return t; }
constexpr Tab<64, 4> mk_c1() { Tab<64, 4> t{}; for (int j = 0; j < 64; ++j) for (int q = 0; q < 4; ++q) t.v[j * 4 + q] = tw2048(8 * j * q); return t; }
constexpr double kSc = 6.9053396600248785e-04;
constexpr cxf tw2048s(int i) {
  i &= 2047;
  int qd = i >> 9, r = i & 511;
  double c = 0, s = 0;
  if (r <= 256) { double a = 2 * kPi * r / 2048.0; c = c_cos(a); s = c_sin(a); }
  else { double a = 2 * kPi * (512 - r) / 2048.0; c = c_sin(a); s = c_cos(a); }
  double cc = 0, ss = 0;
  if (qd == 0) { cc = c; ss = s; } else if (qd == 1) { cc = -s; ss = c; } else if (qd == 2) { cc = -c; ss = -s; } else { cc = s; ss = -c; }
  return cxf{(float)(cc * kSc), (float)(-ss * kSc)};
}
constexpr Tab<8, 8> mk_c2() { Tab<8, 8> t{}; for (int j = 0; j < 8; ++j) for (int q = 0; q < 8; ++q) t.v[((q >> 1) * 8 + j) * 2 + (q & 1)] = tw2048s(32 * j * q); return t; }

__device__ const __attribute__((aligned(64))) Tab<32, 14> g_r1all = mk_r1all();
__device__ const __attribute__((aligned(64))) Tab<256, 1> g_tw512 = mk_512();
__device__ const __attribute__((aligned(64))) Tab<32, 22> g_r2all = mk_r2all();
__device__ const __attribute__((aligned(64))) Tab<256, 8> g_twx = mk_x();
__device__ const __attribute__((aligned(64))) Tab<64, 4> g_twc1 = mk_c1();
__device__ const __attribute__((aligned(64))) Tab<8, 8> g_twc2 = mk_c2();

DI float bf_lo(u32 v) { return __uint_as_float(v << 16); }
DI float bf_hi(u32 v) { return __uint_as_float(v & 0xffff0000u); }
DI f2 unpk(u32 v) { return f2{bf_lo(v), bf_hi(v)}; }
DI float rbf(float x) { return (float)(__bf16)x; }
DI u32 pack2(f2 a) { bf2v v = __builtin_convertvector(a, bf2v); return __builtin_bit_cast(u32, v); }
DI f2 rbf2(f2 a) { return unpk(pack2(a)); }
DI f2 swp(f2 a) { return __builtin_shufflevector(a, a, 1, 0); }
DI f2 fma2(f2 a, f2 b, f2 c) { return __builtin_elementwise_fma(a, b, c); }
DI f2 xx(f2 a) { return __builtin_shufflevector(a, a, 0, 0); }
DI f2 yy(f2 a) { return __builtin_shufflevector(a, a, 1, 1); }
DI f2 cmul(f2 a, f2 b) {
  f2 t, r;
  asm("v_pk_mul_f32 %0, %1, %2 op_sel:[1,1] op_sel_hi:[1,0]" : "=v"(t) : "v"(a), "v"(b));
  asm("v_pk_fma_f32 %0, %1, %2, %3 op_sel:[0,0,0] op_sel_hi:[0,1,1] neg_lo:[0,0,1]" : "=v"(r) : "v"(a), "v"(b), "v"(t));
  return r;
}
DI f2 cmulc(f2 a, f2 b) {
  f2 t, r;
  asm("v_pk_mul_f32 %0, %1, %2 op_sel:[1,1] op_sel_hi:[1,0]" : "=v"(t) : "v"(a), "v"(b));
  asm("v_pk_fma_f32 %0, %1, %2, %3 op_sel:[0,0,0] op_sel_hi:[0,1,1] neg_hi:[0,1,0]" : "=v"(r) : "v"(a), "v"(b), "v"(t));
  return r;
}
DI f2 cmul_s(f2 a, f2 b) {
  f2 t, r;
  asm("v_pk_mul_f32 %0, %1, %2 op_sel:[1,1] op_sel_hi:[1,0]" : "=v"(t) : "v"(a), "s"(b));
  asm("v_pk_fma_f32 %0, %1, %2, %3 op_sel:[0,0,0] op_sel_hi:[0,1,1] neg_lo:[0,0,1]" : "=v"(r) : "v"(a), "s"(b), "v"(t));
  return r;
}
DI f2 cmulc_s(f2 a, f2 b) {
  f2 t, r;
  asm("v_pk_mul_f32 %0, %1, %2 op_sel:[1,1] op_sel_hi:[1,0]" : "=v"(t) : "v"(a), "s"(b));
  asm("v_pk_fma_f32 %0, %1, %2, %3 op_sel:[0,0,0] op_sel_hi:[0,1,1] neg_hi:[0,1,0]" : "=v"(r) : "v"(a), "s"(b), "v"(t));
  return r;
}
DI f2 cmul_add(f2 a, f2 b, f2 e) {
  f2 t, r;
  asm("v_pk_fma_f32 %0, %1, %2, %3 op_sel:[1,1,0] op_sel_hi:[1,0,1] neg_lo:[0,1,0]" : "=v"(t) : "v"(a), "v"(b), "v"(e));
  asm("v_pk_fma_f32 %0, %1, %2, %3 op_sel:[0,0,0] op_sel_hi:[0,1,1]" : "=v"(r) : "v"(a), "v"(b), "v"(t));
  return r;
}
DI f2 cmulc_add(f2 a, f2 b, f2 e) {
  f2 t, r;
  asm("v_pk_fma_f32 %0, %1, %2, %3 op_sel:[1,1,0] op_sel_hi:[1,0,1]" : "=v"(t) : "v"(a), "v"(b), "v"(e));
  asm("v_pk_fma_f32 %0, %1, %2, %3 op_sel:[0,0,0] op_sel_hi:[0,1,1] neg_hi:[0,1,0]" : "=v"(r) : "v"(a), "v"(b), "v"(t));
  return r;
}
DI f2 ccmul_add(f2 a, f2 b, f2 e) {
  f2 t, r;
  asm("v_pk_fma_f32 %0, %1, %2, %3 op_sel:[1,1,0] op_sel_hi:[1,0,1] neg_hi:[0,1,0]" : "=v"(t) : "v"(a), "v"(b), "v"(e));
  asm("v_pk_fma_f32 %0, %1, %2, %3 op_sel:[0,0,0] op_sel_hi:[0,1,1]" : "=v"(r) : "v"(a), "v"(b), "v"(t));
  return r;
}
template <bool INV> DI f2 addmi(f2 a, f2 d) { return fma2(swp(d), INV ? f2{-1.f, 1.f} : f2{1.f, -1.f}, a); }
template <bool INV> DI f2 submi(f2 a, f2 d) { return fma2(swp(d), INV ? f2{1.f, -1.f} : f2{-1.f, 1.f}, a); }

template <bool INV> DI void fft4(f2& b0, f2& b1, f2& b2, f2& b3) {
  f2 c0 = b0 + b2, c2 = b0 - b2, c1 = b1 + b3, d = b1 - b3;
  b0 = c0 + c1; b2 = c0 - c1; b1 = addmi<INV>(c2, d); b3 = submi<INV>(c2, d);
}
template <bool INV> DI void fft8(f2* v) {
  const float h = 0.70710678118654752440f;
  f2 a0 = v[0] + v[4], a4 = v[0] - v[4];
  f2 a1 = v[1] + v[5], a5 = v[1] - v[5];
  f2 a2 = v[2] + v[6], a6 = v[2] - v[6];
  f2 a3 = v[3] + v[7], a7 = v[3] - v[7];
  f2 c0 = addmi<INV>(a4, a6), c2 = submi<INV>(a4, a6);
  f2 u = addmi<INV>(a5, a7), w = submi<INV>(a5, a7);
  f2 tu = addmi<INV>(u, u), tw = addmi<INV>(w, w);
  fft4<INV>(a0, a1, a2, a3);
  v[0] = a0; v[2] = a1; v[4] = a2; v[6] = a3;
  v[1] = fma2(tu, f2{h, h}, c0);
  v[5] = fma2(tu, f2{-h, -h}, c0);
  v[3] = fma2(swp(tw), INV ? f2{-h, h} : f2{h, -h}, c2);
  v[7] = fma2(swp(tw), INV ? f2{h, -h} : f2{-h, h}, c2);
}
template <int CTRL> DI float dpp_add(float x) {
  return x + __int_as_float(__builtin_amdgcn_update_dpp(0, __float_as_int(x), CTRL, 0xf, 0xf, false));
}
DI float red32(float x) {
  x = dpp_add<0xB1>(x); x = dpp_add<0x4E>(x); x = dpp_add<0x141>(x); x = dpp_add<0x140>(x);
  auto r = __builtin_amdgcn_permlane16_swap(__float_as_uint(x), __float_as_uint(x), false, false);
  return __uint_as_float(r[0]) + __uint_as_float(r[1]);
}
DI void wbar() { __builtin_amdgcn_wave_barrier(); }
DI void bar() {
  asm volatile("s_waitcnt lgkmcnt(0)" ::: "memory");
  __builtin_amdgcn_s_barrier();
  asm volatile("" ::: "memory");
}
DI void wait_vm0() { __builtin_amdgcn_s_waitcnt(0x0F70); asm volatile("" ::: "memory"); }
DI void layernorm8(const f2* v, u32* o) {
  f2 s2 = ((v[0] + v[1]) + (v[2] + v[3])) + ((v[4] + v[5]) + (v[6] + v[7]));
  float mean = red32(s2.x + s2.y) * (1.0f / 512.0f);
  f2 mm = f2{mean, mean};
  f2 q0 = f2{0, 0}, q1 = f2{0, 0};
#pragma unroll
  for (int i = 0; i < 8; i += 2) { f2 d0 = v[i] - mm, d1 = v[i + 1] - mm; q0 = fma2(d0, d0, q0); q1 = fma2(d1, d1, q1); }
  q0 = q0 + q1;
  float q = red32(q0.x + q0.y);
  float m = rbf(mean), vr = rbf(q * (1.0f / 512.0f));
  float sd = rbf(__builtin_amdgcn_sqrtf(rbf(vr + 1e-5f)));
  float ri = __builtin_amdgcn_rcpf(sd);
  f2 m2 = f2{m, m}, r2 = f2{ri, ri};
#pragma unroll
  for (int i = 0; i < 8; ++i) o[i] = pack2(rbf2(v[i] - m2) * r2);
}
DI f2 ldtw(const cxf* p) { return *(const f2*)p; }
typedef __amdgpu_buffer_rsrc_t rsrc_t;
DI rsrc_t mk_rsrc(void* p, u32 bytes) { return __builtin_amdgcn_make_buffer_rsrc(p, 0, bytes, 0x00020000); }
template <int AUX> DI u4v bld4(rsrc_t r, u32 off) { return __builtin_amdgcn_raw_buffer_load_b128(r, off, 0, AUX); }
template <int AUX> DI u32 bld1(rsrc_t r, u32 off) { return __builtin_amdgcn_raw_buffer_load_b32(r, off, 0, AUX); }
template <int AUX> DI void bst4(rsrc_t r, u32 off, u4v v) { __builtin_amdgcn_raw_buffer_store_b128(v, r, off, 0, AUX); }
DI u4v f4u(f2 a, f2 b) { return u4v{__float_as_uint(a.x), __float_as_uint(a.y), __float_as_uint(b.x), __float_as_uint(b.y)}; }

constexpr int RP = 288;

constexpr u32 SENT = 0xFFFFFFFFu;
DI u32 nosent(u32 v) { return v < SENT - 1 ? v : SENT - 1; }
DI u32 mx2(u32 a, u32 b) { return a > b ? a : b; }
DI u32 mx4(u32 m, u4v a) { return mx2(mx2(mx2(mx2(m, a.x), a.y), a.z), a.w); }
DI bool anysent(u32 m) { return __builtin_amdgcn_ballot_w64(m == SENT) != 0; }
struct R1Tw { f2 tw1[8], tw2[8], w512; };
DI void r1_load_tw(R1Tw& tw, int t) {
  const f4* p = (const f4*)&g_r1all + (t & 31);
  f2 v[14];
#pragma unroll
  for (int i = 0; i < 7; ++i) { f4 q = p[32 * i]; v[2 * i] = f2{q[0], q[1]}; v[2 * i + 1] = f2{q[2], q[3]}; }
#pragma unroll
  for (int q = 1; q < 8; ++q) { tw.tw1[q] = v[q - 1]; tw.tw2[q] = v[6 + q]; }
  tw.w512 = ldtw(&g_tw512.v[t]);
}
DI void r1_body(const u32* xin, const R1Tw& tw, const f16v& tx, f2* lds, char* ws, int s2, int t) {
  const int s1 = t >> 5, j = t & 31;
  f2 z[8];
#pragma unroll
  for (int m = 0; m < 8; ++m) z[m] = unpk(xin[m]);
  u32 o[8];
  layernorm8(z, o);
#pragma unroll
  for (int m = 0; m < 8; ++m) z[m] = unpk(o[m]);
  f2* L = lds + s1 * RP;
  fft8<false>(z);
#pragma unroll
  for (int q = 1; q < 8; ++q) z[q] = cmul(z[q], tw.tw1[q]);
#pragma unroll
  for (int q = 0; q < 8; ++q) L[q * 36 + j] = z[q];
  wbar();
#pragma unroll
  for (int m = 0; m < 8; ++m) z[m] = L[(j >> 2) * 36 + (j & 3) + 4 * m];
  wbar();
  fft8<false>(z);
#pragma unroll
  for (int q = 1; q < 8; ++q) z[q] = cmul(z[q], tw.tw2[q]);
#pragma unroll
  for (int q = 0; q < 8; ++q) L[(j & 3) * 68 + 8 * q + (j >> 2)] = z[q];
  wbar();
#pragma unroll
  for (int i = 0; i < 8; ++i) z[i] = L[(i & 3) * 68 + 32 * (i >> 2) + j];
  wbar();
  fft4<false>(z[0], z[1], z[2], z[3]);
  fft4<false>(z[4], z[5], z[6], z[7]);
#pragma unroll
  for (int i = 0; i < 8; ++i) L[j + 32 * (i >> 2) + 64 * (i & 3)] = z[i];
  bar();
  f2 X[8];
#pragma unroll
  for (int r = 0; r < 8; ++r) {
    f2 A = lds[r * RP + t], Bc = lds[r * RP + ((256 - t) & 255)];
    f2 E = fma2(Bc, f2{1.f, -1.f}, A);
    f2 Dm = fma2(Bc, f2{-1.f, 1.f}, A);
    f2 x2 = cmul_add(Dm, tw.w512, E);
    if (t == 0) x2.y = E.x - Dm.y;
    X[r] = x2;
  }
  fft8<false>(X);
#pragma unroll
  for (int k = 1; k < 8; ++k) X[k] = cmul_s(X[k], f2{tx[2 * k], tx[2 * k + 1]});
  u4v* dst = (u4v*)(ws + OFF_T + (size_t)s2 * TPB) + t;
  dst[0] = f4u(X[0], X[4]);
  dst[256] = f4u(X[1], X[7]);
  dst[512] = f4u(X[2], X[6]);
  dst[768] = f4u(X[3], X[5]);
}

DI int f_of(int kap, int g, int j) {
  int u = kap / 7, r = kap % 7;
  if (r < 6) { int ct = 2 * u + (r >= 3), p = r % 3; return 112 * ct + 32 * p + (j < 4 ? 4 * g + j : 16 + 4 * g + (j - 4)); }
  return 112 * (2 * u + (g >> 1)) + 96 + 8 * (g & 1) + j;
}
constexpr int WPIT = 528;
#define ARM_A(PARTS) do { if ((PARTS) && first) { \
    const u4v sv = u4v{SENT, SENT, SENT, SENT}; \
    const int xcd_ = b & 7, ii_ = b >> 3, w_ = __builtin_amdgcn_readfirstlane(t >> 6), ln_ = t & 63, g_ = ln_ >> 4; \
    const int s2_ = 32 * xcd_ + ii_, rt_ = 2 * xcd_ + (ii_ >> 4), ct_ = ii_ & 15, rt2_ = 4 * xcd_ + (ii_ >> 3), ct2_ = ii_ & 7; \
    if ((PARTS) & 1) { \
      const u32 co = OFF_LNF + ((s2_ >> 1) * 16 + 4 * w_) * 1024 + (ln_ >> 3) * 256 + (s2_ & 1) * 128 + (ln_ & 7) * 16; \
      bst4<CAUX>(rs, co, sv); bst4<CAUX>(rs, co + 2048, sv); \
      const u32 xo = OFF_XR + ((256 * (ln_ >> 3) + 8 * rt2_ + 2 * w_) * D_ + 64 * ct2_) * 2 + (ln_ & 7) * 16; \
      bst4<CAUX>(rs, xo, sv); bst4<CAUX>(rs, xo + 1024, sv); } \
    if ((PARTS) & 2) { \
      const int tt0_ = 8 * rt_ + 2 * w_, kb_ = 7 * (ct_ >> 1) + 3 * (ct_ & 1); \
      _Pragma("unroll") for (int h = 0; h < 2; ++h) _Pragma("unroll") for (int p = 0; p < 3; ++p) \
        bst4<CAUX>(rs, OFF_H + ((tt0_ + h) * 56 + kb_ + p) * 1024 + ln_ * 16, sv); \
      bst4<CAUX>(rs, OFF_H + (((tt0_ + (g_ & 1)) * 56 + 7 * (ct_ >> 1) + 6) * 64 + (2 * (ct_ & 1) + (g_ >> 1)) * 16 + (ln_ & 15)) * 16, sv); } \
    __builtin_amdgcn_sched_barrier(0); } } while (0)
#define CAUX 0
#define KF_ARM 3
#define KC_ARM (3 - (KF_ARM))
__global__ __launch_bounds__(256) void k_first(const u32* __restrict__ x, const unsigned short* __restrict__ wr,
                                               const unsigned short* __restrict__ wi, const unsigned short* __restrict__ w1,
                                               const unsigned short* __restrict__ w2, char* __restrict__ ws) {
  __shared__ __attribute__((aligned(16))) char smem[8 * RP * 8 + 32 * WPIT];
  const int b = blockIdx.x, t = threadIdx.x, w = __builtin_amdgcn_readfirstlane(t >> 6), lane = t & 63;
  STAMP(0, 0);
  const int s2 = 32 * (b & 7) + (b >> 3);
  u32 xin[8];
  const u32* xr = x + (size_t)(256 * (t >> 5) + s2) * 256 + (t & 31);
#pragma unroll
  for (int m = 0; m < 8; ++m) xin[m] = xr[32 * m];
  R1Tw tw;
  r1_load_tw(tw, t);
  const f16v tx = *(const f16v*)&g_twx.v[s2 * 8];
  unsigned short fr[8], fi[8];
#pragma unroll
  for (int i = 0; i < 8; ++i) { fr[i] = wr[(b + 256 * i) * DF_ + t]; fi[i] = wi[(b + 256 * i) * DF_ + t]; }
  u4v wu[4];
  const int isw2 = b >= 112;
  const int ua = isw2 ? (b - 112) >> 1 : b / 7, ub = isw2 ? (b - 112) & 1 : b % 7;
  if (b < 224) {
#pragma unroll
    for (int g = 0; g < 4; ++g) {
      const int i = t >> 5;
      const unsigned short* p;
      if (isw2) p = w2 + (size_t)f_of(ua, g, i) * D_;
      else p = w1 + (size_t)(256 * (g & 1) + 64 * (i >> 1) + 4 * ua + 2 * (g >> 1) + (i & 1)) * F_;
      wu[g] = *(const u4v*)(p + 256 * ub + (t & 31) * 8);
    }
  } else {
#pragma unroll
    for (int g = 0; g < 4; ++g) asm volatile("" : "=v"(wu[g]));
  }
  unsigned short pv[8] = {};
  if (w == 0) {
    const int k = b + 256 * (t & 7), km = (2048 - k) & 2047;
    pv[0] = wr[k * DF_]; pv[1] = wi[k * DF_]; pv[2] = wr[km * DF_]; pv[3] = wi[km * DF_];
    pv[4] = wr[k * DF_ + 256]; pv[5] = wi[k * DF_ + 256]; pv[6] = wr[km * DF_ + 256]; pv[7] = wi[km * DF_ + 256];
  }
  __builtin_amdgcn_sched_barrier(0);
  {
    const rsrc_t rs = mk_rsrc(ws, WS_USED);
    const bool first = true;
    ARM_A(KF_ARM);
  }
  if (b < 224) {
    char* im = smem + 8 * RP * 8;
#pragma unroll
    for (int g = 0; g < 4; ++g) *(u4v*)(im + (8 * g + (t >> 5)) * WPIT + (t & 31) * 16) = wu[g];
    bar();
    const int g = lane >> 4, q = (lane >> 2) & 3, p = lane & 3;
    const char* a0 = im + (8 * g + q) * WPIT + (isw2 ? 32 * p : 8 * p) + 128 * w;
#pragma unroll
    for (int n = 0; n < 4; ++n) {
      const char* a = a0 + (isw2 ? 8 * n : 32 * n);
      s4v lo = __builtin_amdgcn_ds_read_tr16_b64_v4i16((LAS s4v*)a);
      s4v hi = __builtin_amdgcn_ds_read_tr16_b64_v4i16((LAS s4v*)(a + 4 * WPIT));
      s8v wf = __builtin_shufflevector(lo, hi, 0, 1, 2, 3, 4, 5, 6, 7);
      u32 off;
      if (isw2) off = OFF_W2F + (((4 * ub + w) * 56 + ua) * 4 + n) * 1024;
      else { int ft = 16 * ub + 4 * w + n; off = OFF_W1F + (((ft / 7) * 16 + ua) * 7 + ft % 7) * 1024; }
      *(u4v*)(ws + off + lane * 16) = __builtin_bit_cast(u4v, wf);
    }
  }
  {
    const int k1 = b & 7, kp = k1 <= 4 ? (k1 & 3) : 8 - k1, e = k1 >= 4;
    const int tl = e * 32 + ((b >> 3) & 3) * 8 + (b >> 5);
    u32 o[8];
#pragma unroll
    for (int i = 0; i < 8; ++i) o[i] = (u32)fr[i] | ((u32)fi[i] << 16);
    u4v* dst = (u4v*)(ws + OFF_WT) + (size_t)(kp * 64 + (t >> 2)) * 512 + (t & 3) * 64 + tl;
    dst[0] = u4v{o[0], o[1], o[2], o[3]};
    dst[256] = u4v{o[4], o[5], o[6], o[7]};
    auto cv = [](unsigned short v) { return __uint_as_float((u32)v << 16); };
    float h0r = 0.5f * (cv(pv[0]) + cv(pv[2])), h0i = 0.5f * (cv(pv[1]) - cv(pv[3]));
    float h1r = 0.5f * (cv(pv[4]) + cv(pv[6])), h1i = 0.5f * (cv(pv[5]) - cv(pv[7]));
    if (t < 8) ((f4*)(ws + OFF_PQ))[(kp * 64 + tl) * 8 + t] = f4{0.5f * (h0r + h1r), 0.5f * (h0i + h1i), 0.5f * (h0r - h1r), 0.5f * (h0i - h1i)};
  }
  STAMP(0, 1);
  __builtin_amdgcn_sched_barrier(0);
  r1_body(xin, tw, tx, (f2*)smem, ws, s2, t);
  STAMP(0, 2);
}

constexpr int PK = 292, QS = 72;
DI int idx1(int k1, int q, int j) { return k1 * PK + q * QS + (j ^ ((k1 >> 2) << 2)); }
template <int AUX> DI u4v bld4s(rsrc_t r, u32 off, u32 soff) { return __builtin_amdgcn_raw_buffer_load_b128(r, off, soff, AUX); }
template <int AUX> DI void bst4s(rsrc_t r, u32 off, u32 soff, u4v v) { __builtin_amdgcn_raw_buffer_store_b128(v, r, off + soff, 0, AUX); }
__global__ __launch_bounds__(256) void k_c(char* __restrict__ ws, int first, int kid) {
  __shared__ f2 lds[8 * PK];
  STAMP(kid, 0);
  const rsrc_t rs = mk_rsrc(ws, WS_USED);
  const int b = blockIdx.x, qd = 8 * (b & 7) + ((b >> 3) & 7), kp = b >> 6;
  const int t = threadIdx.x, cc = t & 3, j = t >> 2;
  u4v in[4];
  const u32 tv = j * TPB + cc * 16, ts = OFF_T + kp * 4096 + qd * 64;
#pragma unroll
  for (int m = 0; m < 4; ++m) in[m] = bld4s<0>(rs, tv, ts + m * 64 * TPB);
  const u32 wsof = OFF_WT + (kp * 64 + qd) * 8192;
  u4v w0 = bld4s<0>(rs, t * 16, wsof), w1 = bld4s<0>(rs, t * 16 + 4096, wsof);
  f2 tc1[4], tc2[8];
#pragma unroll
  for (int q = 1; q < 4; ++q) tc1[q] = ldtw(&g_twc1.v[j * 4 + q]);
#pragma unroll
  for (int q = 0; q < 4; ++q) { f4 p = ((const f4*)&g_twc2)[8 * q + (t & 7)]; tc2[2 * q] = f2{p[0], p[1]}; tc2[2 * q + 1] = f2{p[2], p[3]}; }
  const bool special = qd == 0 && __builtin_amdgcn_readfirstlane(t >> 6) == 0;
  u4v pq[8];
  if (special) {
#pragma unroll
    for (int i = 0; i < 8; ++i) pq[i] = bld4s<0>(rs, t * 128 + i * 16, OFF_PQ + kp * 8192);
  } else {
#pragma unroll
    for (int i = 0; i < 8; ++i) asm volatile("" : "=v"(pq[i]));
  }
  ARM_A(KC_ARM);
#pragma unroll
  for (int e = 0; e < 2; ++e) {
    f2 a[4];
#pragma unroll
    for (int m = 0; m < 4; ++m) a[m] = e ? f2{__uint_as_float(in[m].z), __uint_as_float(in[m].w)} : f2{__uint_as_float(in[m].x), __uint_as_float(in[m].y)};
    fft4<false>(a[0], a[1], a[2], a[3]);
#pragma unroll
    for (int q = 1; q < 4; ++q) a[q] = cmul(a[q], tc1[q]);
#pragma unroll
    for (int q = 0; q < 4; ++q) lds[idx1(2 * cc + e, q, j)] = a[q];
  }
  bar();
  const int k1 = t >> 5, q = (t >> 3) & 3, jp = t & 7;
  const float sc = (float)kSc;
  f2 v[8];
#pragma unroll
  for (int m = 0; m < 8; ++m) v[m] = lds[idx1(k1, q, jp + 8 * m)];
  wbar();
  fft8<false>(v);
  v[0] = v[0] * f2{sc, sc};
#pragma unroll
  for (int i = 1; i < 8; ++i) v[i] = cmul(v[i], tc2[i]);
  f2* L2 = lds + k1 * PK + q * QS;
#pragma unroll
  for (int i = 0; i < 8; ++i) L2[jp * 9 + i] = v[i];
  wbar();
#pragma unroll
  for (int i = 0; i < 8; ++i) v[i] = L2[i * 9 + jp];
  wbar();
  fft8<false>(v);
  if (!special) {
    u32 w[8] = {w0.x, w0.y, w0.z, w0.w, w1.x, w1.y, w1.z, w1.w};
#pragma unroll
    for (int i = 0; i < 8; ++i) v[i] = cmul(v[i], unpk(w[i]));
  } else {
#pragma unroll
    for (int i = 0; i < 8; ++i) lds[k1 * PK + i * 32 + (t & 31)] = v[i];
    wbar();
    const bool z0 = kp == 0 && k1 == 0;
    const int u = q + 4 * jp, up = z0 ? (32 - u) & 31 : 31 - u;
    const int zi = (z0 && u == 0) ? 8 : 7;
    const f2* src = lds + (kp == 0 ? k1 : k1 ^ 1) * PK + (up & 3) * 8 + (up >> 2);
#pragma unroll
    for (int i = 0; i < 8; ++i) {
      f2 zm = src[((zi - i) & 7) * 32];
      f2 P = f2{__uint_as_float(pq[i].x), __uint_as_float(pq[i].y)}, Q = f2{__uint_as_float(pq[i].z), __uint_as_float(pq[i].w)};
      v[i] = ccmul_add(zm, Q, cmul(v[i], P));
    }
    wbar();
  }
  fft8<true>(v);
  v[0] = v[0] * f2{sc, sc};
#pragma unroll
  for (int i = 1; i < 8; ++i) v[i] = cmulc(v[i], tc2[i]);
#pragma unroll
  for (int i = 0; i < 8; ++i) L2[i * 9 + jp] = v[i];
  wbar();
#pragma unroll
  for (int i = 0; i < 8; ++i) v[i] = L2[jp * 9 + i];
  wbar();
  fft8<true>(v);
#pragma unroll
  for (int m = 0; m < 8; ++m) lds[idx1(k1, q, jp + 8 * m)] = v[m];
  bar();
  f2 o[2][4];
#pragma unroll
  for (int e = 0; e < 2; ++e) {
#pragma unroll
    for (int qq = 0; qq < 4; ++qq) o[e][qq] = lds[idx1(2 * cc + e, qq, j)];
#pragma unroll
    for (int qq = 1; qq < 4; ++qq) o[e][qq] = cmulc(o[e][qq], tc1[qq]);
    fft4<true>(o[e][0], o[e][1], o[e][2], o[e][3]);
  }
#pragma unroll
  for (int m = 0; m < 4; ++m) bst4s<0>(rs, tv, ts + m * 64 * TPB, f4u(o[0][m], o[1][m]));
  STAMP(kid, 1);
}

#define SB() __builtin_amdgcn_sched_barrier(0)
template <int N> DI void actN(const f2* a, const f2* bias, u32* o) {
  const float c0 = -2.0f * 0.7978845608028654f * 1.4426950408889634f, c1 = c0 * 0.044715f;
  f2 x[N], u[N];
#pragma unroll
  for (int i = 0; i < N; ++i) x[i] = rbf2(a[i]);
  SB();
#pragma unroll
  for (int i = 0; i < N; ++i) x[i] = x[i] + bias[i];
  SB();
#pragma unroll
  for (int i = 0; i < N; ++i) x[i] = rbf2(x[i]);
  SB();
#pragma unroll
  for (int i = 0; i < N; ++i) u[i] = x[i] * x[i];
  SB();
#pragma unroll
  for (int i = 0; i < N; ++i) u[i] = fma2(u[i], f2{c1, c1}, f2{c0, c0});
  SB();
#pragma unroll
  for (int i = 0; i < N; ++i) u[i] = u[i] * x[i];
  SB();
#pragma unroll
  for (int i = 0; i < N; ++i) u[i] = f2{__builtin_amdgcn_exp2f(u[i].x), __builtin_amdgcn_exp2f(u[i].y)};
  SB();
#pragma unroll
  for (int i = 0; i < N; ++i) u[i] = u[i] + f2{1.f, 1.f};
  SB();
#pragma unroll
  for (int i = 0; i < N; ++i) u[i] = f2{__builtin_amdgcn_rcpf(u[i].x), __builtin_amdgcn_rcpf(u[i].y)};
  SB();
#pragma unroll
  for (int i = 0; i < N; ++i) u[i] = u[i] * x[i];
  SB();
#pragma unroll
  for (int i = 0; i < N; ++i) o[i] = pack2(u[i]);
  SB();
#pragma unroll
  for (int i = 0; i < N; ++i) o[i] = nosent(o[i]);
  SB();
}

#define ALD4(dst, voff, sbase, imm) asm volatile("global_load_dwordx4 %0, %1, %2 offset:" #imm : "=v"(dst) : "v"(voff), "s"(sbase))
#define ALD1(dst, voff, sbase, imm) asm volatile("global_load_dword %0, %1, %2 offset:" #imm : "=v"(dst) : "v"(voff), "s"(sbase))
#define TSTEP(i)
#define NSLOW()
#define MEMF() asm volatile("" ::: "memory")
#define MX4F(m, a) do { asm volatile("v_max_u32 %0, %1, %2" : "=v"(m) : "v"((a).x), "v"((a).y)); \
    asm volatile("v_max3_u32 %0, %0, %1, %2" : "+v"(m) : "v"((a).z), "v"((a).w)); } while (0)
#define MX4(m, a) do { asm volatile("v_max3_u32 %0, %0, %1, %2" : "+v"(m) : "v"((a).x), "v"((a).y)); \
    asm volatile("v_max3_u32 %0, %0, %1, %2" : "+v"(m) : "v"((a).z), "v"((a).w)); } while (0)
constexpr int LDS_W1 = 114688;
constexpr int LDS_M = LDS_W1 + 49152;
constexpr int W2E = 7;
constexpr int PA = 6;
#define HPOS 3
#define MAUX 0
#define FAUX 0
#define PD 4

__global__ __launch_bounds__(256, 1) void k_m(const u32* __restrict__ xres, const unsigned short* __restrict__ b1,
                                              const unsigned short* __restrict__ b2, char* ws, unsigned short* out, int last, int kid) {
  extern __shared__ __attribute__((aligned(16))) char smem[];
  STAMP(kid, 0);
  const int b = blockIdx.x, xcd = b & 7, ii = b >> 3;
  const int t = threadIdx.x, w = __builtin_amdgcn_readfirstlane(t >> 6), lane = t & 63, g = lane >> 4, c = lane & 15;
  const int s2 = 32 * xcd + ii, rt = 2 * xcd + (ii >> 4), ct = ii & 15, rt2 = 4 * xcd + (ii >> 3), ct2 = ii & 7;
  const rsrc_t rs = mk_rsrc(ws, WS_USED);
  f2* lds = (f2*)(smem + LDS_W1);
  u4v w2f[14][4];
  f16v tx;
  int budget = 1 << 16;
  const u32 l16 = lane * 16;
  const int tt0 = 8 * rt + 2 * w, kb = 7 * (ct >> 1) + 3 * (ct & 1);
  const int rho2 = 64 * rt2 + 16 * w + c;
  const u32 ol = last ? OFF_LNF2 : OFF_LNF, oh = last ? OFF_H2 : OFF_H;
  const u32 cl = last ? 0x7f000000u : OFF_LNF2, ch = last ? 0x7f000000u : OFF_H2;
  const u32 lno = ((s2 >> 1) * 16 + 4 * w) * 1024 + (lane >> 3) * 256 + (s2 & 1) * 128 + (lane & 7) * 16;
  const u32 hst = (tt0 * 56 + kb) * 1024 + l16;
  const u32 htl = (((tt0 + (g & 1)) * 56 + 7 * (ct >> 1) + 6) * 64 + (2 * (ct & 1) + (g >> 1)) * 16 + c) * 16;
  const int rot = ct2 & 3;
  int kq[4];
#pragma unroll
  for (int q = 0; q < 4; ++q) { const int pr = (q + rot) & 3; kq[q] = 14 * w + 7 * (pr >> 1) + 3 * (pr & 1); }
  const int kt0 = 14 * w + 6 + 7 * (ct2 & 1), kt1 = 14 * w + 13 - 7 * (ct2 & 1);
#define KB(n) ((n) < 12 ? kq[(n) & 3] : ((n) == 12 ? kt0 : kt1))
#define KI(n) ((n) < 12 ? (n) >> 2 : 0)
  const u32 w2s = OFF_W2F + ct2 * 56 * 4096;
#define W2LD1(n, i) w2f[n][i] = bld4s<0>(rs, l16 + (i) * 1024, w2s + (KB(n) + KI(n)) * 4096)
#define W2LD(n) do { _Pragma("unroll") for (int i_ = 0; i_ < 4; ++i_) W2LD1(n, i_); } while (0)
  u2v bias[7];
  {
    const int s1 = t >> 5, j = t & 31;
    u4v in[4], tq[11];
    u32 xin[8];
    {
      const char* tb = ws + OFF_T + (size_t)s2 * TPB;
      const u32 t16 = t * 16;
      ALD4(in[0], t16, tb, 0);
      ALD4(in[1], t16, tb + 4096, 0);
      ALD4(in[2], t16, tb + 8192, 0);
      ALD4(in[3], t16, tb + 12288, 0);
      const u32 jo = j * 16;
      const char* tp = (const char*)&g_r2all;
      ALD4(tq[0], jo, tp, 0); ALD4(tq[1], jo, tp, 512); ALD4(tq[2], jo, tp, 1024); ALD4(tq[3], jo, tp, 1536);
      ALD4(tq[4], jo, tp, 2048); ALD4(tq[5], jo, tp, 2560); ALD4(tq[6], jo, tp, 3072); ALD4(tq[7], jo, tp, 3584);
      ALD4(tq[8], jo, tp + 4096, 0); ALD4(tq[9], jo, tp + 4096, 512); ALD4(tq[10], jo, tp + 4096, 1024);
      const u32 xof = s1 * (256 * 1024) + j * 4;
      const u32* xb = xres + s2 * 256;
      ALD1(xin[0], xof, xb, 0); ALD1(xin[1], xof, xb, 128); ALD1(xin[2], xof, xb, 256); ALD1(xin[3], xof, xb, 384);
      ALD1(xin[4], xof, xb, 512); ALD1(xin[5], xof, xb, 640); ALD1(xin[6], xof, xb, 768); ALD1(xin[7], xof, xb, 896);
    }
    SB();
    tx = *(const f16v*)&g_twx.v[s2 * 8];
    SB();
    const char* gsrc = ws + OFF_W1F + (size_t)ct * LDS_W1 + l16;
#define W1DMA(pa, pb) do { SB(); _Pragma("unroll") for (int p_ = (pa); p_ < (pb); ++p_) { \
      const int sl_ = p_ / 7, pc_ = p_ % 7; \
      const char* gp_ = gsrc + ((4 * w + sl_ + ct) & 15) * 7168 + (pc_ & 4) * 1024; \
      char* lp_ = smem + (4 * w + sl_) * 7168 + (pc_ & 4) * 1024; \
      if ((pc_ & 3) == 0) __builtin_amdgcn_global_load_lds((const void*)gp_, (LAS void*)lp_, 16, 0, 0); \
      if ((pc_ & 3) == 1) __builtin_amdgcn_global_load_lds((const void*)gp_, (LAS void*)lp_, 16, 1024, 0); \
      if ((pc_ & 3) == 2) __builtin_amdgcn_global_load_lds((const void*)gp_, (LAS void*)lp_, 16, 2048, 0); \
      if ((pc_ & 3) == 3) __builtin_amdgcn_global_load_lds((const void*)gp_, (LAS void*)lp_, 16, 3072, 0); } SB(); } while (0)
    W1DMA(0, 4);
    {
      const u4v sv = u4v{SENT, SENT, SENT, SENT};
#pragma unroll
      for (int q = 0; q < 6; ++q) bst4<MAUX>(rs, ch + hst + (q & 1) * 57344 + (q >> 1) * 1024, sv);
      bst4<MAUX>(rs, ch + htl, sv);
      bst4<MAUX>(rs, cl + lno, sv);
      bst4<MAUX>(rs, cl + lno + 2048, sv);
      SB();
    }
    asm volatile("s_waitcnt vmcnt(32)" : "+v"(in[0]), "+v"(in[1]), "+v"(in[2]), "+v"(in[3]));
    STAMP(kid, 1);
    f2 X[8];
    {
      auto lo = [](u4v v) { return f2{__uint_as_float(v.x), __uint_as_float(v.y)}; };
      auto hi = [](u4v v) { return f2{__uint_as_float(v.z), __uint_as_float(v.w)}; };
      X[0] = lo(in[0]); X[4] = hi(in[0]); X[1] = lo(in[1]); X[7] = hi(in[1]);
      X[2] = lo(in[2]); X[6] = hi(in[2]); X[3] = lo(in[3]); X[5] = hi(in[3]);
    }
#pragma unroll
    for (int k = 1; k < 8; ++k) X[k] = cmulc_s(X[k], f2{tx[2 * k], tx[2 * k + 1]});
    fft8<true>(X);
#pragma unroll
    for (int r = 0; r < 8; ++r) lds[r * RP + t] = X[r];
    W1DMA(4, 8);
    asm volatile("s_waitcnt vmcnt(17)" : "+v"(tq[0]), "+v"(tq[1]), "+v"(tq[2]), "+v"(tq[3]), "+v"(tq[4]), "+v"(tq[5]), "+v"(tq[6]), "+v"(tq[7]),
                 "+v"(tq[8]), "+v"(tq[9]), "+v"(tq[10]), "+v"(xin[0]), "+v"(xin[1]), "+v"(xin[2]), "+v"(xin[3]), "+v"(xin[4]), "+v"(xin[5]),
                 "+v"(xin[6]), "+v"(xin[7]));
    f2 tw1[8], twu[8], tw2[2][4];
    {
      f2 tv[22];
#pragma unroll
      for (int i = 0; i < 11; ++i) {
        tv[2 * i] = f2{__uint_as_float(tq[i].x), __uint_as_float(tq[i].y)};
        tv[2 * i + 1] = f2{__uint_as_float(tq[i].z), __uint_as_float(tq[i].w)};
      }
#pragma unroll
      for (int i = 0; i < 8; ++i) twu[i] = tv[i];
#pragma unroll
      for (int a = 0; a < 2; ++a)
#pragma unroll
        for (int i = 1; i < 4; ++i) tw2[a][i] = tv[8 + 3 * a + (i - 1)];
#pragma unroll
      for (int q = 1; q < 8; ++q) tw1[q] = tv[13 + q];
    }
    bar();
    f2* L = lds + s1 * RP;
    f2 z[8];
#pragma unroll
    for (int i = 0; i < 8; ++i) {
      int k = j + 32 * (i >> 2) + 64 * (i & 3);
      f2 A = L[k], Bc = L[(256 - k) & 255];
      f2 E = fma2(Bc, f2{1.f, -1.f}, A);
      f2 Dm = fma2(Bc, f2{-1.f, 1.f}, A);
      f2 zz = cmulc_add(Dm, twu[i], E);
      if (i == 0 && j == 0) zz = f2{A.x + A.y, A.x - A.y};
      z[i] = zz;
    }
    wbar();
    W1DMA(8, 12);
    fft4<true>(z[0], z[1], z[2], z[3]);
    fft4<true>(z[4], z[5], z[6], z[7]);
#pragma unroll
    for (int a = 0; a < 2; ++a)
#pragma unroll
      for (int i = 1; i < 4; ++i) z[4 * a + i] = cmulc(z[4 * a + i], tw2[a][i]);
#pragma unroll
    for (int i = 0; i < 8; ++i) L[(i & 3) * 72 + 32 * (i >> 2) + j] = z[i];
    wbar();
#pragma unroll
    for (int q = 0; q < 8; ++q) z[q] = L[(j & 3) * 72 + 8 * q + (j >> 2)];
    W1DMA(12, 16);
    fft8<true>(z);
#pragma unroll
    for (int m = 0; m < 8; ++m) L[(j >> 2) * 36 + (j & 3) + 4 * m] = z[m];
    wbar();
#pragma unroll
    for (int q = 0; q < 8; ++q) z[q] = L[q * 36 + j];
    W1DMA(16, 22);
#pragma unroll
    for (int q = 1; q < 8; ++q) z[q] = cmulc(z[q], tw1[q]);
    fft8<true>(z);
    W1DMA(22, 28);
#pragma unroll
    for (int jt = 0; jt < 7; ++jt) bias[jt] = *(const u2v*)(b1 + ct * 112 + 16 * jt + 4 * g);
    SB();
    u32 x1[8], o[8];
#pragma unroll
    for (int m = 0; m < 8; ++m) { x1[m] = pack2(unpk(xin[m]) + rbf2(z[m])); z[m] = unpk(x1[m]); }
    const int rho = 8 * s2 + s1;
    u32* Lw = (u32*)L;
#pragma unroll
    for (int m = 0; m < 8; ++m) Lw[32 * m + j] = x1[m];
    wbar();
    u4v xa = *(const u4v*)(Lw + 4 * j), xb = *(const u4v*)(Lw + 128 + 4 * j);
    layernorm8(z, o);
#pragma unroll
    for (int m = 0; m < 8; ++m) o[m] = nosent(o[m]);
    char* sg = smem + LDS_W1 + 20480;
    *(u4v*)(sg + j * 288 + s1 * 16) = u4v{o[0], o[1], o[2], o[3]};
    *(u4v*)(sg + j * 288 + 144 + s1 * 16) = u4v{o[4], o[5], o[6], o[7]};
    bar();
    const char* Lr = sg + (16 * w + (lane >> 3)) * 144 + (lane & 7) * 16;
    const u4v fa = *(const u4v*)Lr, fb = *(const u4v*)(Lr + 8 * 144);
    const u32 lo = ol + lno;
    SB();
    bst4<16>(rs, lo, fa);
    bst4<16>(rs, lo + 2048, fb);
    SB();
    bst4<16>(rs, OFF_X1P + rho * 1024 + j * 16, xa);
    bst4<16>(rs, OFF_X1P + rho * 1024 + j * 16 + 512, xb);
  }
  STAMP(kid, 2);
  u4v hf[14][4];
  u32 hoff[4];
#pragma unroll
  for (int h = 0; h < 4; ++h) hoff[h] = oh + (4 * rt2 + ((w + h) & 3)) * 56 * 1024;
#define HFLDX(n, A) do { _Pragma("unroll") for (int h_ = 0; h_ < 4; ++h_) hf[n][h_] = bld4s<A>(rs, l16 + KI(n) * 1024, hoff[h_] + KB(n) * 1024); } while (0)
#define HFLD(n) HFLDX(n, 16)
#define HFLDF(n) HFLDX(n, FAUX)
#define HFMX(n) mx4(mx4(mx4(mx4(0, hf[n][0]), hf[n][1]), hf[n][2]), hf[n][3])
#define CMPS(bad, m) asm volatile("v_cmp_eq_u32 %0, -1, %1" : "=s"(bad) : "v"(m))
  {
    u4v av[2][16];
#define AVLDX(ks, A) do { _Pragma("unroll") for (int h_ = 0; h_ < 2; ++h_) av[h_][ks] = bld4s<A>(rs, l16 + (((ks) + ct) & 15) * 1024, ol + (tt0 + h_) * 16384); } while (0)
#define AVLD(ks) AVLDX(ks, 16)
    SB();
    wait_vm0();
    __builtin_amdgcn_s_barrier();
    MEMF();
    STAMP(kid, 3);
    f4 acc[7][2];
    u32 o0[8];
#define GELU8(p, o) do { f2 a_[8], bs_[8]; \
      _Pragma("unroll") for (int h = 0; h < 2; ++h) { \
        f4 a0 = acc[2 * (p)][h], a1 = acc[2 * (p) + 1][h]; \
        a_[4 * h] = f2{a0[0], a0[1]}; a_[4 * h + 1] = f2{a0[2], a0[3]}; a_[4 * h + 2] = f2{a1[0], a1[1]}; a_[4 * h + 3] = f2{a1[2], a1[3]}; \
        bs_[4 * h] = unpk(bias[2 * (p)].x); bs_[4 * h + 1] = unpk(bias[2 * (p)].y); bs_[4 * h + 2] = unpk(bias[2 * (p) + 1].x); bs_[4 * h + 3] = unpk(bias[2 * (p) + 1].y); } \
      SB(); W2LD(W2E + 2 * (p)); W2LD(W2E + 2 * (p) + 1); \
      if ((p) == HPOS) { _Pragma("unroll") for (int n = 0; n < PD; ++n) HFLDF(n); } \
      SB(); actN<8>(a_, bs_, o); } while (0)
#define HST8(p, o) do { _Pragma("unroll") for (int h = 0; h < 2; ++h) \
        bst4<16>(rs, oh + hst + h * 57344 + (p) * 1024, u4v{o[4 * h], o[4 * h + 1], o[4 * h + 2], o[4 * h + 3]}); } while (0)
    for (int redo = 0;; ++redo) {
      do {
        MEMF();
#pragma unroll
        for (int ks = 0; ks < PA; ++ks) AVLD(ks);
        u32 m = 0;
#pragma unroll
        for (int ks = 0; ks < PA; ++ks) { m = mx4(m, av[0][ks]); m = mx4(m, av[1][ks]); }
        if (!anysent(m)) break;
      } while (--budget > 0);
      STAMP(kid, 4);
      TSTEP(31);
      SB();
#pragma unroll
      for (int jt = 0; jt < 7; ++jt) { acc[jt][0] = f4{0, 0, 0, 0}; acc[jt][1] = f4{0, 0, 0, 0}; }
      const char* lb = smem + l16;
      u4v wf[2][7];
#pragma unroll
      for (int jt = 0; jt < 7; ++jt) wf[0][jt] = *(const u4v*)(lb + jt * 1024);
      SB();
#pragma unroll
      for (int ks = 0; ks < 16; ++ks) {
        const int cu = ks & 1, nx = cu ^ 1;
        const bool chk = ks + 1 >= PA && ks < 15;
        u32 m = 0;
        unsigned long long bad = 0;
#pragma unroll
        for (int jt = 0; jt < 7; ++jt) {
          bf8v wfb = __builtin_bit_cast(bf8v, wf[cu][jt]);
          acc[jt][0] = __builtin_amdgcn_mfma_f32_16x16x32_bf16(wfb, __builtin_bit_cast(bf8v, av[0][ks]), acc[jt][0], 0, 0, 0);
          SB();
          if (ks < 15) wf[nx][jt] = *(const u4v*)(lb + ((ks + 1) * 7 + jt) * 1024);
          SB();
          acc[jt][1] = __builtin_amdgcn_mfma_f32_16x16x32_bf16(wfb, __builtin_bit_cast(bf8v, av[1][ks]), acc[jt][1], 0, 0, 0);
          SB();
          if (ks + PA < 16 && jt < 2) av[jt][ks + PA] = bld4s<FAUX>(rs, l16 + ((ks + PA + ct) & 15) * 1024, ol + (tt0 + jt) * 16384);
          if (ks >= 16 - W2E && jt < 4) W2LD1(ks - (16 - W2E), jt);
          if (chk && jt == 4) asm volatile("v_max_u32 %0, %1, %2" : "=v"(m) : "v"(av[0][ks + 1].x), "v"(av[1][ks + 1].x));
          if (chk && jt == 5) CMPS(bad, m);
          SB();
        }
        TSTEP(ks);
        if (chk) {
          MEMF();
          if (__builtin_expect(bad != 0, 0)) {
            NSLOW();
            do {
              MEMF();
#pragma unroll
              for (int k2 = ks + 1; k2 < 16 && k2 <= ks + PA; ++k2) AVLD(k2);
              wait_vm0();
            } while (anysent(mx4(mx4(0, av[0][ks + 1]), av[1][ks + 1])) && --budget > 0);
          }
          SB();
        }
      }
      STAMP(kid, 5);
      const bool nan = __builtin_amdgcn_ballot_w64(__builtin_isunordered(acc[0][0][0], acc[0][1][0])) != 0;
      GELU8(0, o0);
      MEMF();
      if (__builtin_expect(!nan || redo >= 3, 1)) break;
    }
    HST8(0, o0);
    {
      u32 o[8];
      GELU8(1, o);
      HST8(1, o);
      GELU8(2, o);
      HST8(2, o);
    }
    {
      f4 a0 = acc[6][0], a1 = acc[6][1];
      f2 a[4] = {f2{a0[0], a0[1]}, f2{a0[2], a0[3]}, f2{a1[0], a1[1]}, f2{a1[2], a1[3]}};
      f2 bs[4] = {unpk(bias[6].x), unpk(bias[6].y), unpk(bias[6].x), unpk(bias[6].y)};
      u32 o4[4];
      SB();
      W2LD(13);
      if (HPOS == 3) {
#pragma unroll
        for (int n = 0; n < PD; ++n) HFLDF(n);
      }
      SB();
      actN<4>(a, bs, o4);
      auto rx = __builtin_amdgcn_permlane16_swap(o4[0], o4[2], false, false);
      auto ry = __builtin_amdgcn_permlane16_swap(o4[1], o4[3], false, false);
      bst4<16>(rs, oh + htl, u4v{rx[0], ry[0], rx[1], ry[1]});
    }
  }
  STAMP(kid, 6);
  R1Tw tw;
  {
    f4 acc[4][4];
    u4v bb0 = *(const u4v*)(b2 + 64 * ct2 + 16 * g), bb1 = *(const u4v*)(b2 + 64 * ct2 + 16 * g + 8);
    const u32 pvo = oh + (((4 * rt2 + w) * 56 + 7 * (4 * (rt2 & 1) + w) + 3 * (lane >> 5)) * 64 + (lane & 31)) * 16;
    u32 pv = 0;
    u4v x1a, x1b;
    f4* red = (f4*)(smem + LDS_W1);
    for (int redo = 0;; ++redo) {
      SB();
      MEMF();
      if (__builtin_expect(redo != 0, 0)) {
#pragma unroll
        for (int n = 0; n < 14; ++n) W2LD(n);
#pragma unroll
        for (int n = 0; n < PD; ++n) HFLD(n);
        wait_vm0();
      }
      if (anysent(HFMX(0))) {
        do {
          MEMF();
#pragma unroll
          for (int n = 0; n < PD; ++n) HFLD(n);
          wait_vm0();
        } while (anysent(HFMX(0)) && --budget > 0);
      }
#pragma unroll
      for (int i = 0; i < 4; ++i)
#pragma unroll
        for (int h = 0; h < 4; ++h) acc[i][h] = f4{0, 0, 0, 0};
      STAMP(kid, 8);
      TSTEP(30);
      SB();
#pragma unroll
      for (int n = 0; n < 14; ++n) {
        u32 m = 0;
        unsigned long long bad = 0;
#pragma unroll
        for (int q = 0; q < 8; ++q) {
          const int h = q >> 1, i = 2 * (q & 1);
          acc[i][h] = __builtin_amdgcn_mfma_f32_16x16x32_bf16(__builtin_bit_cast(bf8v, w2f[n][i]), __builtin_bit_cast(bf8v, hf[n][h]), acc[i][h], 0, 0, 0);
          SB();
          if (n + PD < 14 && q < 4) hf[n + PD][q] = bld4s<FAUX>(rs, l16 + KI(n + PD) * 1024, hoff[q] + KB(n + PD) * 1024);
          if (n == 4 && q == 4) pv = bld1<16>(rs, pvo);
          if (n < 13 && q == 5) asm volatile("v_max3_u32 %0, %1, %2, %3" : "=v"(m) : "v"(hf[n + 1][0].x), "v"(hf[n + 1][1].x), "v"(hf[n + 1][2].x));
          if (n < 13 && q == 6) CMPS(bad, m);
          SB();
          acc[i + 1][h] = __builtin_amdgcn_mfma_f32_16x16x32_bf16(__builtin_bit_cast(bf8v, w2f[n][i + 1]), __builtin_bit_cast(bf8v, hf[n][h]), acc[i + 1][h], 0, 0, 0);
          SB();
          if (n < 13 && n != 8 && q == 5) asm volatile("v_max_u32 %0, %0, %1" : "+v"(m) : "v"(hf[n + 1][3].x));
          if (n == 8 && q == 5) asm volatile("v_max3_u32 %0, %0, %1, %2" : "+v"(m) : "v"(hf[n + 1][3].x), "v"(pv));
          SB();
        }
        TSTEP(16 + n);
        if (n < 13) {
          MEMF();
          if (__builtin_expect(bad != 0, 0)) {
            NSLOW();
            do {
              MEMF();
#pragma unroll
              for (int n2 = n + 1; n2 < 14 && n2 <= n + PD; ++n2) HFLD(n2);
              if (n == 8) pv = bld1<16>(rs, pvo);
              wait_vm0();
              m = HFMX(n + 1);
              if (n == 8) m = mx2(m, pv);
            } while (anysent(m) && --budget > 0);
          }
          SB();
        }
        if (n == 8) {
          MEMF();
          x1a = bld4<16>(rs, OFF_X1P + rho2 * 1024 + ct2 * 128 + g * 32);
          x1b = bld4<16>(rs, OFF_X1P + rho2 * 1024 + ct2 * 128 + g * 32 + 16);
          SB();
        }
      }
      STAMP(kid, 9);
      r1_load_tw(tw, t);
      SB();
      const bool nan = __builtin_amdgcn_ballot_w64(__builtin_isunordered(acc[0][0][0], acc[0][1][0]) || __builtin_isunordered(acc[0][2][0], acc[0][3][0])) != 0;
#pragma unroll
      for (int h = 1; h < 4; ++h)
#pragma unroll
        for (int i = 0; i < 4; ++i) red[((((w + h) & 3) * 3 + (h - 1)) * 4 + i) * 64 + lane] = acc[i][h];
      MEMF();
      if (__builtin_expect(!nan || redo >= 3, 1)) break;
    }
    bar();
    f2 s[8];
#pragma unroll
    for (int i = 0; i < 4; ++i) {
      f4 a = (acc[i][0] + red[((w * 3 + 0) * 4 + i) * 64 + lane]) + (red[((w * 3 + 1) * 4 + i) * 64 + lane] + red[((w * 3 + 2) * 4 + i) * 64 + lane]);
      s[2 * i] = f2{a[0], a[1]}; s[2 * i + 1] = f2{a[2], a[3]};
    }
    const u32 x1v[8] = {x1a.x, x1a.y, x1a.z, x1a.w, x1b.x, x1b.y, x1b.z, x1b.w};
    u32 bs[8] = {bb0.x, bb0.y, bb0.z, bb0.w, bb1.x, bb1.y, bb1.z, bb1.w};
    u32 o[8];
#pragma unroll
    for (int i = 0; i < 8; ++i) o[i] = pack2(unpk(x1v[i]) + rbf2(rbf2(s[i]) + unpk(bs[i])));
#pragma unroll
    for (int i = 0; i < 8; ++i) o[i] = nosent(o[i]);
    char* st = smem + LDS_W1 + w * 12288;
    wbar();
    *(u4v*)(st + c * 144 + g * 32) = u4v{o[0], o[1], o[2], o[3]};
    *(u4v*)(st + c * 144 + g * 32 + 16) = u4v{o[4], o[5], o[6], o[7]};
    wbar();
    const u4v ra = *(const u4v*)(st + (lane >> 3) * 144 + (lane & 7) * 16), rb = *(const u4v*)(st + ((lane >> 3) + 8) * 144 + (lane & 7) * 16);
    const u32 xo = ((256 * (lane >> 3) + 8 * rt2 + 2 * w) * D_ + 64 * ct2) * 2 + (lane & 7) * 16;
    if (last) {
      *(u4v*)((char*)out + xo) = ra;
      *(u4v*)((char*)out + xo + 1024) = rb;
      STAMP(kid, 10);
      return;
    }
    bst4<16>(rs, OFF_XR + xo, ra);
    bst4<16>(rs, OFF_XR + xo + 1024, rb);
  }
  STAMP(kid, 10);
  {
    u32 xin[8];
    const u32 ro = OFF_XR + ((256 * (t >> 5) + s2) * 256 + (t & 31)) * 4;
    wait_vm0();
    do {
      MEMF();
#pragma unroll
      for (int m = 0; m < 8; ++m) xin[m] = bld1<16>(rs, ro + 128 * m);
    } while (anysent(mx2(mx2(mx2(xin[0], xin[1]), mx2(xin[2], xin[3])), mx2(mx2(xin[4], xin[5]), mx2(xin[6], xin[7])))) && --budget > 0);
    STAMP(kid, 11);
    r1_body(xin, tw, tx, (f2*)smem, ws, s2, t);
    STAMP(kid, 12);
  }
}

extern "C" void kernel_launch(void* const* d_in, const int* in_sizes, int n_in, void* d_out, int out_size, void* d_ws,
                              size_t ws_size, hipStream_t stream) {
  const unsigned short* x = (const unsigned short*)d_in[0];
  const unsigned short* wr = (const unsigned short*)d_in[1];
  const unsigned short* wi = (const unsigned short*)d_in[2];
  const unsigned short* w1 = (const unsigned short*)d_in[4];
  const unsigned short* b1 = (const unsigned short*)d_in[5];
  const unsigned short* w2 = (const unsigned short*)d_in[6];
  const unsigned short* b2 = (const unsigned short*)d_in[7];
  char* ws = (char*)d_ws;
  (void)hipFuncSetAttribute((const void*)k_m, hipFuncAttributeMaxDynamicSharedMemorySize, LDS_M);
  k_first<<<256, 256, 0, stream>>>((const u32*)x, wr, wi, w1, w2, ws);
  k_c<<<256, 256, 0, stream>>>(ws, 1, 1);
  k_m<<<256, 256, LDS_M, stream>>>((const u32*)x, b1, b2, ws, (unsigned short*)d_out, 0, 2);
  k_c<<<256, 256, 0, stream>>>(ws, 0, 3);
  k_m<<<256, 256, LDS_M, stream>>>((const u32*)(ws + OFF_XR), b1, b2, ws, (unsigned short*)d_out, 1, 4);
}
```

```cpp
#include <hip/hip_runtime.h>
#include <hip/hip_bf16.h>
#include <stdint.h>

typedef unsigned int u32;
typedef float f2 __attribute__((ext_vector_type(2)));
typedef float f4 __attribute__((ext_vector_type(4)));
typedef float f16v __attribute__((ext_vector_type(16)));
typedef u32 u2v __attribute__((ext_vector_type(2)));
typedef u32 u4v __attribute__((ext_vector_type(4)));
typedef __bf16 bf2v __attribute__((ext_vector_type(2)));
typedef __bf16 bf8v __attribute__((ext_vector_type(8)));
typedef short s4v __attribute__((ext_vector_type(4)));
typedef short s8v __attribute__((ext_vector_type(8)));
#define LAS __attribute__((address_space(3)))
#define DI __device__ __forceinline__
#define RLX_AGENT __ATOMIC_RELAXED, __HIP_MEMORY_SCOPE_AGENT

#define STAMP(K, I)

constexpr int S_ = 2048, D_ = 512, F_ = 1792, DF_ = 257;
constexpr u32 TPB = 16384 + 256;

constexpr u32 OFF_CNT = 0;
constexpr u32 OFF_T = 65536;
constexpr u32 SZ_T = 256u * TPB;
constexpr u32 OFF_WT = OFF_T + SZ_T;
constexpr u32 SZ_WT = 256u * 256 * 8 * 4;
constexpr u32 OFF_PQ = OFF_WT + SZ_WT;
constexpr u32 SZ_PQ = 256u * 8 * 16;
constexpr u32 OFF_W2F = OFF_PQ + SZ_PQ;
constexpr u32 SZ_W2F = (u32)F_ * D_ * 2;
constexpr u32 OFF_W1F = OFF_W2F + SZ_W2F;
constexpr u32 OFF_XR = OFF_W1F + SZ_W2F;
constexpr u32 SZ_X = (u32)S_ * D_ * 2;
constexpr u32 OFF_X1P = OFF_XR + SZ_X;
constexpr u32 OFF_LNF = OFF_X1P + SZ_X;
constexpr u32 OFF_H = OFF_LNF + SZ_X;
constexpr u32 SZ_H = (u32)S_ * F_ * 2;
constexpr u32 OFF_LNF2 = OFF_H + SZ_H;
constexpr u32 OFF_H2 = OFF_LNF2 + SZ_X;
constexpr u32 WS_USED = OFF_H2 + SZ_H;

struct cxf { float x, y; };
constexpr double kPi = 3.14159265358979323846264338327950288;
constexpr double c_sin(double x) { double x2 = x * x, t = x, s = x; for (int i = 1; i < 11; ++i) { t *= -x2 / double((2 * i) * (2 * i + 1)); s += t; } return s; }
constexpr double c_cos(double x) { double x2 = x * x, t = 1, s = 1; for (int i = 1; i < 11; ++i) { t *= -x2 / double((2 * i - 1) * (2 * i)); s += t; } return s; }
constexpr cxf tw2048(int i) {
  i &= 2047;
  int qd = i >> 9, r = i & 511;
  double c = 0, s = 0;
  if (r <= 256) { double a = 2 * kPi * r / 2048.0; c = c_cos(a); s = c_sin(a); }
  else { double a = 2 * kPi * (512 - r) / 2048.0; c = c_sin(a); s = c_cos(a); }
  double cc = 0, ss = 0;
  if (qd == 0) { cc = c; ss = s; } else if (qd == 1) { cc = -s; ss = c; } else if (qd == 2) { cc = -c; ss = -s; } else { cc = s; ss = -c; }
  return cxf{(float)cc, (float)(-ss)};
}
template <int R, int C> struct Tab { cxf v[R * C]; };
constexpr Tab<32, 8> mk_r1() { Tab<32, 8> t{}; for (int j = 0; j < 32; ++j) for (int q = 0; q < 8; ++q) t.v[j * 8 + q] = tw2048(8 * j * q); return t; }
constexpr Tab<8, 8> mk_r2() { Tab<8, 8> t{}; for (int j = 0; j < 8; ++j) for (int q = 0; q < 8; ++q) t.v[j * 8 + q] = tw2048(64 * j * q); return t; }
constexpr Tab<256, 1> mk_512() { Tab<256, 1> t{}; for (int c = 0; c < 256; ++c) t.v[c] = tw2048(4 * c + 512); return t; }
constexpr Tab<32, 8> mk_512u() { Tab<32, 8> t{}; for (int u = 0; u < 32; ++u) for (int i = 0; i < 8; ++i) t.v[u * 8 + i] = tw2048(4 * (u + 32 * (i >> 2) + 64 * (i & 3)) + 512); return t; }
constexpr Tab<32, 22> mk_r2all() {
  Tab<32, 22> t{};
  for (int j = 0; j < 32; ++j) {
    cxf v[22] = {};
    for (int i = 0; i < 8; ++i) v[i] = tw2048(4 * (j + 32 * (i >> 2) + 64 * (i & 3)) + 512);
    for (int a = 0; a < 2; ++a) for (int i = 1; i < 4; ++i) v[8 + 3 * a + (i - 1)] = tw2048(64 * ((j >> 3) + 4 * a) * i);
    for (int q = 1; q < 8; ++q) v[13 + q] = tw2048(8 * j * q);
    for (int i = 0; i < 22; ++i) t.v[((i >> 1) * 32 + j) * 2 + (i & 1)] = v[i];
  }
  return t;
}
constexpr Tab<32, 14> mk_r1all() {
  Tab<32, 14> t{};
  for (int j = 0; j < 32; ++j)
    for (int i = 0; i < 14; ++i) t.v[((i >> 1) * 32 + j) * 2 + (i & 1)] = i < 7 ? tw2048(8 * j * (i + 1)) : tw2048(64 * (j & 3) * (i - 6));
  return t;
}
constexpr Tab<256, 8> mk_x() { Tab<256, 8> t{}; for (int s = 0; s < 256; ++s) for (int k = 0; k < 8; ++k) t.v[s * 8 + k] = tw2048(s * k); return t; }
constexpr Tab<64, 4> mk_c1() { Tab<64, 4> t{}; for (int j = 0; j < 64; ++j) for (int q = 0; q < 4; ++q) t.v[j * 4 + q] = tw2048(8 * j * q); return t; }
constexpr double kSc = 6.9053396600248785e-04;
constexpr cxf tw2048s(int i) {
  i &= 2047;
  int qd = i >> 9, r = i & 511;
  double c = 0, s = 0;
  if (r <= 256) { double a = 2 * kPi * r / 2048.0; c = c_cos(a); s = c_sin(a); }
  else { double a = 2 * kPi * (512 - r) / 2048.0; c = c_sin(a); s = c_cos(a); }
  double cc = 0, ss = 0;
  if (qd == 0) { cc = c; ss = s; } else if (qd == 1) { cc = -s; ss = c; } else if (qd == 2) { cc = -c; ss = -s; } else { cc = s; ss = -c; }
  return cxf{(float)(cc * kSc), (float)(-ss * kSc)};
}
constexpr Tab<8, 8> mk_c2() { Tab<8, 8> t{}; for (int j = 0; j < 8; ++j) for (int q = 0; q < 8; ++q) t.v[((q >> 1) * 8 + j) * 2 + (q & 1)] = tw2048s(32 * j * q); return t; }

__device__ const __attribute__((aligned(64))) Tab<32, 14> g_r1all = mk_r1all();
__device__ const __attribute__((aligned(64))) Tab<256, 1> g_tw512 = mk_512();
__device__ const __attribute__((aligned(64))) Tab<32, 22> g_r2all = mk_r2all();
__device__ const __attribute__((aligned(64))) Tab<256, 8> g_twx = mk_x();
__device__ const __attribute__((aligned(64))) Tab<64, 4> g_twc1 = mk_c1();
__device__ const __attribute__((aligned(64))) Tab<8, 8> g_twc2 = mk_c2();

DI float bf_lo(u32 v) { return __uint_as_float(v << 16); }
DI float bf_hi(u32 v) { return __uint_as_float(v & 0xffff0000u); }
DI f2 unpk(u32 v) { return f2{bf_lo(v), bf_hi(v)}; }
DI float rbf(float x) { return (float)(__bf16)x; }
DI u32 pack2(f2 a) { bf2v v = __builtin_convertvector(a, bf2v); return __builtin_bit_cast(u32, v); }
DI f2 rbf2(f2 a) { return unpk(pack2(a)); }
DI f2 swp(f2 a) { return __builtin_shufflevector(a, a, 1, 0); }
DI f2 fma2(f2 a, f2 b, f2 c) { return __builtin_elementwise_fma(a, b, c); }
DI f2 xx(f2 a) { return __builtin_shufflevector(a, a, 0, 0); }
DI f2 yy(f2 a) { return __builtin_shufflevector(a, a, 1, 1); }
DI f2 cmul(f2 a, f2 b) {
  f2 t, r;
  asm("v_pk_mul_f32 %0, %1, %2 op_sel:[1,1] op_sel_hi:[1,0]" : "=v"(t) : "v"(a), "v"(b));
  asm("v_pk_fma_f32 %0, %1, %2, %3 op_sel:[0,0,0] op_sel_hi:[0,1,1] neg_lo:[0,0,1]" : "=v"(r) : "v"(a), "v"(b), "v"(t));
  return r;
}
DI f2 cmulc(f2 a, f2 b) {
  f2 t, r;
  asm("v_pk_mul_f32 %0, %1, %2 op_sel:[1,1] op_sel_hi:[1,0]" : "=v"(t) : "v"(a), "v"(b));
  asm("v_pk_fma_f32 %0, %1, %2, %3 op_sel:[0,0,0] op_sel_hi:[0,1,1] neg_hi:[0,1,0]" : "=v"(r) : "v"(a), "v"(b), "v"(t));
  return r;
}
DI f2 cmul_s(f2 a, f2 b) {
  f2 t, r;
  asm("v_pk_mul_f32 %0, %1, %2 op_sel:[1,1] op_sel_hi:[1,0]" : "=v"(t) : "v"(a), "s"(b));
  asm("v_pk_fma_f32 %0, %1, %2, %3 op_sel:[0,0,0] op_sel_hi:[0,1,1] neg_lo:[0,0,1]" : "=v"(r) : "v"(a), "s"(b), "v"(t));
  return r;
}
DI f2 cmulc_s(f2 a, f2 b) {
  f2 t, r;
  asm("v_pk_mul_f32 %0, %1, %2 op_sel:[1,1] op_sel_hi:[1,0]" : "=v"(t) : "v"(a), "s"(b));
  asm("v_pk_fma_f32 %0, %1, %2, %3 op_sel:[0,0,0] op_sel_hi:[0,1,1] neg_hi:[0,1,0]" : "=v"(r) : "v"(a), "s"(b), "v"(t));
  return r;
}
DI f2 cmul_add(f2 a, f2 b, f2 e) {
  f2 t, r;
  asm("v_pk_fma_f32 %0, %1, %2, %3 op_sel:[1,1,0] op_sel_hi:[1,0,1] neg_lo:[0,1,0]" : "=v"(t) : "v"(a), "v"(b), "v"(e));
  asm("v_pk_fma_f32 %0, %1, %2, %3 op_sel:[0,0,0] op_sel_hi:[0,1,1]" : "=v"(r) : "v"(a), "v"(b), "v"(t));
  return r;
}
DI f2 cmulc_add(f2 a, f2 b, f2 e) {
  f2 t, r;
  asm("v_pk_fma_f32 %0, %1, %2, %3 op_sel:[1,1,0] op_sel_hi:[1,0,1]" : "=v"(t) : "v"(a), "v"(b), "v"(e));
  asm("v_pk_fma_f32 %0, %1, %2, %3 op_sel:[0,0,0] op_sel_hi:[0,1,1] neg_hi:[0,1,0]" : "=v"(r) : "v"(a), "v"(b), "v"(t));
  return r;
}
DI f2 ccmul_add(f2 a, f2 b, f2 e) {
  f2 t, r;
  asm("v_pk_fma_f32 %0, %1, %2, %3 op_sel:[1,1,0] op_sel_hi:[1,0,1] neg_hi:[0,1,0]" : "=v"(t) : "v"(a), "v"(b), "v"(e));
  asm("v_pk_fma_f32 %0, %1, %2, %3 op_sel:[0,0,0] op_sel_hi:[0,1,1]" : "=v"(r) : "v"(a), "v"(b), "v"(t));
  return r;
}
template <bool INV> DI f2 addmi(f2 a, f2 d) { return fma2(swp(d), INV ? f2{-1.f, 1.f} : f2{1.f, -1.f}, a); }
template <bool INV> DI f2 submi(f2 a, f2 d) { return fma2(swp(d), INV ? f2{1.f, -1.f} : f2{-1.f, 1.f}, a); }

template <bool INV> DI void fft4(f2& b0, f2& b1, f2& b2, f2& b3) {
  f2 c0 = b0 + b2, c2 = b0 - b2, c1 = b1 + b3, d = b1 - b3;
  b0 = c0 + c1; b2 = c0 - c1; b1 = addmi<INV>(c2, d); b3 = submi<INV>(c2, d);
}
template <bool INV> DI void fft8(f2* v) {
  const float h = 0.70710678118654752440f;
  f2 a0 = v[0] + v[4], a4 = v[0] - v[4];
  f2 a1 = v[1] + v[5], a5 = v[1] - v[5];
  f2 a2 = v[2] + v[6], a6 = v[2] - v[6];
  f2 a3 = v[3] + v[7], a7 = v[3] - v[7];
  f2 c0 = addmi<INV>(a4, a6), c2 = submi<INV>(a4, a6);
  f2 u = addmi<INV>(a5, a7), w = submi<INV>(a5, a7);
  f2 tu = addmi<INV>(u, u), tw = addmi<INV>(w, w);
  fft4<INV>(a0, a1, a2, a3);
  v[0] = a0; v[2] = a1; v[4] = a2; v[6] = a3;
  v[1] = fma2(tu, f2{h, h}, c0);
  v[5] = fma2(tu, f2{-h, -h}, c0);
  v[3] = fma2(swp(tw), INV ? f2{-h, h} : f2{h, -h}, c2);
  v[7] = fma2(swp(tw), INV ? f2{h, -h} : f2{-h, h}, c2);
}
template <int CTRL> DI float dpp_add(float x) {
  return x + __int_as_float(__builtin_amdgcn_update_dpp(0, __float_as_int(x), CTRL, 0xf, 0xf, false));
}
DI float red32(float x) {
  x = dpp_add<0xB1>(x); x = dpp_add<0x4E>(x); x = dpp_add<0x141>(x); x = dpp_add<0x140>(x);
  auto r = __builtin_amdgcn_permlane16_swap(__float_as_uint(x), __float_as_uint(x), false, false);
  return __uint_as_float(r[0]) + __uint_as_float(r[1]);
}
DI void wbar() { __builtin_amdgcn_wave_barrier(); }
DI void bar() {
  asm volatile("s_waitcnt lgkmcnt(0)" ::: "memory");
  __builtin_amdgcn_s_barrier();
  asm volatile("" ::: "memory");
}
DI void wait_vm0() { __builtin_amdgcn_s_waitcnt(0x0F70); asm volatile("" ::: "memory"); }
DI void layernorm8(const f2* v, u32* o) {
  f2 s2 = ((v[0] + v[1]) + (v[2] + v[3])) + ((v[4] + v[5]) + (v[6] + v[7]));
  float mean = red32(s2.x + s2.y) * (1.0f / 512.0f);
  f2 mm = f2{mean, mean};
  f2 q0 = f2{0, 0}, q1 = f2{0, 0};
#pragma unroll
  for (int i = 0; i < 8; i += 2) { f2 d0 = v[i] - mm, d1 = v[i + 1] - mm; q0 = fma2(d0, d0, q0); q1 = fma2(d1, d1, q1); }
  q0 = q0 + q1;
  float q = red32(q0.x + q0.y);
  float m = rbf(mean), vr = rbf(q * (1.0f / 512.0f));
  float sd = rbf(__builtin_amdgcn_sqrtf(rbf(vr + 1e-5f)));
  float ri = __builtin_amdgcn_rcpf(sd);
  f2 m2 = f2{m, m}, r2 = f2{ri, ri};
#pragma unroll
  for (int i = 0; i < 8; ++i) o[i] = pack2(rbf2(v[i] - m2) * r2);
}
DI f2 ldtw(const cxf* p) { return *(const f2*)p; }
typedef __amdgpu_buffer_rsrc_t rsrc_t;
DI rsrc_t mk_rsrc(void* p, u32 bytes) { return __builtin_amdgcn_make_buffer_rsrc(p, 0, bytes, 0x00020000); }
template <int AUX> DI u4v bld4(rsrc_t r, u32 off) { return __builtin_amdgcn_raw_buffer_load_b128(r, off, 0, AUX); }
template <int AUX> DI u32 bld1(rsrc_t r, u32 off) { return __builtin_amdgcn_raw_buffer_load_b32(r, off, 0, AUX); }
template <int AUX> DI void bst4(rsrc_t r, u32 off, u4v v) { __builtin_amdgcn_raw_buffer_store_b128(v, r, off, 0, AUX); }
DI u4v f4u(f2 a, f2 b) { return u4v{__float_as_uint(a.x), __float_as_uint(a.y), __float_as_uint(b.x), __float_as_uint(b.y)}; }

constexpr int RP = 288;

constexpr u32 SENT = 0xFFFFFFFFu;
DI u32 nosent(u32 v) { return v < SENT - 1 ? v : SENT - 1; }
DI u32 mx2(u32 a, u32 b) { return a > b ? a : b; }
DI u32 mx4(u32 m, u4v a) { return mx2(mx2(mx2(mx2(m, a.x), a.y), a.z), a.w); }
DI bool anysent(u32 m) { return __builtin_amdgcn_ballot_w64(m == SENT) != 0; }
struct R1Tw { f2 tw1[8], tw2[8], w512; };
DI void r1_load_tw(R1Tw& tw, int t) {
  const f4* p = (const f4*)&g_r1all + (t & 31);
  f2 v[14];
#pragma unroll
  for (int i = 0; i < 7; ++i) { f4 q = p[32 * i]; v[2 * i] = f2{q[0], q[1]}; v[2 * i + 1] = f2{q[2], q[3]}; }
#pragma unroll
  for (int q = 1; q < 8; ++q) { tw.tw1[q] = v[q - 1]; tw.tw2[q] = v[6 + q]; }
  tw.w512 = ldtw(&g_tw512.v[t]);
}
DI void r1_body(const u32* xin, const R1Tw& tw, const f16v& tx, f2* lds, char* ws, int s2, int t) {
  const int s1 = t >> 5, j = t & 31;
  f2 z[8];
#pragma unroll
  for (int m = 0; m < 8; ++m) z[m] = unpk(xin[m]);
  u32 o[8];
  layernorm8(z, o);
#pragma unroll
  for (int m = 0; m < 8; ++m) z[m] = unpk(o[m]);
  f2* L = lds + s1 * RP;
  fft8<false>(z);
#pragma unroll
  for (int q = 1; q < 8; ++q) z[q] = cmul(z[q], tw.tw1[q]);
#pragma unroll
  for (int q = 0; q < 8; ++q) L[q * 36 + j] = z[q];
  wbar();
#pragma unroll
  for (int m = 0; m < 8; ++m) z[m] = L[(j >> 2) * 36 + (j & 3) + 4 * m];
  wbar();
  fft8<false>(z);
#pragma unroll
  for (int q = 1; q < 8; ++q) z[q] = cmul(z[q], tw.tw2[q]);
#pragma unroll
  for (int q = 0; q < 8; ++q) L[(j & 3) * 68 + 8 * q + (j >> 2)] = z[q];
  wbar();
#pragma unroll
  for (int i = 0; i < 8; ++i) z[i] = L[(i & 3) * 68 + 32 * (i >> 2) + j];
  wbar();
  fft4<false>(z[0], z[1], z[2], z[3]);
  fft4<false>(z[4], z[5], z[6], z[7]);
#pragma unroll
  for (int i = 0; i < 8; ++i) L[j + 32 * (i >> 2) + 64 * (i & 3)] = z[i];
  bar();
  f2 X[8];
#pragma unroll
  for (int r = 0; r < 8; ++r) {
    f2 A = lds[r * RP + t], Bc = lds[r * RP + ((256 - t) & 255)];
    f2 E = fma2(Bc, f2{1.f, -1.f}, A);
    f2 Dm = fma2(Bc, f2{-1.f, 1.f}, A);
    f2 x2 = cmul_add(Dm, tw.w512, E);
    if (t == 0) x2.y = E.x - Dm.y;
    X[r] = x2;
  }
  fft8<false>(X);
#pragma unroll
  for (int k = 1; k < 8; ++k) X[k] = cmul_s(X[k], f2{tx[2 * k], tx[2 * k + 1]});
  u4v* dst = (u4v*)(ws + OFF_T + (size_t)s2 * TPB) + t;
  dst[0] = f4u(X[0], X[4]);
  dst[256] = f4u(X[1], X[7]);
  dst[512] = f4u(X[2], X[6]);
  dst[768] = f4u(X[3], X[5]);
}

DI int f_of(int kap, int g, int j) {
  int u = kap / 7, r = kap % 7;
  if (r < 6) { int ct = 2 * u + (r >= 3), p = r % 3; return 112 * ct + 32 * p + (j < 4 ? 4 * g + j : 16 + 4 * g + (j - 4)); }
  return 112 * (2 * u + (g >> 1)) + 96 + 8 * (g & 1) + j;
}
constexpr int WPIT = 528;
#define ARM_A(PARTS) do { if ((PARTS) && first) { \
    const u4v sv = u4v{SENT, SENT, SENT, SENT}; \
    const int xcd_ = b & 7, ii_ = b >> 3, w_ = __builtin_amdgcn_readfirstlane(t >> 6), ln_ = t & 63, g_ = ln_ >> 4; \
    const int s2_ = 32 * xcd_ + ii_, rt_ = 2 * xcd_ + (ii_ >> 4), ct_ = ii_ & 15, rt2_ = 4 * xcd_ + (ii_ >> 3), ct2_ = ii_ & 7; \
    if ((PARTS) & 1) { \
      const u32 co = OFF_LNF + ((s2_ >> 1) * 16 + 4 * w_) * 1024 + (ln_ >> 3) * 256 + (s2_ & 1) * 128 + (ln_ & 7) * 16; \
      bst4<CAUX>(rs, co, sv); bst4<CAUX>(rs, co + 2048, sv); \
      const u32 xo = OFF_XR + ((256 * (ln_ >> 3) + 8 * rt2_ + 2 * w_) * D_ + 64 * ct2_) * 2 + (ln_ & 7) * 16; \
      bst4<CAUX>(rs, xo, sv); bst4<CAUX>(rs, xo + 1024, sv); } \
    if ((PARTS) & 2) { \
      const int tt0_ = 8 * rt_ + 2 * w_, kb_ = 7 * (ct_ >> 1) + 3 * (ct_ & 1); \
      _Pragma("unroll") for (int h = 0; h < 2; ++h) _Pragma("unroll") for (int p = 0; p < 3; ++p) \
        bst4<CAUX>(rs, OFF_H + ((tt0_ + h) * 56 + kb_ + p) * 1024 + ln_ * 16, sv); \
      bst4<CAUX>(rs, OFF_H + (((tt0_ + (g_ & 1)) * 56 + 7 * (ct_ >> 1) + 6) * 64 + (2 * (ct_ & 1) + (g_ >> 1)) * 16 + (ln_ & 15)) * 16, sv); } \
    __builtin_amdgcn_sched_barrier(0); } } while (0)
#define CAUX 0
#define KF_ARM 3
#define KC_ARM (3 - (KF_ARM))
__global__ __launch_bounds__(256) void k_first(const u32* __restrict__ x, const unsigned short* __restrict__ wr,
                                               const unsigned short* __restrict__ wi, const unsigned short* __restrict__ w1,
                                               const unsigned short* __restrict__ w2, char* __restrict__ ws) {
  __shared__ __attribute__((aligned(16))) char smem[8 * RP * 8 + 32 * WPIT];
  const int b = blockIdx.x, t = threadIdx.x, w = __builtin_amdgcn_readfirstlane(t >> 6), lane = t & 63;
  STAMP(0, 0);
  asm volatile("" :: "s"(x), "s"(wr), "s"(wi), "s"(w1), "s"(w2), "s"(ws));
  const int s2 = 32 * (b & 7) + (b >> 3);
  u32 xin[8];
  const u32* xr = x + (size_t)(256 * (t >> 5) + s2) * 256 + (t & 31);
#pragma unroll
  for (int m = 0; m < 8; ++m) xin[m] = xr[32 * m];
  R1Tw tw;
  r1_load_tw(tw, t);
  const f16v tx = *(const f16v*)&g_twx.v[s2 * 8];
  unsigned short fr[8], fi[8];
#pragma unroll
  for (int i = 0; i < 8; ++i) { fr[i] = wr[(b + 256 * i) * DF_ + t]; fi[i] = wi[(b + 256 * i) * DF_ + t]; }
  u4v wu[4];
  const int isw2 = b >= 112;
  const int ua = isw2 ? (b - 112) >> 1 : b / 7, ub = isw2 ? (b - 112) & 1 : b % 7;
  if (b < 224) {
#pragma unroll
    for (int g = 0; g < 4; ++g) {
      const int i = t >> 5;
      const unsigned short* p;
      if (isw2) p = w2 + (size_t)f_of(ua, g, i) * D_;
      else p = w1 + (size_t)(256 * (g & 1) + 64 * (i >> 1) + 4 * ua + 2 * (g >> 1) + (i & 1)) * F_;
      wu[g] = *(const u4v*)(p + 256 * ub + (t & 31) * 8);
    }
  } else {
#pragma unroll
    for (int g = 0; g < 4; ++g) asm volatile("" : "=v"(wu[g]));
  }
  unsigned short pv[8] = {};
  if (w == 0) {
    const int k = b + 256 * (t & 7), km = (2048 - k) & 2047;
    pv[0] = wr[k * DF_]; pv[1] = wi[k * DF_]; pv[2] = wr[km * DF_]; pv[3] = wi[km * DF_];
    pv[4] = wr[k * DF_ + 256]; pv[5] = wi[k * DF_ + 256]; pv[6] = wr[km * DF_ + 256]; pv[7] = wi[km * DF_ + 256];
  }
  __builtin_amdgcn_sched_barrier(0);
  u32 pf0 = 0;
  if (w == 3 && b < 64) {
    u32 alo, ahi, elo, ehi;
    asm volatile("s_getpc_b64 s[40:41]\n\ts_mov_b32 %0, s40\n\ts_mov_b32 %1, s41" : "=s"(alo), "=s"(ahi) :: "s40", "s41");
    asm volatile("s_getpc_b64 s[40:41]\n\ts_add_u32 %0, s40, k_c@rel32@lo+4\n\ts_addc_u32 %1, s41, k_c@rel32@hi+12" : "=s"(elo), "=s"(ehi) :: "s40", "s41", "scc");
    const unsigned long long a = ((unsigned long long)ahi << 32) | alo, e = ((unsigned long long)ehi << 32) | elo;
    const u32 lim = e > a && e - a < 65536 ? (u32)(e - a) - 4 : 0u;
    const u32 o = lane * 128, oc = (o < lim ? o : lim) & ~3u;
    asm volatile("global_load_dword %0, %1, %2" : "=v"(pf0) : "v"(oc), "s"(a));
  }
  __builtin_amdgcn_sched_barrier(0);
  {
    const rsrc_t rs = mk_rsrc(ws, WS_USED);
    const bool first = true;
    ARM_A(KF_ARM);
  }
  if (b < 224) {
    char* im = smem + 8 * RP * 8;
#pragma unroll
    for (int g = 0; g < 4; ++g) *(u4v*)(im + (8 * g + (t >> 5)) * WPIT + (t & 31) * 16) = wu[g];
    bar();
    const int g = lane >> 4, q = (lane >> 2) & 3, p = lane & 3;
    const char* a0 = im + (8 * g + q) * WPIT + (isw2 ? 32 * p : 8 * p) + 128 * w;
#pragma unroll
    for (int n = 0; n < 4; ++n) {
      const char* a = a0 + (isw2 ? 8 * n : 32 * n);
      s4v lo = __builtin_amdgcn_ds_read_tr16_b64_v4i16((LAS s4v*)a);
      s4v hi = __builtin_amdgcn_ds_read_tr16_b64_v4i16((LAS s4v*)(a + 4 * WPIT));
      s8v wf = __builtin_shufflevector(lo, hi, 0, 1, 2, 3, 4, 5, 6, 7);
      u32 off;
      if (isw2) off = OFF_W2F + (((4 * ub + w) * 56 + ua) * 4 + n) * 1024;
      else { int ft = 16 * ub + 4 * w + n; off = OFF_W1F + (((ft / 7) * 16 + ua) * 7 + ft % 7) * 1024; }
      *(u4v*)(ws + off + lane * 16) = __builtin_bit_cast(u4v, wf);
    }
  }
  {
    const int k1 = b & 7, kp = k1 <= 4 ? (k1 & 3) : 8 - k1, e = k1 >= 4;
    const int tl = e * 32 + ((b >> 3) & 3) * 8 + (b >> 5);
    u32 o[8];
#pragma unroll
    for (int i = 0; i < 8; ++i) o[i] = (u32)fr[i] | ((u32)fi[i] << 16);
    u4v* dst = (u4v*)(ws + OFF_WT) + (size_t)(kp * 64 + (t >> 2)) * 512 + (t & 3) * 64 + tl;
    dst[0] = u4v{o[0], o[1], o[2], o[3]};
    dst[256] = u4v{o[4], o[5], o[6], o[7]};
    auto cv = [](unsigned short v) { return __uint_as_float((u32)v << 16); };
    float h0r = 0.5f * (cv(pv[0]) + cv(pv[2])), h0i = 0.5f * (cv(pv[1]) - cv(pv[3]));
    float h1r = 0.5f * (cv(pv[4]) + cv(pv[6])), h1i = 0.5f * (cv(pv[5]) - cv(pv[7]));
    if (t < 8) ((f4*)(ws + OFF_PQ))[(kp * 64 + tl) * 8 + t] = f4{0.5f * (h0r + h1r), 0.5f * (h0i + h1i), 0.5f * (h0r - h1r), 0.5f * (h0i - h1i)};
  }
  STAMP(0, 1);
  u32 pf[6];
#pragma unroll
  for (int i = 0; i < 6; ++i) pf[i] = 0;
  asm volatile("" :: "v"(xin[0]), "v"(xin[1]), "v"(xin[2]), "v"(xin[3]), "v"(xin[4]), "v"(xin[5]), "v"(xin[6]), "v"(xin[7]), "v"(tw.w512),
               "v"(tw.tw1[1]), "v"(tw.tw1[3]), "v"(tw.tw1[5]), "v"(tw.tw1[7]), "v"(tw.tw2[2]), "v"(tw.tw2[4]), "v"(tw.tw2[6]));
  if (w == 3 && b < 64) {
    u32 alo, ahi, elo, ehi;
    asm volatile("s_getpc_b64 s[40:41]\n\ts_add_u32 %0, s40, k_c@rel32@lo+4\n\ts_addc_u32 %1, s41, k_c@rel32@hi+12" : "=s"(alo), "=s"(ahi) :: "s40", "s41", "scc");
    asm volatile("s_getpc_b64 s[40:41]\n\ts_add_u32 %0, s40, z_end@rel32@lo+4\n\ts_addc_u32 %1, s41, z_end@rel32@hi+12" : "=s"(elo), "=s"(ehi) :: "s40", "s41", "scc");
    const unsigned long long a = ((unsigned long long)ahi << 32) | alo, e = ((unsigned long long)ehi << 32) | elo;
    const u32 lim = e > a && e - a < 65536 ? (u32)(e - a) - 4 : 0u;
#pragma unroll
    for (int i = 0; i < 5; ++i) {
      const u32 o = (i * 64 + lane) * 128, oc = (o < lim ? o : lim) & ~3u;
      asm volatile("global_load_dword %0, %1, %2" : "=v"(pf[i]) : "v"(oc), "s"(a));
    }
    const char* tb = lane < 44 ? (const char*)&g_r2all + lane * 128 : lane < 60 ? (const char*)&g_twc1 + (lane - 44) * 128 : (const char*)&g_twc2 + (lane - 60) * 128;
    asm volatile("global_load_dword %0, %1, off" : "=v"(pf[5]) : "v"(tb));
  }
  __builtin_amdgcn_sched_barrier(0);
  r1_body(xin, tw, tx, (f2*)smem, ws, s2, t);
  asm volatile("s_waitcnt vmcnt(0)" :: "v"(pf[0]), "v"(pf[1]), "v"(pf[2]), "v"(pf[3]), "v"(pf[4]), "v"(pf[5]), "v"(pf0));
  STAMP(0, 2);
}

constexpr int PK = 292, QS = 72;
DI int idx1(int k1, int q, int j) { return k1 * PK + q * QS + (j ^ ((k1 >> 2) << 2)); }
template <int AUX> DI u4v bld4s(rsrc_t r, u32 off, u32 soff) { return __builtin_amdgcn_raw_buffer_load_b128(r, off, soff, AUX); }
template <int AUX> DI void bst4s(rsrc_t r, u32 off, u32 soff, u4v v) { __builtin_amdgcn_raw_buffer_store_b128(v, r, off + soff, 0, AUX); }
extern "C" __global__ __launch_bounds__(256) void k_c(char* __restrict__ ws, int first, int kid) {
  __shared__ f2 lds[8 * PK];
  STAMP(kid, 0);
  const rsrc_t rs = mk_rsrc(ws, WS_USED);
  const int b = blockIdx.x, qd = 8 * (b & 7) + ((b >> 3) & 7), kp = b >> 6;
  const int t = threadIdx.x, cc = t & 3, j = t >> 2;
  u4v in[4];
  const u32 tv = j * TPB + cc * 16, ts = OFF_T + kp * 4096 + qd * 64;
#pragma unroll
  for (int m = 0; m < 4; ++m) in[m] = bld4s<0>(rs, tv, ts + m * 64 * TPB);
  const u32 wsof = OFF_WT + (kp * 64 + qd) * 8192;
  u4v w0 = bld4s<0>(rs, t * 16, wsof), w1 = bld4s<0>(rs, t * 16 + 4096, wsof);
  f2 tc1[4], tc2[8];
#pragma unroll
  for (int q = 1; q < 4; ++q) tc1[q] = ldtw(&g_twc1.v[j * 4 + q]);
#pragma unroll
  for (int q = 0; q < 4; ++q) { f4 p = ((const f4*)&g_twc2)[8 * q + (t & 7)]; tc2[2 * q] = f2{p[0], p[1]}; tc2[2 * q + 1] = f2{p[2], p[3]}; }
  const bool special = qd == 0 && __builtin_amdgcn_readfirstlane(t >> 6) == 0;
  u4v pq[8];
  if (special) {
#pragma unroll
    for (int i = 0; i < 8; ++i) pq[i] = bld4s<0>(rs, t * 128 + i * 16, OFF_PQ + kp * 8192);
  } else {
#pragma unroll
    for (int i = 0; i < 8; ++i) asm volatile("" : "=v"(pq[i]));
  }
  ARM_A(KC_ARM);
#pragma unroll
  for (int e = 0; e < 2; ++e) {
    f2 a[4];
#pragma unroll
    for (int m = 0; m < 4; ++m) a[m] = e ? f2{__uint_as_float(in[m].z), __uint_as_float(in[m].w)} : f2{__uint_as_float(in[m].x), __uint_as_float(in[m].y)};
    fft4<false>(a[0], a[1], a[2], a[3]);
#pragma unroll
    for (int q = 1; q < 4; ++q) a[q] = cmul(a[q], tc1[q]);
#pragma unroll
    for (int q = 0; q < 4; ++q) lds[idx1(2 * cc + e, q, j)] = a[q];
  }
  bar();
  const int k1 = t >> 5, q = (t >> 3) & 3, jp = t & 7;
  const float sc = (float)kSc;
  f2 v[8];
#pragma unroll
  for (int m = 0; m < 8; ++m) v[m] = lds[idx1(k1, q, jp + 8 * m)];
  wbar();
  fft8<false>(v);
  v[0] = v[0] * f2{sc, sc};
#pragma unroll
  for (int i = 1; i < 8; ++i) v[i] = cmul(v[i], tc2[i]);
  f2* L2 = lds + k1 * PK + q * QS;
#pragma unroll
  for (int i = 0; i < 8; ++i) L2[jp * 9 + i] = v[i];
  wbar();
#pragma unroll
  for (int i = 0; i < 8; ++i) v[i] = L2[i * 9 + jp];
  wbar();
  fft8<false>(v);
  if (!special) {
    u32 w[8] = {w0.x, w0.y, w0.z, w0.w, w1.x, w1.y, w1.z, w1.w};
#pragma unroll
    for (int i = 0; i < 8; ++i) v[i] = cmul(v[i], unpk(w[i]));
  } else {
#pragma unroll
    for (int i = 0; i < 8; ++i) lds[k1 * PK + i * 32 + (t & 31)] = v[i];
    wbar();
    const bool z0 = kp == 0 && k1 == 0;
    const int u = q + 4 * jp, up = z0 ? (32 - u) & 31 : 31 - u;
    const int zi = (z0 && u == 0) ? 8 : 7;
    const f2* src = lds + (kp == 0 ? k1 : k1 ^ 1) * PK + (up & 3) * 8 + (up >> 2);
#pragma unroll
    for (int i = 0; i < 8; ++i) {
      f2 zm = src[((zi - i) & 7) * 32];
      f2 P = f2{__uint_as_float(pq[i].x), __uint_as_float(pq[i].y)}, Q = f2{__uint_as_float(pq[i].z), __uint_as_float(pq[i].w)};
      v[i] = ccmul_add(zm, Q, cmul(v[i], P));
    }
    wbar();
  }
  fft8<true>(v);
  v[0] = v[0] * f2{sc, sc};
#pragma unroll
  for (int i = 1; i < 8; ++i) v[i] = cmulc(v[i], tc2[i]);
#pragma unroll
  for (int i = 0; i < 8; ++i) L2[i * 9 + jp] = v[i];
  wbar();
#pragma unroll
  for (int i = 0; i < 8; ++i) v[i] = L2[jp * 9 + i];
  wbar();
  fft8<true>(v);
#pragma unroll
  for (int m = 0; m < 8; ++m) lds[idx1(k1, q, jp + 8 * m)] = v[m];
  bar();
  f2 o[2][4];
#pragma unroll
  for (int e = 0; e < 2; ++e) {
#pragma unroll
    for (int qq = 0; qq < 4; ++qq) o[e][qq] = lds[idx1(2 * cc + e, qq, j)];
#pragma unroll
    for (int qq = 1; qq < 4; ++qq) o[e][qq] = cmulc(o[e][qq], tc1[qq]);
    fft4<true>(o[e][0], o[e][1], o[e][2], o[e][3]);
  }
#pragma unroll
  for (int m = 0; m < 4; ++m) bst4s<0>(rs, tv, ts + m * 64 * TPB, f4u(o[0][m], o[1][m]));
  STAMP(kid, 1);
}

#define SB() __builtin_amdgcn_sched_barrier(0)
template <int N> DI void actN(const f2* a, const f2* bias, u32* o) {
  const float c0 = -2.0f * 0.7978845608028654f * 1.4426950408889634f, c1 = c0 * 0.044715f;
  f2 x[N], u[N];
#pragma unroll
  for (int i = 0; i < N; ++i) x[i] = rbf2(a[i]);
  SB();
#pragma unroll
  for (int i = 0; i < N; ++i) x[i] = x[i] + bias[i];
  SB();
#pragma unroll
  for (int i = 0; i < N; ++i) x[i] = rbf2(x[i]);
  SB();
#pragma unroll
  for (int i = 0; i < N; ++i) u[i] = x[i] * x[i];
  SB();
#pragma unroll
  for (int i = 0; i < N; ++i) u[i] = fma2(u[i], f2{c1, c1}, f2{c0, c0});
  SB();
#pragma unroll
  for (int i = 0; i < N; ++i) u[i] = u[i] * x[i];
  SB();
#pragma unroll
  for (int i = 0; i < N; ++i) u[i] = f2{__builtin_amdgcn_exp2f(u[i].x), __builtin_amdgcn_exp2f(u[i].y)};
  SB();
#pragma unroll
  for (int i = 0; i < N; ++i) u[i] = u[i] + f2{1.f, 1.f};
  SB();
#pragma unroll
  for (int i = 0; i < N; ++i) u[i] = f2{__builtin_amdgcn_rcpf(u[i].x), __builtin_amdgcn_rcpf(u[i].y)};
  SB();
#pragma unroll
  for (int i = 0; i < N; ++i) u[i] = u[i] * x[i];
  SB();
#pragma unroll
  for (int i = 0; i < N; ++i) o[i] = pack2(u[i]);
  SB();
#pragma unroll
  for (int i = 0; i < N; ++i) o[i] = nosent(o[i]);
  SB();
}

#define ALD4(dst, voff, sbase, imm) asm volatile("global_load_dwordx4 %0, %1, %2 offset:" #imm : "=v"(dst) : "v"(voff), "s"(sbase))
#define ALD1(dst, voff, sbase, imm) asm volatile("global_load_dword %0, %1, %2 offset:" #imm : "=v"(dst) : "v"(voff), "s"(sbase))
#define TSTEP(i)
#define NSLOW()
#define MEMF() asm volatile("" ::: "memory")
#define MX4F(m, a) do { asm volatile("v_max_u32 %0, %1, %2" : "=v"(m) : "v"((a).x), "v"((a).y)); \
    asm volatile("v_max3_u32 %0, %0, %1, %2" : "+v"(m) : "v"((a).z), "v"((a).w)); } while (0)
#define MX4(m, a) do { asm volatile("v_max3_u32 %0, %0, %1, %2" : "+v"(m) : "v"((a).x), "v"((a).y)); \
    asm volatile("v_max3_u32 %0, %0, %1, %2" : "+v"(m) : "v"((a).z), "v"((a).w)); } while (0)
constexpr int LDS_W1 = 114688;
constexpr int LDS_M = LDS_W1 + 49152;
constexpr int W2E = 7;
constexpr int PA = 6;
#define HPOS 3
#define MAUX 0
#define FAUX 0
#define PD 4

extern "C" __global__ __launch_bounds__(256, 1) void k_m(const u32* __restrict__ xres, const unsigned short* __restrict__ b1,
                                              const unsigned short* __restrict__ b2, char* ws, unsigned short* out, int last, int kid) {
  extern __shared__ __attribute__((aligned(16))) char smem[];
  STAMP(kid, 0);
  const int b = blockIdx.x, xcd = b & 7, ii = b >> 3;
  const int t = threadIdx.x, w = __builtin_amdgcn_readfirstlane(t >> 6), lane = t & 63, g = lane >> 4, c = lane & 15;
  const int s2 = 32 * xcd + ii, rt = 2 * xcd + (ii >> 4), ct = ii & 15, rt2 = 4 * xcd + (ii >> 3), ct2 = ii & 7;
  const rsrc_t rs = mk_rsrc(ws, WS_USED);
  f2* lds = (f2*)(smem + LDS_W1);
  u4v w2f[14][4];
  f16v tx;
  int budget = 1 << 16;
  const u32 l16 = lane * 16;
  const int tt0 = 8 * rt + 2 * w, kb = 7 * (ct >> 1) + 3 * (ct & 1);
  const int rho2 = 64 * rt2 + 16 * w + c;
  const u32 ol = last ? OFF_LNF2 : OFF_LNF, oh = last ? OFF_H2 : OFF_H;
  const u32 cl = last ? 0x7f000000u : OFF_LNF2, ch = last ? 0x7f000000u : OFF_H2;
  const u32 lno = ((s2 >> 1) * 16 + 4 * w) * 1024 + (lane >> 3) * 256 + (s2 & 1) * 128 + (lane & 7) * 16;
  const u32 hst = (tt0 * 56 + kb) * 1024 + l16;
  const u32 htl = (((tt0 + (g & 1)) * 56 + 7 * (ct >> 1) + 6) * 64 + (2 * (ct & 1) + (g >> 1)) * 16 + c) * 16;
  const int rot = ct2 & 3;
  int kq[4];
#pragma unroll
  for (int q = 0; q < 4; ++q) { const int pr = (q + rot) & 3; kq[q] = 14 * w + 7 * (pr >> 1) + 3 * (pr & 1); }
  const int kt0 = 14 * w + 6 + 7 * (ct2 & 1), kt1 = 14 * w + 13 - 7 * (ct2 & 1);
#define KB(n) ((n) < 12 ? kq[(n) & 3] : ((n) == 12 ? kt0 : kt1))
#define KI(n) ((n) < 12 ? (n) >> 2 : 0)
  const u32 w2s = OFF_W2F + ct2 * 56 * 4096;
#define W2LD1(n, i) w2f[n][i] = bld4s<0>(rs, l16 + (i) * 1024, w2s + (KB(n) + KI(n)) * 4096)
#define W2LD(n) do { _Pragma("unroll") for (int i_ = 0; i_ < 4; ++i_) W2LD1(n, i_); } while (0)
  u2v bias[7];
  {
    const int s1 = t >> 5, j = t & 31;
    u4v in[4], tq[11];
    u32 xin[8];
    {
      const char* tb = ws + OFF_T + (size_t)s2 * TPB;
      const u32 t16 = t * 16;
      ALD4(in[0], t16, tb, 0);
      ALD4(in[1], t16, tb + 4096, 0);
      ALD4(in[2], t16, tb + 8192, 0);
      ALD4(in[3], t16, tb + 12288, 0);
      const u32 jo = j * 16;
      const char* tp = (const char*)&g_r2all;
      ALD4(tq[0], jo, tp, 0); ALD4(tq[1], jo, tp, 512); ALD4(tq[2], jo, tp, 1024); ALD4(tq[3], jo, tp, 1536);
      ALD4(tq[4], jo, tp, 2048); ALD4(tq[5], jo, tp, 2560); ALD4(tq[6], jo, tp, 3072); ALD4(tq[7], jo, tp, 3584);
      ALD4(tq[8], jo, tp + 4096, 0); ALD4(tq[9], jo, tp + 4096, 512); ALD4(tq[10], jo, tp + 4096, 1024);
      const u32 xof = s1 * (256 * 1024) + j * 4;
      const u32* xb = xres + s2 * 256;
      ALD1(xin[0], xof, xb, 0); ALD1(xin[1], xof, xb, 128); ALD1(xin[2], xof, xb, 256); ALD1(xin[3], xof, xb, 384);
      ALD1(xin[4], xof, xb, 512); ALD1(xin[5], xof, xb, 640); ALD1(xin[6], xof, xb, 768); ALD1(xin[7], xof, xb, 896);
    }
    SB();
    tx = *(const f16v*)&g_twx.v[s2 * 8];
    SB();
    const char* gsrc = ws + OFF_W1F + (size_t)ct * LDS_W1 + l16;
#define W1DMA(pa, pb) do { SB(); _Pragma("unroll") for (int p_ = (pa); p_ < (pb); ++p_) { \
      const int sl_ = p_ / 7, pc_ = p_ % 7; \
      const char* gp_ = gsrc + ((4 * w + sl_ + ct) & 15) * 7168 + (pc_ & 4) * 1024; \
      char* lp_ = smem + (4 * w + sl_) * 7168 + (pc_ & 4) * 1024; \
      if ((pc_ & 3) == 0) __builtin_amdgcn_global_load_lds((const void*)gp_, (LAS void*)lp_, 16, 0, 0); \
      if ((pc_ & 3) == 1) __builtin_amdgcn_global_load_lds((const void*)gp_, (LAS void*)lp_, 16, 1024, 0); \
      if ((pc_ & 3) == 2) __builtin_amdgcn_global_load_lds((const void*)gp_, (LAS void*)lp_, 16, 2048, 0); \
      if ((pc_ & 3) == 3) __builtin_amdgcn_global_load_lds((const void*)gp_, (LAS void*)lp_, 16, 3072, 0); } SB(); } while (0)
    W1DMA(0, 4);
    {
      const u4v sv = u4v{SENT, SENT, SENT, SENT};
#pragma unroll
      for (int q = 0; q < 6; ++q) bst4<MAUX>(rs, ch + hst + (q & 1) * 57344 + (q >> 1) * 1024, sv);
      bst4<MAUX>(rs, ch + htl, sv);
      bst4<MAUX>(rs, cl + lno, sv);
      bst4<MAUX>(rs, cl + lno + 2048, sv);
      SB();
    }
    asm volatile("s_waitcnt vmcnt(32)" : "+v"(in[0]), "+v"(in[1]), "+v"(in[2]), "+v"(in[3]));
    STAMP(kid, 1);
    f2 X[8];
    {
      auto lo = [](u4v v) { return f2{__uint_as_float(v.x), __uint_as_float(v.y)}; };
      auto hi = [](u4v v) { return f2{__uint_as_float(v.z), __uint_as_float(v.w)}; };
      X[0] = lo(in[0]); X[4] = hi(in[0]); X[1] = lo(in[1]); X[7] = hi(in[1]);
      X[2] = lo(in[2]); X[6] = hi(in[2]); X[3] = lo(in[3]); X[5] = hi(in[3]);
    }
#pragma unroll
    for (int k = 1; k < 8; ++k) X[k] = cmulc_s(X[k], f2{tx[2 * k], tx[2 * k + 1]});
    fft8<true>(X);
#pragma unroll
    for (int r = 0; r < 8; ++r) lds[r * RP + t] = X[r];
    W1DMA(4, 8);
    asm volatile("s_waitcnt vmcnt(17)" : "+v"(tq[0]), "+v"(tq[1]), "+v"(tq[2]), "+v"(tq[3]), "+v"(tq[4]), "+v"(tq[5]), "+v"(tq[6]), "+v"(tq[7]),
                 "+v"(tq[8]), "+v"(tq[9]), "+v"(tq[10]), "+v"(xin[0]), "+v"(xin[1]), "+v"(xin[2]), "+v"(xin[3]), "+v"(xin[4]), "+v"(xin[5]),
                 "+v"(xin[6]), "+v"(xin[7]));
    f2 tw1[8], twu[8], tw2[2][4];
    {
      f2 tv[22];
#pragma unroll
      for (int i = 0; i < 11; ++i) {
        tv[2 * i] = f2{__uint_as_float(tq[i].x), __uint_as_float(tq[i].y)};
        tv[2 * i + 1] = f2{__uint_as_float(tq[i].z), __uint_as_float(tq[i].w)};
      }
#pragma unroll
      for (int i = 0; i < 8; ++i) twu[i] = tv[i];
#pragma unroll
      for (int a = 0; a < 2; ++a)
#pragma unroll
        for (int i = 1; i < 4; ++i) tw2[a][i] = tv[8 + 3 * a + (i - 1)];
#pragma unroll
      for (int q = 1; q < 8; ++q) tw1[q] = tv[13 + q];
    }
    bar();
    f2* L = lds + s1 * RP;
    f2 z[8];
#pragma unroll
    for (int i = 0; i < 8; ++i) {
      int k = j + 32 * (i >> 2) + 64 * (i & 3);
      f2 A = L[k], Bc = L[(256 - k) & 255];
      f2 E = fma2(Bc, f2{1.f, -1.f}, A);
      f2 Dm = fma2(Bc, f2{-1.f, 1.f}, A);
      f2 zz = cmulc_add(Dm, twu[i], E);
      if (i == 0 && j == 0) zz = f2{A.x + A.y, A.x - A.y};
      z[i] = zz;
    }
    wbar();
    W1DMA(8, 12);
    fft4<true>(z[0], z[1], z[2], z[3]);
    fft4<true>(z[4], z[5], z[6], z[7]);
#pragma unroll
    for (int a = 0; a < 2; ++a)
#pragma unroll
      for (int i = 1; i < 4; ++i) z[4 * a + i] = cmulc(z[4 * a + i], tw2[a][i]);
#pragma unroll
    for (int i = 0; i < 8; ++i) L[(i & 3) * 72 + 32 * (i >> 2) + j] = z[i];
    wbar();
#pragma unroll
    for (int q = 0; q < 8; ++q) z[q] = L[(j & 3) * 72 + 8 * q + (j >> 2)];
    W1DMA(12, 16);
    fft8<true>(z);
#pragma unroll
    for (int m = 0; m < 8; ++m) L[(j >> 2) * 36 + (j & 3) + 4 * m] = z[m];
    wbar();
#pragma unroll
    for (int q = 0; q < 8; ++q) z[q] = L[q * 36 + j];
    W1DMA(16, 22);
#pragma unroll
    for (int q = 1; q < 8; ++q) z[q] = cmulc(z[q], tw1[q]);
    fft8<true>(z);
    W1DMA(22, 28);
#pragma unroll
    for (int jt = 0; jt < 7; ++jt) bias[jt] = *(const u2v*)(b1 + ct * 112 + 16 * jt + 4 * g);
    SB();
    u32 x1[8], o[8];
#pragma unroll
    for (int m = 0; m < 8; ++m) { x1[m] = pack2(unpk(xin[m]) + rbf2(z[m])); z[m] = unpk(x1[m]); }
    const int rho = 8 * s2 + s1;
    u32* Lw = (u32*)L;
#pragma unroll
    for (int m = 0; m < 8; ++m) Lw[32 * m + j] = x1[m];
    wbar();
    u4v xa = *(const u4v*)(Lw + 4 * j), xb = *(const u4v*)(Lw + 128 + 4 * j);
    layernorm8(z, o);
#pragma unroll
    for (int m = 0; m < 8; ++m) o[m] = nosent(o[m]);
    char* sg = smem + LDS_W1 + 20480;
    *(u4v*)(sg + j * 288 + s1 * 16) = u4v{o[0], o[1], o[2], o[3]};
    *(u4v*)(sg + j * 288 + 144 + s1 * 16) = u4v{o[4], o[5], o[6], o[7]};
    bar();
    const char* Lr = sg + (16 * w + (lane >> 3)) * 144 + (lane & 7) * 16;
    const u4v fa = *(const u4v*)Lr, fb = *(const u4v*)(Lr + 8 * 144);
    const u32 lo = ol + lno;
    SB();
    bst4<16>(rs, lo, fa);
    bst4<16>(rs, lo + 2048, fb);
    SB();
    bst4<16>(rs, OFF_X1P + rho * 1024 + j * 16, xa);
    bst4<16>(rs, OFF_X1P + rho * 1024 + j * 16 + 512, xb);
  }
  STAMP(kid, 2);
  u4v hf[14][4];
  u32 hoff[4];
#pragma unroll
  for (int h = 0; h < 4; ++h) hoff[h] = oh + (4 * rt2 + ((w + h) & 3)) * 56 * 1024;
#define HFLDX(n, A) do { _Pragma("unroll") for (int h_ = 0; h_ < 4; ++h_) hf[n][h_] = bld4s<A>(rs, l16 + KI(n) * 1024, hoff[h_] + KB(n) * 1024); } while (0)
#define HFLD(n) HFLDX(n, 16)
#define HFLDF(n) HFLDX(n, FAUX)
#define HFMX(n) mx4(mx4(mx4(mx4(0, hf[n][0]), hf[n][1]), hf[n][2]), hf[n][3])
#define CMPS(bad, m) asm volatile("v_cmp_eq_u32 %0, -1, %1" : "=s"(bad) : "v"(m))
  {
    u4v av[2][16];
#define AVLDX(ks, A) do { _Pragma("unroll") for (int h_ = 0; h_ < 2; ++h_) av[h_][ks] = bld4s<A>(rs, l16 + (((ks) + ct) & 15) * 1024, ol + (tt0 + h_) * 16384); } while (0)
#define AVLD(ks) AVLDX(ks, 16)
    SB();
    wait_vm0();
    __builtin_amdgcn_s_barrier();
    MEMF();
    STAMP(kid, 3);
    f4 acc[7][2];
    u32 o0[8];
#define GELU8(p, o) do { f2 a_[8], bs_[8]; \
      _Pragma("unroll") for (int h = 0; h < 2; ++h) { \
        f4 a0 = acc[2 * (p)][h], a1 = acc[2 * (p) + 1][h]; \
        a_[4 * h] = f2{a0[0], a0[1]}; a_[4 * h + 1] = f2{a0[2], a0[3]}; a_[4 * h + 2] = f2{a1[0], a1[1]}; a_[4 * h + 3] = f2{a1[2], a1[3]}; \
        bs_[4 * h] = unpk(bias[2 * (p)].x); bs_[4 * h + 1] = unpk(bias[2 * (p)].y); bs_[4 * h + 2] = unpk(bias[2 * (p) + 1].x); bs_[4 * h + 3] = unpk(bias[2 * (p) + 1].y); } \
      SB(); W2LD(W2E + 2 * (p)); W2LD(W2E + 2 * (p) + 1); \
      if ((p) == HPOS) { _Pragma("unroll") for (int n = 0; n < PD; ++n) HFLDF(n); } \
      SB(); actN<8>(a_, bs_, o); } while (0)
#define HST8(p, o) do { _Pragma("unroll") for (int h = 0; h < 2; ++h) \
        bst4<16>(rs, oh + hst + h * 57344 + (p) * 1024, u4v{o[4 * h], o[4 * h + 1], o[4 * h + 2], o[4 * h + 3]}); } while (0)
    for (int redo = 0;; ++redo) {
      do {
        MEMF();
#pragma unroll
        for (int ks = 0; ks < PA; ++ks) AVLD(ks);
        u32 m = 0;
#pragma unroll
        for (int ks = 0; ks < PA; ++ks) { m = mx4(m, av[0][ks]); m = mx4(m, av[1][ks]); }
        if (!anysent(m)) break;
      } while (--budget > 0);
      STAMP(kid, 4);
      TSTEP(31);
      SB();
#pragma unroll
      for (int jt = 0; jt < 7; ++jt) { acc[jt][0] = f4{0, 0, 0, 0}; acc[jt][1] = f4{0, 0, 0, 0}; }
      const char* lb = smem + l16;
      u4v wf[2][7];
#pragma unroll
      for (int jt = 0; jt < 7; ++jt) wf[0][jt] = *(const u4v*)(lb + jt * 1024);
      SB();
#pragma unroll
      for (int ks = 0; ks < 16; ++ks) {
        const int cu = ks & 1, nx = cu ^ 1;
        const bool chk = ks + 1 >= PA && ks < 15;
        u32 m = 0;
        unsigned long long bad = 0;
#pragma unroll
        for (int jt = 0; jt < 7; ++jt) {
          bf8v wfb = __builtin_bit_cast(bf8v, wf[cu][jt]);
          acc[jt][0] = __builtin_amdgcn_mfma_f32_16x16x32_bf16(wfb, __builtin_bit_cast(bf8v, av[0][ks]), acc[jt][0], 0, 0, 0);
          SB();
          if (ks < 15) wf[nx][jt] = *(const u4v*)(lb + ((ks + 1) * 7 + jt) * 1024);
          SB();
          acc[jt][1] = __builtin_amdgcn_mfma_f32_16x16x32_bf16(wfb, __builtin_bit_cast(bf8v, av[1][ks]), acc[jt][1], 0, 0, 0);
          SB();
          if (ks + PA < 16 && jt < 2) av[jt][ks + PA] = bld4s<FAUX>(rs, l16 + ((ks + PA + ct) & 15) * 1024, ol + (tt0 + jt) * 16384);
          if (ks >= 16 - W2E && jt < 4) W2LD1(ks - (16 - W2E), jt);
          if (chk && jt == 4) asm volatile("v_max_u32 %0, %1, %2" : "=v"(m) : "v"(av[0][ks + 1].x), "v"(av[1][ks + 1].x));
          if (chk && jt == 5) CMPS(bad, m);
          SB();
        }
        TSTEP(ks);
        if (chk) {
          MEMF();
          if (__builtin_expect(bad != 0, 0)) {
            NSLOW();
            do {
              MEMF();
#pragma unroll
              for (int k2 = ks + 1; k2 < 16 && k2 <= ks + PA; ++k2) AVLD(k2);
              wait_vm0();
            } while (anysent(mx4(mx4(0, av[0][ks + 1]), av[1][ks + 1])) && --budget > 0);
          }
          SB();
        }
      }
      STAMP(kid, 5);
      const bool nan = __builtin_amdgcn_ballot_w64(__builtin_isunordered(acc[0][0][0], acc[0][1][0])) != 0;
      GELU8(0, o0);
      MEMF();
      if (__builtin_expect(!nan || redo >= 3, 1)) break;
    }
    HST8(0, o0);
    {
      u32 o[8];
      GELU8(1, o);
      HST8(1, o);
      GELU8(2, o);
      HST8(2, o);
    }
    {
      f4 a0 = acc[6][0], a1 = acc[6][1];
      f2 a[4] = {f2{a0[0], a0[1]}, f2{a0[2], a0[3]}, f2{a1[0], a1[1]}, f2{a1[2], a1[3]}};
      f2 bs[4] = {unpk(bias[6].x), unpk(bias[6].y), unpk(bias[6].x), unpk(bias[6].y)};
      u32 o4[4];
      SB();
      W2LD(13);
      if (HPOS == 3) {
#pragma unroll
        for (int n = 0; n < PD; ++n) HFLDF(n);
      }
      SB();
      actN<4>(a, bs, o4);
      auto rx = __builtin_amdgcn_permlane16_swap(o4[0], o4[2], false, false);
      auto ry = __builtin_amdgcn_permlane16_swap(o4[1], o4[3], false, false);
      bst4<16>(rs, oh + htl, u4v{rx[0], ry[0], rx[1], ry[1]});
    }
  }
  STAMP(kid, 6);
  R1Tw tw;
  {
    f4 acc[4][4];
    u4v bb0 = *(const u4v*)(b2 + 64 * ct2 + 16 * g), bb1 = *(const u4v*)(b2 + 64 * ct2 + 16 * g + 8);
    const u32 pvo = oh + (((4 * rt2 + w) * 56 + 7 * (4 * (rt2 & 1) + w) + 3 * (lane >> 5)) * 64 + (lane & 31)) * 16;
    u32 pv = 0;
    u4v x1a, x1b;
    f4* red = (f4*)(smem + LDS_W1);
    for (int redo = 0;; ++redo) {
      SB();
      MEMF();
      if (__builtin_expect(redo != 0, 0)) {
#pragma unroll
        for (int n = 0; n < 14; ++n) W2LD(n);
#pragma unroll
        for (int n = 0; n < PD; ++n) HFLD(n);
        wait_vm0();
      }
      if (anysent(HFMX(0))) {
        do {
          MEMF();
#pragma unroll
          for (int n = 0; n < PD; ++n) HFLD(n);
          wait_vm0();
        } while (anysent(HFMX(0)) && --budget > 0);
      }
#pragma unroll
      for (int i = 0; i < 4; ++i)
#pragma unroll
        for (int h = 0; h < 4; ++h) acc[i][h] = f4{0, 0, 0, 0};
      STAMP(kid, 8);
      TSTEP(30);
      SB();
#pragma unroll
      for (int n = 0; n < 14; ++n) {
        u32 m = 0;
        unsigned long long bad = 0;
#pragma unroll
        for (int q = 0; q < 8; ++q) {
          const int h = q >> 1, i = 2 * (q & 1);
          acc[i][h] = __builtin_amdgcn_mfma_f32_16x16x32_bf16(__builtin_bit_cast(bf8v, w2f[n][i]), __builtin_bit_cast(bf8v, hf[n][h]), acc[i][h], 0, 0, 0);
          SB();
          if (n + PD < 14 && q < 4) hf[n + PD][q] = bld4s<FAUX>(rs, l16 + KI(n + PD) * 1024, hoff[q] + KB(n + PD) * 1024);
          if (n == 4 && q == 4) pv = bld1<16>(rs, pvo);
          if (n < 13 && q == 5) asm volatile("v_max3_u32 %0, %1, %2, %3" : "=v"(m) : "v"(hf[n + 1][0].x), "v"(hf[n + 1][1].x), "v"(hf[n + 1][2].x));
          if (n < 13 && q == 6) CMPS(bad, m);
          SB();
          acc[i + 1][h] = __builtin_amdgcn_mfma_f32_16x16x32_bf16(__builtin_bit_cast(bf8v, w2f[n][i + 1]), __builtin_bit_cast(bf8v, hf[n][h]), acc[i + 1][h], 0, 0, 0);
          SB();
          if (n < 13 && n != 8 && q == 5) asm volatile("v_max_u32 %0, %0, %1" : "+v"(m) : "v"(hf[n + 1][3].x));
          if (n == 8 && q == 5) asm volatile("v_max3_u32 %0, %0, %1, %2" : "+v"(m) : "v"(hf[n + 1][3].x), "v"(pv));
          SB();
        }
        TSTEP(16 + n);
        if (n < 13) {
          MEMF();
          if (__builtin_expect(bad != 0, 0)) {
            NSLOW();
            do {
              MEMF();
#pragma unroll
              for (int n2 = n + 1; n2 < 14 && n2 <= n + PD; ++n2) HFLD(n2);
              if (n == 8) pv = bld1<16>(rs, pvo);
              wait_vm0();
              m = HFMX(n + 1);
              if (n == 8) m = mx2(m, pv);
            } while (anysent(m) && --budget > 0);
          }
          SB();
        }
        if (n == 8) {
          MEMF();
          x1a = bld4<16>(rs, OFF_X1P + rho2 * 1024 + ct2 * 128 + g * 32);
          x1b = bld4<16>(rs, OFF_X1P + rho2 * 1024 + ct2 * 128 + g * 32 + 16);
          SB();
        }
      }
      STAMP(kid, 9);
      r1_load_tw(tw, t);
      SB();
      const bool nan = __builtin_amdgcn_ballot_w64(__builtin_isunordered(acc[0][0][0], acc[0][1][0]) || __builtin_isunordered(acc[0][2][0], acc[0][3][0])) != 0;
#pragma unroll
      for (int h = 1; h < 4; ++h)
#pragma unroll
        for (int i = 0; i < 4; ++i) red[((((w + h) & 3) * 3 + (h - 1)) * 4 + i) * 64 + lane] = acc[i][h];
      MEMF();
      if (__builtin_expect(!nan || redo >= 3, 1)) break;
    }
    bar();
    f2 s[8];
#pragma unroll
    for (int i = 0; i < 4; ++i) {
      f4 a = (acc[i][0] + red[((w * 3 + 0) * 4 + i) * 64 + lane]) + (red[((w * 3 + 1) * 4 + i) * 64 + lane] + red[((w * 3 + 2) * 4 + i) * 64 + lane]);
      s[2 * i] = f2{a[0], a[1]}; s[2 * i + 1] = f2{a[2], a[3]};
    }
    const u32 x1v[8] = {x1a.x, x1a.y, x1a.z, x1a.w, x1b.x, x1b.y, x1b.z, x1b.w};
    u32 bs[8] = {bb0.x, bb0.y, bb0.z, bb0.w, bb1.x, bb1.y, bb1.z, bb1.w};
    u32 o[8];
#pragma unroll
    for (int i = 0; i < 8; ++i) o[i] = pack2(unpk(x1v[i]) + rbf2(rbf2(s[i]) + unpk(bs[i])));
#pragma unroll
    for (int i = 0; i < 8; ++i) o[i] = nosent(o[i]);
    char* st = smem + LDS_W1 + w * 12288;
    wbar();
    *(u4v*)(st + c * 144 + g * 32) = u4v{o[0], o[1], o[2], o[3]};
    *(u4v*)(st + c * 144 + g * 32 + 16) = u4v{o[4], o[5], o[6], o[7]};
    wbar();
    const u4v ra = *(const u4v*)(st + (lane >> 3) * 144 + (lane & 7) * 16), rb = *(const u4v*)(st + ((lane >> 3) + 8) * 144 + (lane & 7) * 16);
    const u32 xo = ((256 * (lane >> 3) + 8 * rt2 + 2 * w) * D_ + 64 * ct2) * 2 + (lane & 7) * 16;
    if (last) {
      *(u4v*)((char*)out + xo) = ra;
      *(u4v*)((char*)out + xo + 1024) = rb;
      STAMP(kid, 10);
      return;
    }
    bst4<16>(rs, OFF_XR + xo, ra);
    bst4<16>(rs, OFF_XR + xo + 1024, rb);
  }
  STAMP(kid, 10);
  {
    u32 xin[8];
    const u32 ro = OFF_XR + ((256 * (t >> 5) + s2) * 256 + (t & 31)) * 4;
    wait_vm0();
    do {
      MEMF();
#pragma unroll
      for (int m = 0; m < 8; ++m) xin[m] = bld1<16>(rs, ro + 128 * m);
    } while (anysent(mx2(mx2(mx2(xin[0], xin[1]), mx2(xin[2], xin[3])), mx2(mx2(xin[4], xin[5]), mx2(xin[6], xin[7])))) && --budget > 0);
    STAMP(kid, 11);
    r1_body(xin, tw, tx, (f2*)smem, ws, s2, t);
    STAMP(kid, 12);
  }
}

extern "C" __device__ __attribute__((used, noinline)) void z_end() {}

extern "C" void kernel_launch(void* const* d_in, const int* in_sizes, int n_in, void* d_out, int out_size, void* d_ws,
                              size_t ws_size, hipStream_t stream) {
  const unsigned short* x = (const unsigned short*)d_in[0];
  const unsigned short* wr = (const unsigned short*)d_in[1];
  const unsigned short* wi = (const unsigned short*)d_in[2];
  const unsigned short* w1 = (const unsigned short*)d_in[4];
  const unsigned short* b1 = (const unsigned short*)d_in[5];
  const unsigned short* w2 = (const unsigned short*)d_in[6];
  const unsigned short* b2 = (const unsigned short*)d_in[7];
  char* ws = (char*)d_ws;
  (void)hipFuncSetAttribute((const void*)k_m, hipFuncAttributeMaxDynamicSharedMemorySize, LDS_M);
  k_first<<<256, 256, 0, stream>>>((const u32*)x, wr, wi, w1, w2, ws);
  k_c<<<256, 256, 0, stream>>>(ws, 1, 1);
  k_m<<<256, 256, LDS_M, stream>>>((const u32*)x, b1, b2, ws, (unsigned short*)d_out, 0, 2);
  k_c<<<256, 256, 0, stream>>>(ws, 0, 3);
  k_m<<<256, 256, LDS_M, stream>>>((const u32*)(ws + OFF_XR), b1, b2, ws, (unsigned short*)d_out, 1, 4);
}
```

```cpp
#include <hip/hip_runtime.h>
#include <hip/hip_bf16.h>
#include <stdint.h>

typedef unsigned int u32;
typedef float f2 __attribute__((ext_vector_type(2)));
typedef float f4 __attribute__((ext_vector_type(4)));
typedef float f16v __attribute__((ext_vector_type(16)));
typedef u32 u2v __attribute__((ext_vector_type(2)));
typedef u32 u4v __attribute__((ext_vector_type(4)));
typedef __bf16 bf2v __attribute__((ext_vector_type(2)));
typedef __bf16 bf8v __attribute__((ext_vector_type(8)));
typedef short s4v __attribute__((ext_vector_type(4)));
typedef short s8v __attribute__((ext_vector_type(8)));
#define LAS __attribute__((address_space(3)))
#define DI __device__ __forceinline__
#define RLX_AGENT __ATOMIC_RELAXED, __HIP_MEMORY_SCOPE_AGENT

#define STAMP(K, I)

constexpr int S_ = 2048, D_ = 512, F_ = 1792, DF_ = 257;
constexpr u32 TPB = 16384 + 256;

constexpr u32 OFF_CNT = 0;
constexpr u32 OFF_T = 65536;
constexpr u32 SZ_T = 256u * TPB;
constexpr u32 OFF_WT = OFF_T + SZ_T;
constexpr u32 SZ_WT = 256u * 256 * 8 * 4;
constexpr u32 OFF_PQ = OFF_WT + SZ_WT;
constexpr u32 SZ_PQ = 256u * 8 * 16;
constexpr u32 OFF_W2F = OFF_PQ + SZ_PQ;
constexpr u32 SZ_W2F = (u32)F_ * D_ * 2;
constexpr u32 OFF_W1F = OFF_W2F + SZ_W2F;
constexpr u32 OFF_XR = OFF_W1F + SZ_W2F;
constexpr u32 SZ_X = (u32)S_ * D_ * 2;
constexpr u32 OFF_X1P = OFF_XR + SZ_X;
constexpr u32 OFF_LNF = OFF_X1P + SZ_X;
constexpr u32 OFF_H = OFF_LNF + SZ_X;
constexpr u32 SZ_H = (u32)S_ * F_ * 2;
constexpr u32 OFF_LNF2 = OFF_H + SZ_H;
constexpr u32 OFF_H2 = OFF_LNF2 + SZ_X;
constexpr u32 WS_USED = OFF_H2 + SZ_H;

struct cxf { float x, y; };
constexpr double kPi = 3.14159265358979323846264338327950288;
constexpr double c_sin(double x) { double x2 = x * x, t = x, s = x; for (int i = 1; i < 11; ++i) { t *= -x2 / double((2 * i) * (2 * i + 1)); s += t; } return s; }
constexpr double c_cos(double x) { double x2 = x * x, t = 1, s = 1; for (int i = 1; i < 11; ++i) { t *= -x2 / double((2 * i - 1) * (2 * i)); s += t; } return s; }
constexpr cxf tw2048(int i) {
  i &= 2047;
  int qd = i >> 9, r = i & 511;
  double c = 0, s = 0;
  if (r <= 256) { double a = 2 * kPi * r / 2048.0; c = c_cos(a); s = c_sin(a); }
  else { double a = 2 * kPi * (512 - r) / 2048.0; c = c_sin(a); s = c_cos(a); }
  double cc = 0, ss = 0;
  if (qd == 0) { cc = c; ss = s; } else if (qd == 1) { cc = -s; ss = c; } else if (qd == 2) { cc = -c; ss = -s; } else { cc = s; ss = -c; }
  return cxf{(float)cc, (float)(-ss)};
}
template <int R, int C> struct Tab { cxf v[R * C]; };
constexpr Tab<32, 8> mk_r1() { Tab<32, 8> t{}; for (int j = 0; j < 32; ++j) for (int q = 0; q < 8; ++q) t.v[j * 8 + q] = tw2048(8 * j * q); return t; }
constexpr Tab<8, 8> mk_r2() { Tab<8, 8> t{}; for (int j = 0; j < 8; ++j) for (int q = 0; q < 8; ++q) t.v[j * 8 + q] = tw2048(64 * j * q); return t; }
constexpr Tab<256, 1> mk_512() { Tab<256, 1> t{}; for (int c = 0; c < 256; ++c) t.v[c] = tw2048(4 * c + 512); return t; }
constexpr Tab<32, 8> mk_512u() { Tab<32, 8> t{}; for (int u = 0; u < 32; ++u) for (int i = 0; i < 8; ++i) t.v[u * 8 + i] = tw2048(4 * (u + 32 * (i >> 2) + 64 * (i & 3)) + 512); return t; }
constexpr Tab<32, 22> mk_r2all() {
  Tab<32, 22> t{};
  for (int j = 0; j < 32; ++j) {
    cxf v[22] = {};
    for (int i = 0; i < 8; ++i) v[i] = tw2048(4 * (j + 32 * (i >> 2) + 64 * (i & 3)) + 512);
    for (int a = 0; a < 2; ++a) for (int i = 1; i < 4; ++i) v[8 + 3 * a + (i - 1)] = tw2048(64 * ((j >> 3) + 4 * a) * i);
    for (int q = 1; q < 8; ++q) v[13 + q] = tw2048(8 * j * q);
    for (int i = 0; i < 22; ++i) t.v[((i >> 1) * 32 + j) * 2 + (i & 1)] = v[i];
  }
  return t;
}
constexpr Tab<32, 14> mk_r1all() {
  Tab<32, 14> t{};
  for (int j = 0; j < 32; ++j)
    for (int i = 0; i < 14; ++i) t.v[((i >> 1) * 32 + j) * 2 + (i & 1)] = i < 7 ? tw2048(8 * j * (i + 1)) : tw2048(64 * (j & 3) * (i - 6));
  return t;
}
constexpr Tab<256, 8> mk_x() { Tab<256, 8> t{}; for (int s = 0; s < 256; ++s) for (int k = 0; k < 8; ++k) t.v[s * 8 + k] = tw2048(s * k); return t; }
constexpr Tab<64, 4> mk_c1() { Tab<64, 4> t{}; for (int j = 0; j < 64; ++j) for (int q = 0; q < 4; ++q) t.v[j * 4 + q] = tw2048(8 * j * q); return t; }
constexpr double kSc = 6.9053396600248785e-04;
constexpr cxf tw2048s(int i) {
  i &= 2047;
  int qd = i >> 9, r = i & 511;
  double c = 0, s = 0;
  if (r <= 256) { double a = 2 * kPi * r / 2048.0; c = c_cos(a); s = c_sin(a); }
  else { double a = 2 * kPi * (512 - r) / 2048.0; c = c_sin(a); s = c_cos(a); }
  double cc = 0, ss = 0;
  if (qd == 0) { cc = c; ss = s; } else if (qd == 1) { cc = -s; ss = c; } else if (qd == 2) { cc = -c; ss = -s; } else { cc = s; ss = -c; }
  return cxf{(float)(cc * kSc), (float)(-ss * kSc)};
}
constexpr Tab<8, 8> mk_c2() { Tab<8, 8> t{}; for (int j = 0; j < 8; ++j) for (int q = 0; q < 8; ++q) t.v[((q >> 1) * 8 + j) * 2 + (q & 1)] = tw2048s(32 * j * q); return t; }

__device__ const __attribute__((aligned(64))) Tab<32, 14> g_r1all = mk_r1all();
__device__ const __attribute__((aligned(64))) Tab<256, 1> g_tw512 = mk_512();
__device__ const __attribute__((aligned(64))) Tab<32, 22> g_r2all = mk_r2all();
__device__ const __attribute__((aligned(64))) Tab<256, 8> g_twx = mk_x();
__device__ const __attribute__((aligned(64))) Tab<64, 4> g_twc1 = mk_c1();
__device__ const __attribute__((aligned(64))) Tab<8, 8> g_twc2 = mk_c2();

DI float bf_lo(u32 v) { return __uint_as_float(v << 16); }
DI float bf_hi(u32 v) { return __uint_as_float(v & 0xffff0000u); }
DI f2 unpk(u32 v) { return f2{bf_lo(v), bf_hi(v)}; }
DI float rbf(float x) { return (float)(__bf16)x; }
DI u32 pack2(f2 a) { bf2v v = __builtin_convertvector(a, bf2v); return __builtin_bit_cast(u32, v); }
DI f2 rbf2(f2 a) { return unpk(pack2(a)); }
DI f2 swp(f2 a) { return __builtin_shufflevector(a, a, 1, 0); }
DI f2 fma2(f2 a, f2 b, f2 c) { return __builtin_elementwise_fma(a, b, c); }
DI f2 xx(f2 a) { return __builtin_shufflevector(a, a, 0, 0); }
DI f2 yy(f2 a) { return __builtin_shufflevector(a, a, 1, 1); }
DI f2 cmul(f2 a, f2 b) {
  f2 t, r;
  asm("v_pk_mul_f32 %0, %1, %2 op_sel:[1,1] op_sel_hi:[1,0]" : "=v"(t) : "v"(a), "v"(b));
  asm("v_pk_fma_f32 %0, %1, %2, %3 op_sel:[0,0,0] op_sel_hi:[0,1,1] neg_lo:[0,0,1]" : "=v"(r) : "v"(a), "v"(b), "v"(t));
  return r;
}
DI f2 cmulc(f2 a, f2 b) {
  f2 t, r;
  asm("v_pk_mul_f32 %0, %1, %2 op_sel:[1,1] op_sel_hi:[1,0]" : "=v"(t) : "v"(a), "v"(b));
  asm("v_pk_fma_f32 %0, %1, %2, %3 op_sel:[0,0,0] op_sel_hi:[0,1,1] neg_hi:[0,1,0]" : "=v"(r) : "v"(a), "v"(b), "v"(t));
  return r;
}
DI f2 cmul_s(f2 a, f2 b) {
  f2 t, r;
  asm("v_pk_mul_f32 %0, %1, %2 op_sel:[1,1] op_sel_hi:[1,0]" : "=v"(t) : "v"(a), "s"(b));
  asm("v_pk_fma_f32 %0, %1, %2, %3 op_sel:[0,0,0] op_sel_hi:[0,1,1] neg_lo:[0,0,1]" : "=v"(r) : "v"(a), "s"(b), "v"(t));
  return r;
}
DI f2 cmulc_s(f2 a, f2 b) {
  f2 t, r;
  asm("v_pk_mul_f32 %0, %1, %2 op_sel:[1,1] op_sel_hi:[1,0]" : "=v"(t) : "v"(a), "s"(b));
  asm("v_pk_fma_f32 %0, %1, %2, %3 op_sel:[0,0,0] op_sel_hi:[0,1,1] neg_hi:[0,1,0]" : "=v"(r) : "v"(a), "s"(b), "v"(t));
  return r;
}
DI f2 cmul_add(f2 a, f2 b, f2 e) {
  f2 t, r;
  asm("v_pk_fma_f32 %0, %1, %2, %3 op_sel:[1,1,0] op_sel_hi:[1,0,1] neg_lo:[0,1,0]" : "=v"(t) : "v"(a), "v"(b), "v"(e));
  asm("v_pk_fma_f32 %0, %1, %2, %3 op_sel:[0,0,0] op_sel_hi:[0,1,1]" : "=v"(r) : "v"(a), "v"(b), "v"(t));
  return r;
}
DI f2 cmulc_add(f2 a, f2 b, f2 e) {
  f2 t, r;
  asm("v_pk_fma_f32 %0, %1, %2, %3 op_sel:[1,1,0] op_sel_hi:[1,0,1]" : "=v"(t) : "v"(a), "v"(b), "v"(e));
  asm("v_pk_fma_f32 %0, %1, %2, %3 op_sel:[0,0,0] op_sel_hi:[0,1,1] neg_hi:[0,1,0]" : "=v"(r) : "v"(a), "v"(b), "v"(t));
  return r;
}
DI f2 ccmul_add(f2 a, f2 b, f2 e) {
  f2 t, r;
  asm("v_pk_fma_f32 %0, %1, %2, %3 op_sel:[1,1,0] op_sel_hi:[1,0,1] neg_hi:[0,1,0]" : "=v"(t) : "v"(a), "v"(b), "v"(e));
  asm("v_pk_fma_f32 %0, %1, %2, %3 op_sel:[0,0,0] op_sel_hi:[0,1,1]" : "=v"(r) : "v"(a), "v"(b), "v"(t));
  return r;
}
template <bool INV> DI f2 addmi(f2 a, f2 d) { return fma2(swp(d), INV ? f2{-1.f, 1.f} : f2{1.f, -1.f}, a); }
template <bool INV> DI f2 submi(f2 a, f2 d) { return fma2(swp(d), INV ? f2{1.f, -1.f} : f2{-1.f, 1.f}, a); }

template <bool INV> DI void fft4(f2& b0, f2& b1, f2& b2, f2& b3) {
  f2 c0 = b0 + b2, c2 = b0 - b2, c1 = b1 + b3, d = b1 - b3;
  b0 = c0 + c1; b2 = c0 - c1; b1 = addmi<INV>(c2, d); b3 = submi<INV>(c2, d);
}
template <bool INV> DI void fft8(f2* v) {
  const float h = 0.70710678118654752440f;
  f2 a0 = v[0] + v[4], a4 = v[0] - v[4];
  f2 a1 = v[1] + v[5], a5 = v[1] - v[5];
  f2 a2 = v[2] + v[6], a6 = v[2] - v[6];
  f2 a3 = v[3] + v[7], a7 = v[3] - v[7];
  f2 c0 = addmi<INV>(a4, a6), c2 = submi<INV>(a4, a6);
  f2 u = addmi<INV>(a5, a7), w = submi<INV>(a5, a7);
  f2 tu = addmi<INV>(u, u), tw = addmi<INV>(w, w);
  fft4<INV>(a0, a1, a2, a3);
  v[0] = a0; v[2] = a1; v[4] = a2; v[6] = a3;
  v[1] = fma2(tu, f2{h, h}, c0);
  v[5] = fma2(tu, f2{-h, -h}, c0);
  v[3] = fma2(swp(tw), INV ? f2{-h, h} : f2{h, -h}, c2);
  v[7] = fma2(swp(tw), INV ? f2{h, -h} : f2{-h, h}, c2);
}
template <int CTRL> DI float dpp_add(float x) {
  return x + __int_as_float(__builtin_amdgcn_update_dpp(0, __float_as_int(x), CTRL, 0xf, 0xf, false));
}
DI float red32(float x) {
  x = dpp_add<0xB1>(x); x = dpp_add<0x4E>(x); x = dpp_add<0x141>(x); x = dpp_add<0x140>(x);
  auto r = __builtin_amdgcn_permlane16_swap(__float_as_uint(x), __float_as_uint(x), false, false);
  return __uint_as_float(r[0]) + __uint_as_float(r[1]);
}
DI void wbar() { __builtin_amdgcn_wave_barrier(); }
DI void bar() {
  asm volatile("s_waitcnt lgkmcnt(0)" ::: "memory");
  __builtin_amdgcn_s_barrier();
  asm volatile("" ::: "memory");
}
DI void wait_vm0() { __builtin_amdgcn_s_waitcnt(0x0F70); asm volatile("" ::: "memory"); }
DI void layernorm8(const f2* v, u32* o) {
  f2 s2 = ((v[0] + v[1]) + (v[2] + v[3])) + ((v[4] + v[5]) + (v[6] + v[7]));
  float mean = red32(s2.x + s2.y) * (1.0f / 512.0f);
  f2 mm = f2{mean, mean};
  f2 q0 = f2{0, 0}, q1 = f2{0, 0};
#pragma unroll
  for (int i = 0; i < 8; i += 2) { f2 d0 = v[i] - mm, d1 = v[i + 1] - mm; q0 = fma2(d0, d0, q0); q1 = fma2(d1, d1, q1); }
  q0 = q0 + q1;
  float q = red32(q0.x + q0.y);
  float m = rbf(mean), vr = rbf(q * (1.0f / 512.0f));
  float sd = rbf(__builtin_amdgcn_sqrtf(rbf(vr + 1e-5f)));
  float ri = __builtin_amdgcn_rcpf(sd);
  f2 m2 = f2{m, m}, r2 = f2{ri, ri};
#pragma unroll
  for (int i = 0; i < 8; ++i) o[i] = pack2(rbf2(v[i] - m2) * r2);
}
DI f2 ldtw(const cxf* p) { return *(const f2*)p; }
typedef __amdgpu_buffer_rsrc_t rsrc_t;
DI rsrc_t mk_rsrc(void* p, u32 bytes) { return __builtin_amdgcn_make_buffer_rsrc(p, 0, bytes, 0x00020000); }
template <int AUX> DI u4v bld4(rsrc_t r, u32 off) { return __builtin_amdgcn_raw_buffer_load_b128(r, off, 0, AUX); }
template <int AUX> DI u32 bld1(rsrc_t r, u32 off) { return __builtin_amdgcn_raw_buffer_load_b32(r, off, 0, AUX); }
template <int AUX> DI void bst4(rsrc_t r, u32 off, u4v v) { __builtin_amdgcn_raw_buffer_store_b128(v, r, off, 0, AUX); }
DI u4v f4u(f2 a, f2 b) { return u4v{__float_as_uint(a.x), __float_as_uint(a.y), __float_as_uint(b.x), __float_as_uint(b.y)}; }

constexpr int RP = 288;

constexpr u32 SENT = 0xFFFFFFFFu;
DI u32 nosent(u32 v) { return v < SENT - 1 ? v : SENT - 1; }
DI u32 mx2(u32 a, u32 b) { return a > b ? a : b; }
DI u32 mx4(u32 m, u4v a) { return mx2(mx2(mx2(mx2(m, a.x), a.y), a.z), a.w); }
DI bool anysent(u32 m) { return __builtin_amdgcn_ballot_w64(m == SENT) != 0; }
struct R1Tw { f2 tw1[8], tw2[8], w512; };
DI void r1_load_tw(R1Tw& tw, int t) {
  const f4* p = (const f4*)&g_r1all + (t & 31);
  f2 v[14];
#pragma unroll
  for (int i = 0; i < 7; ++i) { f4 q = p[32 * i]; v[2 * i] = f2{q[0], q[1]}; v[2 * i + 1] = f2{q[2], q[3]}; }
#pragma unroll
  for (int q = 1; q < 8; ++q) { tw.tw1[q] = v[q - 1]; tw.tw2[q] = v[6 + q]; }
  tw.w512 = ldtw(&g_tw512.v[t]);
}
DI u4v tag0(u4v v) { return v & u4v{~1u, ~1u, ~1u, ~1u}; }
DI u4v tag1(u4v v) { return v | u4v{1u, 1u, 1u, 1u}; }
template <int AUX, int RW, int R0> DI void r1_body(const u32* xin, const R1Tw& tw, const f16v& tx, f2* lds, rsrc_t rs, int s2, int t) {
  const int s1 = t >> 5, j = t & 31;
  f2 z[8];
#pragma unroll
  for (int m = 0; m < 8; ++m) z[m] = unpk(xin[m]);
  u32 o[8];
  layernorm8(z, o);
#pragma unroll
  for (int m = 0; m < 8; ++m) z[m] = unpk(o[m]);
  f2* L = lds + R0 + (s1 >> 1) * RW + (s1 & 1) * RP;
  fft8<false>(z);
#pragma unroll
  for (int q = 1; q < 8; ++q) z[q] = cmul(z[q], tw.tw1[q]);
#pragma unroll
  for (int q = 0; q < 8; ++q) L[q * 36 + j] = z[q];
  wbar();
#pragma unroll
  for (int m = 0; m < 8; ++m) z[m] = L[(j >> 2) * 36 + (j & 3) + 4 * m];
  wbar();
  fft8<false>(z);
#pragma unroll
  for (int q = 1; q < 8; ++q) z[q] = cmul(z[q], tw.tw2[q]);
#pragma unroll
  for (int q = 0; q < 8; ++q) L[(j & 3) * 68 + 8 * q + (j >> 2)] = z[q];
  wbar();
#pragma unroll
  for (int i = 0; i < 8; ++i) z[i] = L[(i & 3) * 68 + 32 * (i >> 2) + j];
  wbar();
  fft4<false>(z[0], z[1], z[2], z[3]);
  fft4<false>(z[4], z[5], z[6], z[7]);
#pragma unroll
  for (int i = 0; i < 8; ++i) L[j + 32 * (i >> 2) + 64 * (i & 3)] = z[i];
  bar();
  f2 X[8];
#pragma unroll
  for (int r = 0; r < 8; ++r) {
    const f2* Lr = lds + R0 + (r >> 1) * RW + (r & 1) * RP;
    f2 A = Lr[t], Bc = Lr[(256 - t) & 255];
    f2 E = fma2(Bc, f2{1.f, -1.f}, A);
    f2 Dm = fma2(Bc, f2{-1.f, 1.f}, A);
    f2 x2 = cmul_add(Dm, tw.w512, E);
    if (t == 0) x2.y = E.x - Dm.y;
    X[r] = x2;
  }
  fft8<false>(X);
#pragma unroll
  for (int k = 1; k < 8; ++k) X[k] = cmul_s(X[k], f2{tx[2 * k], tx[2 * k + 1]});
  const u32 dst = OFF_T + s2 * TPB + t * 16;
  bst4<AUX>(rs, dst, tag0(f4u(X[0], X[4])));
  bst4<AUX>(rs, dst + 4096, tag0(f4u(X[1], X[7])));
  bst4<AUX>(rs, dst + 8192, tag0(f4u(X[2], X[6])));
  bst4<AUX>(rs, dst + 12288, tag0(f4u(X[3], X[5])));
}

DI int f_of(int kap, int g, int j) {
  int u = kap / 7, r = kap % 7;
  if (r < 6) { int ct = 2 * u + (r >= 3), p = r % 3; return 112 * ct + 32 * p + (j < 4 ? 4 * g + j : 16 + 4 * g + (j - 4)); }
  return 112 * (2 * u + (g >> 1)) + 96 + 8 * (g & 1) + j;
}
constexpr int WPIT = 528;
#define ARM_A(PARTS) do { if ((PARTS) && first) { \
    const u4v sv = u4v{SENT, SENT, SENT, SENT}; \
    const int xcd_ = b & 7, ii_ = b >> 3, w_ = __builtin_amdgcn_readfirstlane(t >> 6), ln_ = t & 63, g_ = ln_ >> 4; \
    const int s2_ = 32 * xcd_ + ii_, rt_ = 2 * xcd_ + (ii_ >> 4), ct_ = ii_ & 15, rt2_ = 4 * xcd_ + (ii_ >> 3), ct2_ = ii_ & 7; \
    if ((PARTS) & 1) { \
      const u32 co = OFF_LNF + ((s2_ >> 1) * 16 + 4 * w_) * 1024 + (ln_ >> 3) * 256 + (s2_ & 1) * 128 + (ln_ & 7) * 16; \
      bst4<CAUX>(rs, co, sv); bst4<CAUX>(rs, co + 2048, sv); \
      const u32 xo = OFF_XR + ((256 * (ln_ >> 3) + 8 * rt2_ + 2 * w_) * D_ + 64 * ct2_) * 2 + (ln_ & 7) * 16; \
      bst4<CAUX>(rs, xo, sv); bst4<CAUX>(rs, xo + 1024, sv); } \
    if ((PARTS) & 2) { \
      const int tt0_ = 8 * rt_ + 2 * w_, kb_ = 7 * (ct_ >> 1) + 3 * (ct_ & 1); \
      _Pragma("unroll") for (int h = 0; h < 2; ++h) _Pragma("unroll") for (int p = 0; p < 3; ++p) \
        bst4<CAUX>(rs, OFF_H + ((tt0_ + h) * 56 + kb_ + p) * 1024 + ln_ * 16, sv); \
      bst4<CAUX>(rs, OFF_H + (((tt0_ + (g_ & 1)) * 56 + 7 * (ct_ >> 1) + 6) * 64 + (2 * (ct_ & 1) + (g_ >> 1)) * 16 + (ln_ & 15)) * 16, sv); } \
    __builtin_amdgcn_sched_barrier(0); } } while (0)
#define CAUX 0
#define KF_ARM 3
#define KC_ARM (3 - (KF_ARM))
#define KF_P0 3
#define WAUX 0
#define TAUX 0
__global__ __launch_bounds__(256) void k_first(const u32* __restrict__ x, const unsigned short* __restrict__ wr,
                                               const unsigned short* __restrict__ wi, const unsigned short* __restrict__ w1,
                                               const unsigned short* __restrict__ w2, char* __restrict__ ws) {
  __shared__ __attribute__((aligned(16))) char smem[8 * RP * 8 + 32 * WPIT];
  const int b = blockIdx.x, t = threadIdx.x, w = __builtin_amdgcn_readfirstlane(t >> 6), lane = t & 63;
  STAMP(0, 0);
  asm volatile("" :: "s"(x), "s"(wr), "s"(wi), "s"(w1), "s"(w2), "s"(ws));
  const rsrc_t rs = mk_rsrc(ws, WS_USED);
  const bool first = true;
  const int s2 = 32 * (b & 7) + (b >> 3);
  u32 xin[8];
  const u32* xr = x + (size_t)(256 * (t >> 5) + s2) * 256 + (t & 31);
#pragma unroll
  for (int m = 0; m < 8; ++m) xin[m] = xr[32 * m];
  R1Tw tw;
  r1_load_tw(tw, t);
  const f16v tx = *(const f16v*)&g_twx.v[s2 * 8];
  unsigned short fr[8], fi[8];
#pragma unroll
  for (int i = 0; i < 8; ++i) { fr[i] = wr[(b + 256 * i) * DF_ + t]; fi[i] = wi[(b + 256 * i) * DF_ + t]; }
  u4v wu[4];
  const int isw2 = b >= 112;
  const int ua = isw2 ? (b - 112) >> 1 : b / 7, ub = isw2 ? (b - 112) & 1 : b % 7;
  if (b < 224) {
#pragma unroll
    for (int g = 0; g < 4; ++g) {
      const int i = t >> 5;
      const unsigned short* p;
      if (isw2) p = w2 + (size_t)f_of(ua, g, i) * D_;
      else p = w1 + (size_t)(256 * (g & 1) + 64 * (i >> 1) + 4 * ua + 2 * (g >> 1) + (i & 1)) * F_;
      wu[g] = *(const u4v*)(p + 256 * ub + (t & 31) * 8);
    }
  } else {
#pragma unroll
    for (int g = 0; g < 4; ++g) asm volatile("" : "=v"(wu[g]));
  }
  unsigned short pv[8] = {};
  if (w == 0) {
    const int k = b + 256 * (t & 7), km = (2048 - k) & 2047;
    pv[0] = wr[k * DF_]; pv[1] = wi[k * DF_]; pv[2] = wr[km * DF_]; pv[3] = wi[km * DF_];
    pv[4] = wr[k * DF_ + 256]; pv[5] = wi[k * DF_ + 256]; pv[6] = wr[km * DF_ + 256]; pv[7] = wi[km * DF_ + 256];
  }
  __builtin_amdgcn_sched_barrier(0);
  u32 pf0 = 0;
  if (w == 3 && b < 64) {
    u32 alo, ahi, elo, ehi;
    asm volatile("s_getpc_b64 s[40:41]\n\ts_mov_b32 %0, s40\n\ts_mov_b32 %1, s41" : "=s"(alo), "=s"(ahi) :: "s40", "s41");
    asm volatile("s_getpc_b64 s[40:41]\n\ts_add_u32 %0, s40, k_m@rel32@lo+4\n\ts_addc_u32 %1, s41, k_m@rel32@hi+12" : "=s"(elo), "=s"(ehi) :: "s40", "s41", "scc");
    const unsigned long long a = ((unsigned long long)ahi << 32) | alo, e = ((unsigned long long)ehi << 32) | elo;
    const u32 lim = e > a && e - a < 65536 ? (u32)(e - a) - 4 : 0u;
    const u32 o = lane * 128, oc = (o < lim ? o : lim) & ~3u;
    asm volatile("global_load_dword %0, %1, %2" : "=v"(pf0) : "v"(oc), "s"(a));
  }
  __builtin_amdgcn_sched_barrier(0);
  ARM_A(KF_ARM & KF_P0);
  if (b < 224) {
    char* im = smem + 8 * RP * 8;
#pragma unroll
    for (int g = 0; g < 4; ++g) *(u4v*)(im + (8 * g + (t >> 5)) * WPIT + (t & 31) * 16) = wu[g];
    bar();
    const int g = lane >> 4, q = (lane >> 2) & 3, p = lane & 3;
    const char* a0 = im + (8 * g + q) * WPIT + (isw2 ? 32 * p : 8 * p) + 128 * w;
#pragma unroll
    for (int n = 0; n < 4; ++n) {
      const char* a = a0 + (isw2 ? 8 * n : 32 * n);
      s4v lo = __builtin_amdgcn_ds_read_tr16_b64_v4i16((LAS s4v*)a);
      s4v hi = __builtin_amdgcn_ds_read_tr16_b64_v4i16((LAS s4v*)(a + 4 * WPIT));
      s8v wf = __builtin_shufflevector(lo, hi, 0, 1, 2, 3, 4, 5, 6, 7);
      u32 off;
      if (isw2) off = OFF_W2F + (((4 * ub + w) * 56 + ua) * 4 + n) * 1024;
      else { int ft = 16 * ub + 4 * w + n; off = OFF_W1F + (((ft / 7) * 16 + ua) * 7 + ft % 7) * 1024; }
      bst4<WAUX>(rs, off + lane * 16, __builtin_bit_cast(u4v, wf));
    }
  }
  {
    const int k1 = b & 7, kp = k1 <= 4 ? (k1 & 3) : 8 - k1, e = k1 >= 4;
    const int tl = e * 32 + ((b >> 3) & 3) * 8 + (b >> 5);
    u32 o[8];
#pragma unroll
    for (int i = 0; i < 8; ++i) o[i] = (u32)fr[i] | ((u32)fi[i] << 16);
    const u32 dst = OFF_WT + ((kp * 64 + (t >> 2)) * 512 + (t & 3) * 64 + tl) * 16;
    bst4<TAUX>(rs, dst, u4v{o[0], o[1], o[2], o[3]});
    bst4<TAUX>(rs, dst + 4096, u4v{o[4], o[5], o[6], o[7]});
    auto cv = [](unsigned short v) { return __uint_as_float((u32)v << 16); };
    float h0r = 0.5f * (cv(pv[0]) + cv(pv[2])), h0i = 0.5f * (cv(pv[1]) - cv(pv[3]));
    float h1r = 0.5f * (cv(pv[4]) + cv(pv[6])), h1i = 0.5f * (cv(pv[5]) - cv(pv[7]));
    if (t < 8) ((f4*)(ws + OFF_PQ))[(kp * 64 + tl) * 8 + t] = f4{0.5f * (h0r + h1r), 0.5f * (h0i + h1i), 0.5f * (h0r - h1r), 0.5f * (h0i - h1i)};
  }
  ARM_A(KF_ARM & ~KF_P0);
  STAMP(0, 1);
  u32 pf[7];
#pragma unroll
  for (int i = 0; i < 7; ++i) pf[i] = 0;
  asm volatile("" :: "v"(xin[0]), "v"(xin[1]), "v"(xin[2]), "v"(xin[3]), "v"(xin[4]), "v"(xin[5]), "v"(xin[6]), "v"(xin[7]), "v"(tw.w512),
               "v"(tw.tw1[1]), "v"(tw.tw1[3]), "v"(tw.tw1[5]), "v"(tw.tw1[7]), "v"(tw.tw2[2]), "v"(tw.tw2[4]), "v"(tw.tw2[6]));
  if (w == 3 && b < 64) {
    u32 alo, ahi, elo, ehi;
    asm volatile("s_getpc_b64 s[40:41]\n\ts_add_u32 %0, s40, k_m@rel32@lo+4\n\ts_addc_u32 %1, s41, k_m@rel32@hi+12" : "=s"(alo), "=s"(ahi) :: "s40", "s41", "scc");
    asm volatile("s_getpc_b64 s[40:41]\n\ts_add_u32 %0, s40, z_end@rel32@lo+4\n\ts_addc_u32 %1, s41, z_end@rel32@hi+12" : "=s"(elo), "=s"(ehi) :: "s40", "s41", "scc");
    const unsigned long long a = ((unsigned long long)ahi << 32) | alo, e = ((unsigned long long)ehi << 32) | elo;
    const u32 lim = e > a && e - a < 65536 ? (u32)(e - a) - 4 : 0u;
#pragma unroll
    for (int i = 0; i < 6; ++i) {
      const u32 o = (i * 64 + lane) * 128, oc = (o < lim ? o : lim) & ~3u;
      asm volatile("global_load_dword %0, %1, %2" : "=v"(pf[i]) : "v"(oc), "s"(a));
    }
    const char* tb = lane < 44 ? (const char*)&g_r2all + lane * 128 : lane < 60 ? (const char*)&g_twc1 + (lane - 44) * 128 : (const char*)&g_twc2 + (lane - 60) * 128;
    asm volatile("global_load_dword %0, %1, off" : "=v"(pf[6]) : "v"(tb));
  }
  __builtin_amdgcn_sched_barrier(0);
  r1_body<0, 2 * RP, 0>(xin, tw, tx, (f2*)smem, rs, s2, t);
  asm volatile("s_waitcnt vmcnt(0)" :: "v"(pf[0]), "v"(pf[1]), "v"(pf[2]), "v"(pf[3]), "v"(pf[4]), "v"(pf[5]), "v"(pf[6]), "v"(pf0));
  STAMP(0, 2);
}

constexpr int PK = 292, QS = 72;
DI int idx1(int k1, int q, int j) { return k1 * PK + q * QS + (j ^ ((k1 >> 2) << 2)); }
template <int AUX> DI u4v bld4s(rsrc_t r, u32 off, u32 soff) { return __builtin_amdgcn_raw_buffer_load_b128(r, off, soff, AUX); }
template <int AUX> DI void bst4s(rsrc_t r, u32 off, u32 soff, u4v v) { __builtin_amdgcn_raw_buffer_store_b128(v, r, off + soff, 0, AUX); }
struct CIn { u4v w0, w1; f2 tc1[4], tc2[8]; u4v pq[8]; };
DI void c_load(CIn& ci, rsrc_t rs, int t, int kp, int qd, bool special) {
  const u32 wsof = OFF_WT + (kp * 64 + qd) * 8192;
  ci.w0 = bld4s<0>(rs, t * 16, wsof); ci.w1 = bld4s<0>(rs, t * 16 + 4096, wsof);
#pragma unroll
  for (int q = 1; q < 4; ++q) ci.tc1[q] = ldtw(&g_twc1.v[(t >> 2) * 4 + q]);
#pragma unroll
  for (int q = 0; q < 4; ++q) { f4 p = ((const f4*)&g_twc2)[8 * q + (t & 7)]; ci.tc2[2 * q] = f2{p[0], p[1]}; ci.tc2[2 * q + 1] = f2{p[2], p[3]}; }
  if (special) {
#pragma unroll
    for (int i = 0; i < 8; ++i) ci.pq[i] = bld4s<0>(rs, t * 128 + i * 16, OFF_PQ + kp * 8192);
  } else {
#pragma unroll
    for (int i = 0; i < 8; ++i) asm volatile("" : "=v"(ci.pq[i]));
  }
}
template <class HK> DI void c_body(const u4v* in, const CIn& ci, f2* lds, rsrc_t rs, u32 tv, u32 ts, int t, int kp, bool special, HK hook) {
  const int cc = t & 3, j = t >> 2;
  const f2* tc1 = ci.tc1; const f2* tc2 = ci.tc2; const u4v* pq = ci.pq; const u4v w0 = ci.w0, w1 = ci.w1;
#pragma unroll
  for (int e = 0; e < 2; ++e) {
    f2 a[4];
#pragma unroll
    for (int m = 0; m < 4; ++m) a[m] = e ? f2{__uint_as_float(in[m].z), __uint_as_float(in[m].w)} : f2{__uint_as_float(in[m].x), __uint_as_float(in[m].y)};
    fft4<false>(a[0], a[1], a[2], a[3]);
#pragma unroll
    for (int q = 1; q < 4; ++q) a[q] = cmul(a[q], tc1[q]);
#pragma unroll
    for (int q = 0; q < 4; ++q) lds[idx1(2 * cc + e, q, j)] = a[q];
  }
  hook(1);
  bar();
  const int k1 = t >> 5, q = (t >> 3) & 3, jp = t & 7;
  const float sc = (float)kSc;
  f2 v[8];
#pragma unroll
  for (int m = 0; m < 8; ++m) v[m] = lds[idx1(k1, q, jp + 8 * m)];
  wbar();
  hook(2);
  fft8<false>(v);
  v[0] = v[0] * f2{sc, sc};
#pragma unroll
  for (int i = 1; i < 8; ++i) v[i] = cmul(v[i], tc2[i]);
  f2* L2 = lds + k1 * PK + q * QS;
#pragma unroll
  for (int i = 0; i < 8; ++i) L2[jp * 9 + i] = v[i];
  wbar();
#pragma unroll
  for (int i = 0; i < 8; ++i) v[i] = L2[i * 9 + jp];
  wbar();
  hook(3);
  fft8<false>(v);
  if (!special) {
    u32 w[8] = {w0.x, w0.y, w0.z, w0.w, w1.x, w1.y, w1.z, w1.w};
#pragma unroll
    for (int i = 0; i < 8; ++i) v[i] = cmul(v[i], unpk(w[i]));
  } else {
#pragma unroll
    for (int i = 0; i < 8; ++i) lds[k1 * PK + i * 32 + (t & 31)] = v[i];
    wbar();
    const bool z0 = kp == 0 && k1 == 0;
    const int u = q + 4 * jp, up = z0 ? (32 - u) & 31 : 31 - u;
    const int zi = (z0 && u == 0) ? 8 : 7;
    const f2* src = lds + (kp == 0 ? k1 : k1 ^ 1) * PK + (up & 3) * 8 + (up >> 2);
#pragma unroll
    for (int i = 0; i < 8; ++i) {
      f2 zm = src[((zi - i) & 7) * 32];
      f2 P = f2{__uint_as_float(pq[i].x), __uint_as_float(pq[i].y)}, Q = f2{__uint_as_float(pq[i].z), __uint_as_float(pq[i].w)};
      v[i] = ccmul_add(zm, Q, cmul(v[i], P));
    }
    wbar();
  }
  hook(4);
  fft8<true>(v);
  v[0] = v[0] * f2{sc, sc};
#pragma unroll
  for (int i = 1; i < 8; ++i) v[i] = cmulc(v[i], tc2[i]);
#pragma unroll
  for (int i = 0; i < 8; ++i) L2[i * 9 + jp] = v[i];
  wbar();
#pragma unroll
  for (int i = 0; i < 8; ++i) v[i] = L2[jp * 9 + i];
  wbar();
  hook(5);
  fft8<true>(v);
#pragma unroll
  for (int m = 0; m < 8; ++m) lds[idx1(k1, q, jp + 8 * m)] = v[m];
  hook(6);
  bar();
  f2 o[2][4];
#pragma unroll
  for (int e = 0; e < 2; ++e) {
#pragma unroll
    for (int qq = 0; qq < 4; ++qq) o[e][qq] = lds[idx1(2 * cc + e, qq, j)];
#pragma unroll
    for (int qq = 1; qq < 4; ++qq) o[e][qq] = cmulc(o[e][qq], tc1[qq]);
    fft4<true>(o[e][0], o[e][1], o[e][2], o[e][3]);
  }
#pragma unroll
  for (int m = 0; m < 4; ++m) bst4s<16>(rs, tv, ts + m * 64 * TPB, tag1(f4u(o[0][m], o[1][m])));
}

#define SB() __builtin_amdgcn_sched_barrier(0)
template <int N> DI void actN(const f2* a, const f2* bias, u32* o) {
  const float c0 = -2.0f * 0.7978845608028654f * 1.4426950408889634f, c1 = c0 * 0.044715f;
  f2 x[N], u[N];
#pragma unroll
  for (int i = 0; i < N; ++i) x[i] = rbf2(a[i]);
  SB();
#pragma unroll
  for (int i = 0; i < N; ++i) x[i] = x[i] + bias[i];
  SB();
#pragma unroll
  for (int i = 0; i < N; ++i) x[i] = rbf2(x[i]);
  SB();
#pragma unroll
  for (int i = 0; i < N; ++i) u[i] = x[i] * x[i];
  SB();
#pragma unroll
  for (int i = 0; i < N; ++i) u[i] = fma2(u[i], f2{c1, c1}, f2{c0, c0});
  SB();
#pragma unroll
  for (int i = 0; i < N; ++i) u[i] = u[i] * x[i];
  SB();
#pragma unroll
  for (int i = 0; i < N; ++i) u[i] = f2{__builtin_amdgcn_exp2f(u[i].x), __builtin_amdgcn_exp2f(u[i].y)};
  SB();
#pragma unroll
  for (int i = 0; i < N; ++i) u[i] = u[i] + f2{1.f, 1.f};
  SB();
#pragma unroll
  for (int i = 0; i < N; ++i) u[i] = f2{__builtin_amdgcn_rcpf(u[i].x), __builtin_amdgcn_rcpf(u[i].y)};
  SB();
#pragma unroll
  for (int i = 0; i < N; ++i) u[i] = u[i] * x[i];
  SB();
#pragma unroll
  for (int i = 0; i < N; ++i) o[i] = pack2(u[i]);
  SB();
#pragma unroll
  for (int i = 0; i < N; ++i) o[i] = nosent(o[i]);
  SB();
}

#define ALD4(dst, voff, sbase, imm) asm volatile("global_load_dwordx4 %0, %1, %2 offset:" #imm : "=v"(dst) : "v"(voff), "s"(sbase))
#define ALD1(dst, voff, sbase, imm) asm volatile("global_load_dword %0, %1, %2 offset:" #imm : "=v"(dst) : "v"(voff), "s"(sbase))
#define TSTEP(i)
#define NSLOW()
#define MEMF() asm volatile("" ::: "memory")
#define MX4F(m, a) do { asm volatile("v_max_u32 %0, %1, %2" : "=v"(m) : "v"((a).x), "v"((a).y)); \
    asm volatile("v_max3_u32 %0, %0, %1, %2" : "+v"(m) : "v"((a).z), "v"((a).w)); } while (0)
#define MX4(m, a) do { asm volatile("v_max3_u32 %0, %0, %1, %2" : "+v"(m) : "v"((a).x), "v"((a).y)); \
    asm volatile("v_max3_u32 %0, %0, %1, %2" : "+v"(m) : "v"((a).z), "v"((a).w)); } while (0)
constexpr int LDS_W1 = 114688;
constexpr int LDS_M = LDS_W1 + 49152;
constexpr int W2E = 7;
constexpr int PA = 6;
#define HPOS 3
#define MAUX 16
#define FAUX 0
#define PD 4

extern "C" __global__ __launch_bounds__(256, 1) void k_m(const u32* __restrict__ xres, const unsigned short* __restrict__ b1,
                                              const unsigned short* __restrict__ b2, char* ws, unsigned short* out) {
  extern __shared__ __attribute__((aligned(16))) char smem[];
  STAMP(1, 0);
  const int b = blockIdx.x, xcd = b & 7, ii = b >> 3;
  const int t = threadIdx.x, w = __builtin_amdgcn_readfirstlane(t >> 6), lane = t & 63;
  const int s2 = 32 * xcd + ii, rt = 2 * xcd + (ii >> 4), ct = ii & 15, rt2 = 4 * xcd + (ii >> 3), ct2 = ii & 7;
  const rsrc_t rs = mk_rsrc(ws, WS_USED);
  f2* lds = (f2*)(smem + LDS_W1);
  u4v w2f[14][4];
  int budget = 1 << 16;
  const u32 l16 = lane * 16;
  const int rot = ct2 & 3;
  int kq[4];
#pragma unroll
  for (int q = 0; q < 4; ++q) { const int pr = (q + rot) & 3; kq[q] = 14 * w + 7 * (pr >> 1) + 3 * (pr & 1); }
  const int kt0 = 14 * w + 6 + 7 * (ct2 & 1), kt1 = 14 * w + 13 - 7 * (ct2 & 1);
#define KB(n) ((n) < 12 ? kq[(n) & 3] : ((n) == 12 ? kt0 : kt1))
#define KI(n) ((n) < 12 ? (n) >> 2 : 0)
  const u32 w2s = OFF_W2F + ct2 * 56 * 4096;
#define W2LD1(n, i) w2f[n][i] = bld4s<0>(rs, l16 + (i) * 1024, w2s + (KB(n) + KI(n)) * 4096)
#define W2LD(n) do { _Pragma("unroll") for (int i_ = 0; i_ < 4; ++i_) W2LD1(n, i_); } while (0)
  const int qd = 8 * (b & 7) + ((b >> 3) & 7), kp = b >> 6;
  const bool special = qd == 0 && w == 0;
  const u32 tv = (t >> 2) * TPB + (t & 3) * 16, ts = OFF_T + kp * 4096 + qd * 64;
  u32 xin[8];
  {
    u4v in[4];
#pragma unroll
    for (int m = 0; m < 4; ++m) in[m] = bld4s<0>(rs, tv, ts + m * 64 * TPB);
    CIn ci;
    c_load(ci, rs, t, kp, qd, special);
    SB();
    const char* gsrc = ws + OFF_W1F + (size_t)ct * LDS_W1 + l16;
#define W1DMA(pa, pb) do { SB(); _Pragma("unroll") for (int p_ = (pa); p_ < (pb); ++p_) { \
      const int sl_ = p_ / 7, pc_ = p_ % 7; \
      const char* gp_ = gsrc + ((4 * w + sl_ + ct) & 15) * 7168 + (pc_ & 4) * 1024; \
      char* lp_ = smem + (4 * w + sl_) * 7168 + (pc_ & 4) * 1024; \
      if ((pc_ & 3) == 0) __builtin_amdgcn_global_load_lds((const void*)gp_, (LAS void*)lp_, 16, 0, 0); \
      if ((pc_ & 3) == 1) __builtin_amdgcn_global_load_lds((const void*)gp_, (LAS void*)lp_, 16, 1024, 0); \
      if ((pc_ & 3) == 2) __builtin_amdgcn_global_load_lds((const void*)gp_, (LAS void*)lp_, 16, 2048, 0); \
      if ((pc_ & 3) == 3) __builtin_amdgcn_global_load_lds((const void*)gp_, (LAS void*)lp_, 16, 3072, 0); } SB(); } while (0)
#pragma unroll
    for (int n = 0; n < 8; ++n) W2LD(n);
    SB();
    c_body(in, ci, lds, rs, tv, ts, t, kp, special, [&](int h) __attribute__((always_inline)) {
      SB();
      if (h <= 3) { W2LD(6 + 2 * h); W2LD(7 + 2 * h); }
      if (h == 4) W1DMA(0, 10);
      if (h == 5) W1DMA(10, 20);
      if (h == 6) W1DMA(20, 28);
      SB(); });
    SB();
    const u32* xb = xres + (size_t)(256 * (t >> 5) + s2) * 256 + (t & 31);
#pragma unroll
    for (int m = 0; m < 8; ++m) xin[m] = xb[32 * m];
    STAMP(1, 1);
  }
  int last = 0;
#pragma nounroll
  for (;;) {
  asm volatile("" : "+s"(last));
  int t = threadIdx.x;
  asm volatile("" : "+v"(t));
  int b = blockIdx.x, w = __builtin_amdgcn_readfirstlane(threadIdx.x >> 6);
  asm volatile("" : "+s"(b), "+s"(w));
  const int xcd = b & 7, ii = b >> 3;
  const int s2 = 32 * xcd + ii, rt = 2 * xcd + (ii >> 4), ct = ii & 15, rt2 = 4 * xcd + (ii >> 3), ct2 = ii & 7;
  const int tt0 = 8 * rt + 2 * w, kb = 7 * (ct >> 1) + 3 * (ct & 1);
  const int rot = ct2 & 3;
  int kq[4];
#pragma unroll
  for (int q = 0; q < 4; ++q) { const int pr = (q + rot) & 3; kq[q] = 14 * w + 7 * (pr >> 1) + 3 * (pr & 1); }
  const int kt0 = 14 * w + 6 + 7 * (ct2 & 1), kt1 = 14 * w + 13 - 7 * (ct2 & 1);
  const int qd = 8 * (b & 7) + ((b >> 3) & 7), kp = b >> 6;
  const bool special = qd == 0 && w == 0;
  const u32 ts = OFF_T + kp * 4096 + qd * 64;
  const f16v tx = *(const f16v*)&g_twx.v[s2 * 8];
  const int lane = t & 63, g = lane >> 4, c = lane & 15;
  const u32 l16 = lane * 16;
  const int rho2 = 64 * rt2 + 16 * w + c;
  const u32 lno = ((s2 >> 1) * 16 + 4 * w) * 1024 + (lane >> 3) * 256 + (s2 & 1) * 128 + (lane & 7) * 16;
  const u32 hst = (tt0 * 56 + kb) * 1024 + l16;
  const u32 htl = (((tt0 + (g & 1)) * 56 + 7 * (ct >> 1) + 6) * 64 + (2 * (ct & 1) + (g >> 1)) * 16 + c) * 16;
  const u32 ol = last ? OFF_LNF2 : OFF_LNF, oh = last ? OFF_H2 : OFF_H;
  const u32 cl = last ? 0x7f000000u : OFF_LNF2, ch = last ? 0x7f000000u : OFF_H2;
  u2v bias[7];
  {
    const int s1 = t >> 5, j = t & 31;
    u4v in[4], tq[11];
    SB();
#pragma unroll
    for (int i = 0; i < 11; ++i) tq[i] = ((const u4v*)&g_r2all)[32 * i + j];
#pragma unroll
    for (int jt = 0; jt < 7; ++jt) bias[jt] = *(const u2v*)(b1 + ct * 112 + 16 * jt + 4 * g);
    SB();
    wait_vm0();
    __builtin_amdgcn_s_barrier();
    MEMF();
    STAMP(kid, 0);
    do {
      MEMF();
#pragma unroll
      for (int m = 0; m < 4; ++m) in[m] = bld4s<16>(rs, t * 16, OFF_T + s2 * TPB + m * 4096);
      const u4v a = (in[0] & in[1]) & (in[2] & in[3]);
      if (__builtin_amdgcn_ballot_w64(((a.x & a.y) & (a.z & a.w) & 1u) == 0) == 0) break;
    } while (--budget > 0);
    SB();
    {
      const u4v sv = u4v{SENT, SENT, SENT, SENT};
#pragma unroll
      for (int q = 0; q < 6; ++q) bst4<MAUX>(rs, ch + hst + (q & 1) * 57344 + (q >> 1) * 1024, sv);
      bst4<MAUX>(rs, ch + htl, sv);
      bst4<MAUX>(rs, cl + lno, sv);
      bst4<MAUX>(rs, cl + lno + 2048, sv);
      SB();
    }
    STAMP(kid, 1);
    f2 X[8];
    {
      auto lo = [](u4v v) { return f2{__uint_as_float(v.x), __uint_as_float(v.y)}; };
      auto hi = [](u4v v) { return f2{__uint_as_float(v.z), __uint_as_float(v.w)}; };
      X[0] = lo(in[0]); X[4] = hi(in[0]); X[1] = lo(in[1]); X[7] = hi(in[1]);
      X[2] = lo(in[2]); X[6] = hi(in[2]); X[3] = lo(in[3]); X[5] = hi(in[3]);
    }
#pragma unroll
    for (int k = 1; k < 8; ++k) X[k] = cmulc_s(X[k], f2{tx[2 * k], tx[2 * k + 1]});
    fft8<true>(X);
#pragma unroll
    for (int r = 0; r < 8; ++r) lds[r * RP + t] = X[r];
    f2 tw1[8], twu[8], tw2[2][4];
    {
      f2 tv[22];
#pragma unroll
      for (int i = 0; i < 11; ++i) {
        tv[2 * i] = f2{__uint_as_float(tq[i].x), __uint_as_float(tq[i].y)};
        tv[2 * i + 1] = f2{__uint_as_float(tq[i].z), __uint_as_float(tq[i].w)};
      }
#pragma unroll
      for (int i = 0; i < 8; ++i) twu[i] = tv[i];
#pragma unroll
      for (int a = 0; a < 2; ++a)
#pragma unroll
        for (int i = 1; i < 4; ++i) tw2[a][i] = tv[8 + 3 * a + (i - 1)];
#pragma unroll
      for (int q = 1; q < 8; ++q) tw1[q] = tv[13 + q];
    }
    bar();
    f2* L = lds + s1 * RP;
    f2 z[8];
#pragma unroll
    for (int i = 0; i < 8; ++i) {
      int k = j + 32 * (i >> 2) + 64 * (i & 3);
      f2 A = L[k], Bc = L[(256 - k) & 255];
      f2 E = fma2(Bc, f2{1.f, -1.f}, A);
      f2 Dm = fma2(Bc, f2{-1.f, 1.f}, A);
      f2 zz = cmulc_add(Dm, twu[i], E);
      if (i == 0 && j == 0) zz = f2{A.x + A.y, A.x - A.y};
      z[i] = zz;
    }
    wbar();
    fft4<true>(z[0], z[1], z[2], z[3]);
    fft4<true>(z[4], z[5], z[6], z[7]);
#pragma unroll
    for (int a = 0; a < 2; ++a)
#pragma unroll
      for (int i = 1; i < 4; ++i) z[4 * a + i] = cmulc(z[4 * a + i], tw2[a][i]);
#pragma unroll
    for (int i = 0; i < 8; ++i) L[(i & 3) * 72 + 32 * (i >> 2) + j] = z[i];
    wbar();
#pragma unroll
    for (int q = 0; q < 8; ++q) z[q] = L[(j & 3) * 72 + 8 * q + (j >> 2)];
    fft8<true>(z);
#pragma unroll
    for (int m = 0; m < 8; ++m) L[(j >> 2) * 36 + (j & 3) + 4 * m] = z[m];
    wbar();
#pragma unroll
    for (int q = 0; q < 8; ++q) z[q] = L[q * 36 + j];
#pragma unroll
    for (int q = 1; q < 8; ++q) z[q] = cmulc(z[q], tw1[q]);
    fft8<true>(z);
    u32 x1[8], o[8];
#pragma unroll
    for (int m = 0; m < 8; ++m) { x1[m] = pack2(unpk(xin[m]) + rbf2(z[m])); z[m] = unpk(x1[m]); }
    const int rho = 8 * s2 + s1;
    u32* Lw = (u32*)L;
#pragma unroll
    for (int m = 0; m < 8; ++m) Lw[32 * m + j] = x1[m];
    wbar();
    u4v xa = *(const u4v*)(Lw + 4 * j), xb = *(const u4v*)(Lw + 128 + 4 * j);
    layernorm8(z, o);
#pragma unroll
    for (int m = 0; m < 8; ++m) o[m] = nosent(o[m]);
    char* sg = smem + LDS_W1 + 20480;
    *(u4v*)(sg + j * 288 + s1 * 16) = u4v{o[0], o[1], o[2], o[3]};
    *(u4v*)(sg + j * 288 + 144 + s1 * 16) = u4v{o[4], o[5], o[6], o[7]};
    bar();
    const char* Lr = sg + (16 * w + (lane >> 3)) * 144 + (lane & 7) * 16;
    const u4v fa = *(const u4v*)Lr, fb = *(const u4v*)(Lr + 8 * 144);
    const u32 lo = ol + lno;
    SB();
    bst4<16>(rs, lo, fa);
    bst4<16>(rs, lo + 2048, fb);
    SB();
    bst4<16>(rs, OFF_X1P + rho * 1024 + j * 16, xa);
    bst4<16>(rs, OFF_X1P + rho * 1024 + j * 16 + 512, xb);
  }
  STAMP(kid, 2);
  u4v hf[14][4];
  u32 hoff[4];
#pragma unroll
  for (int h = 0; h < 4; ++h) hoff[h] = oh + (4 * rt2 + ((w + h) & 3)) * 56 * 1024;
#define HFLDX(n, A) do { _Pragma("unroll") for (int h_ = 0; h_ < 4; ++h_) hf[n][h_] = bld4s<A>(rs, l16 + KI(n) * 1024, hoff[h_] + KB(n) * 1024); } while (0)
#define HFLD(n) HFLDX(n, 16)
#define HFLDF(n) HFLDX(n, FAUX)
#define HFMX(n) mx4(mx4(mx4(mx4(0, hf[n][0]), hf[n][1]), hf[n][2]), hf[n][3])
#define CMPS(bad, m) asm volatile("v_cmp_eq_u32 %0, -1, %1" : "=s"(bad) : "v"(m))
  {
    u4v av[2][16];
#define AVLDX(ks, A) do { _Pragma("unroll") for (int h_ = 0; h_ < 2; ++h_) av[h_][ks] = bld4s<A>(rs, l16 + (((ks) + ct) & 15) * 1024, ol + (tt0 + h_) * 16384); } while (0)
#define AVLD(ks) AVLDX(ks, 16)
    SB();
    wait_vm0();
    __builtin_amdgcn_s_barrier();
    MEMF();
    STAMP(kid, 3);
    f4 acc[7][2];
    u32 o0[8];
#define GELU8(p, o) do { f2 a_[8], bs_[8]; \
      _Pragma("unroll") for (int h = 0; h < 2; ++h) { \
        f4 a0 = acc[2 * (p)][h], a1 = acc[2 * (p) + 1][h]; \
        a_[4 * h] = f2{a0[0], a0[1]}; a_[4 * h + 1] = f2{a0[2], a0[3]}; a_[4 * h + 2] = f2{a1[0], a1[1]}; a_[4 * h + 3] = f2{a1[2], a1[3]}; \
        bs_[4 * h] = unpk(bias[2 * (p)].x); bs_[4 * h + 1] = unpk(bias[2 * (p)].y); bs_[4 * h + 2] = unpk(bias[2 * (p) + 1].x); bs_[4 * h + 3] = unpk(bias[2 * (p) + 1].y); } \
      SB(); \
      if ((p) == HPOS) { _Pragma("unroll") for (int n = 0; n < PD; ++n) HFLDF(n); } \
      SB(); actN<8>(a_, bs_, o); } while (0)
#define HST8(p, o) do { _Pragma("unroll") for (int h = 0; h < 2; ++h) \
        bst4<16>(rs, oh + hst + h * 57344 + (p) * 1024, u4v{o[4 * h], o[4 * h + 1], o[4 * h + 2], o[4 * h + 3]}); } while (0)
    for (int redo = 0;; ++redo) {
      do {
        MEMF();
#pragma unroll
        for (int ks = 0; ks < PA; ++ks) AVLD(ks);
        u32 m = 0;
#pragma unroll
        for (int ks = 0; ks < PA; ++ks) { m = mx4(m, av[0][ks]); m = mx4(m, av[1][ks]); }
        if (!anysent(m)) break;
      } while (--budget > 0);
      STAMP(kid, 4);
      TSTEP(31);
      SB();
#pragma unroll
      for (int jt = 0; jt < 7; ++jt) { acc[jt][0] = f4{0, 0, 0, 0}; acc[jt][1] = f4{0, 0, 0, 0}; }
      const char* lb = smem + l16;
      u4v wf[2][7];
#pragma unroll
      for (int jt = 0; jt < 7; ++jt) wf[0][jt] = *(const u4v*)(lb + jt * 1024);
      SB();
#pragma unroll
      for (int ks = 0; ks < 16; ++ks) {
        const int cu = ks & 1, nx = cu ^ 1;
        const bool chk = ks + 1 >= PA && ks < 15;
        u32 m = 0;
        unsigned long long bad = 0;
#pragma unroll
        for (int jt = 0; jt < 7; ++jt) {
          bf8v wfb = __builtin_bit_cast(bf8v, wf[cu][jt]);
          acc[jt][0] = __builtin_amdgcn_mfma_f32_16x16x32_bf16(wfb, __builtin_bit_cast(bf8v, av[0][ks]), acc[jt][0], 0, 0, 0);
          SB();
          if (ks < 15) wf[nx][jt] = *(const u4v*)(lb + ((ks + 1) * 7 + jt) * 1024);
          SB();
          acc[jt][1] = __builtin_amdgcn_mfma_f32_16x16x32_bf16(wfb, __builtin_bit_cast(bf8v, av[1][ks]), acc[jt][1], 0, 0, 0);
          SB();
          if (ks + PA < 16 && jt < 2) av[jt][ks + PA] = bld4s<FAUX>(rs, l16 + ((ks + PA + ct) & 15) * 1024, ol + (tt0 + jt) * 16384);
          if (chk && jt == 4) asm volatile("v_max_u32 %0, %1, %2" : "=v"(m) : "v"(av[0][ks + 1].x), "v"(av[1][ks + 1].x));
          if (chk && jt == 5) CMPS(bad, m);
          SB();
        }
        TSTEP(ks);
        if (chk) {
          MEMF();
          if (__builtin_expect(bad != 0, 0)) {
            NSLOW();
            do {
              MEMF();
#pragma unroll
              for (int k2 = ks + 1; k2 < 16 && k2 <= ks + PA; ++k2) AVLD(k2);
              wait_vm0();
            } while (anysent(mx4(mx4(0, av[0][ks + 1]), av[1][ks + 1])) && --budget > 0);
          }
          SB();
        }
      }
      STAMP(kid, 5);
      const bool nan = __builtin_amdgcn_ballot_w64(__builtin_isunordered(acc[0][0][0], acc[0][1][0])) != 0;
      GELU8(0, o0);
      MEMF();
      if (__builtin_expect(!nan || redo >= 3, 1)) break;
    }
    HST8(0, o0);
    {
      u32 o[8];
      GELU8(1, o);
      HST8(1, o);
      GELU8(2, o);
      HST8(2, o);
    }
    {
      f4 a0 = acc[6][0], a1 = acc[6][1];
      f2 a[4] = {f2{a0[0], a0[1]}, f2{a0[2], a0[3]}, f2{a1[0], a1[1]}, f2{a1[2], a1[3]}};
      f2 bs[4] = {unpk(bias[6].x), unpk(bias[6].y), unpk(bias[6].x), unpk(bias[6].y)};
      u32 o4[4];
      SB();
      if (HPOS == 3) {
#pragma unroll
        for (int n = 0; n < PD; ++n) HFLDF(n);
      }
      SB();
      actN<4>(a, bs, o4);
      auto rx = __builtin_amdgcn_permlane16_swap(o4[0], o4[2], false, false);
      auto ry = __builtin_amdgcn_permlane16_swap(o4[1], o4[3], false, false);
      bst4<16>(rs, oh + htl, u4v{rx[0], ry[0], rx[1], ry[1]});
    }
  }
  STAMP(kid, 6);
  R1Tw tw;
  {
    f4 acc[4][4];
    u4v bb0 = *(const u4v*)(b2 + 64 * ct2 + 16 * g), bb1 = *(const u4v*)(b2 + 64 * ct2 + 16 * g + 8);
    const u32 pvo = oh + (((4 * rt2 + w) * 56 + 7 * (4 * (rt2 & 1) + w) + 3 * (lane >> 5)) * 64 + (lane & 31)) * 16;
    u32 pv = 0;
    u4v x1a, x1b;
    f4* red = (f4*)(smem + LDS_W1);
    for (int redo = 0;; ++redo) {
      SB();
      MEMF();
      if (__builtin_expect(redo != 0, 0)) {
#pragma unroll
        for (int n = 0; n < PD; ++n) HFLD(n);
        wait_vm0();
      }
      if (anysent(HFMX(0))) {
        do {
          MEMF();
#pragma unroll
          for (int n = 0; n < PD; ++n) HFLD(n);
          wait_vm0();
        } while (anysent(HFMX(0)) && --budget > 0);
      }
#pragma unroll
      for (int i = 0; i < 4; ++i)
#pragma unroll
        for (int h = 0; h < 4; ++h) acc[i][h] = f4{0, 0, 0, 0};
      STAMP(kid, 8);
      TSTEP(30);
      SB();
#pragma unroll
      for (int n = 0; n < 14; ++n) {
        u32 m = 0;
        unsigned long long bad = 0;
#pragma unroll
        for (int q = 0; q < 8; ++q) {
          const int h = q >> 1, i = 2 * (q & 1);
          acc[i][h] = __builtin_amdgcn_mfma_f32_16x16x32_bf16(__builtin_bit_cast(bf8v, w2f[n][i]), __builtin_bit_cast(bf8v, hf[n][h]), acc[i][h], 0, 0, 0);
          SB();
          if (n + PD < 14 && q < 4) hf[n + PD][q] = bld4s<FAUX>(rs, l16 + KI(n + PD) * 1024, hoff[q] + KB(n + PD) * 1024);
          if (n == 4 && q == 4) pv = bld1<16>(rs, pvo);
          if (n < 13 && q == 5) asm volatile("v_max3_u32 %0, %1, %2, %3" : "=v"(m) : "v"(hf[n + 1][0].x), "v"(hf[n + 1][1].x), "v"(hf[n + 1][2].x));
          if (n < 13 && q == 6) CMPS(bad, m);
          SB();
          acc[i + 1][h] = __builtin_amdgcn_mfma_f32_16x16x32_bf16(__builtin_bit_cast(bf8v, w2f[n][i + 1]), __builtin_bit_cast(bf8v, hf[n][h]), acc[i + 1][h], 0, 0, 0);
          SB();
          if (n < 13 && n != 8 && q == 5) asm volatile("v_max_u32 %0, %0, %1" : "+v"(m) : "v"(hf[n + 1][3].x));
          if (n == 8 && q == 5) asm volatile("v_max3_u32 %0, %0, %1, %2" : "+v"(m) : "v"(hf[n + 1][3].x), "v"(pv));
          SB();
        }
        TSTEP(16 + n);
        if (n < 13) {
          MEMF();
          if (__builtin_expect(bad != 0, 0)) {
            NSLOW();
            do {
              MEMF();
#pragma unroll
              for (int n2 = n + 1; n2 < 14 && n2 <= n + PD; ++n2) HFLD(n2);
              if (n == 8) pv = bld1<16>(rs, pvo);
              wait_vm0();
              m = HFMX(n + 1);
              if (n == 8) m = mx2(m, pv);
            } while (anysent(m) && --budget > 0);
          }
          SB();
        }
        if (n == 8) {
          MEMF();
          x1a = bld4<16>(rs, OFF_X1P + rho2 * 1024 + ct2 * 128 + g * 32);
          x1b = bld4<16>(rs, OFF_X1P + rho2 * 1024 + ct2 * 128 + g * 32 + 16);
          SB();
        }
      }
      STAMP(kid, 9);
      r1_load_tw(tw, t);
      SB();
      const bool nan = __builtin_amdgcn_ballot_w64(__builtin_isunordered(acc[0][0][0], acc[0][1][0]) || __builtin_isunordered(acc[0][2][0], acc[0][3][0])) != 0;
#pragma unroll
      for (int h = 1; h < 4; ++h)
#pragma unroll
        for (int i = 0; i < 4; ++i) red[((((w + h) & 3) * 3 + (h - 1)) * 4 + i) * 64 + lane] = acc[i][h];
      MEMF();
      if (__builtin_expect(!nan || redo >= 3, 1)) break;
    }
    bar();
    f2 s[8];
#pragma unroll
    for (int i = 0; i < 4; ++i) {
      f4 a = (acc[i][0] + red[((w * 3 + 0) * 4 + i) * 64 + lane]) + (red[((w * 3 + 1) * 4 + i) * 64 + lane] + red[((w * 3 + 2) * 4 + i) * 64 + lane]);
      s[2 * i] = f2{a[0], a[1]}; s[2 * i + 1] = f2{a[2], a[3]};
    }
    const u32 x1v[8] = {x1a.x, x1a.y, x1a.z, x1a.w, x1b.x, x1b.y, x1b.z, x1b.w};
    u32 bs[8] = {bb0.x, bb0.y, bb0.z, bb0.w, bb1.x, bb1.y, bb1.z, bb1.w};
    u32 o[8];
#pragma unroll
    for (int i = 0; i < 8; ++i) o[i] = pack2(unpk(x1v[i]) + rbf2(rbf2(s[i]) + unpk(bs[i])));
#pragma unroll
    for (int i = 0; i < 8; ++i) o[i] = nosent(o[i]);
    char* st = smem + LDS_W1 + w * 12288;
    wbar();
    *(u4v*)(st + c * 144 + g * 32) = u4v{o[0], o[1], o[2], o[3]};
    *(u4v*)(st + c * 144 + g * 32 + 16) = u4v{o[4], o[5], o[6], o[7]};
    wbar();
    const u4v ra = *(const u4v*)(st + (lane >> 3) * 144 + (lane & 7) * 16), rb = *(const u4v*)(st + ((lane >> 3) + 8) * 144 + (lane & 7) * 16);
    const u32 xo = ((256 * (lane >> 3) + 8 * rt2 + 2 * w) * D_ + 64 * ct2) * 2 + (lane & 7) * 16;
    if (last) {
      *(u4v*)((char*)out + xo) = ra;
      *(u4v*)((char*)out + xo + 1024) = rb;
      STAMP(kid, 10);
      return;
    }
    bst4<16>(rs, OFF_XR + xo, ra);
    bst4<16>(rs, OFF_XR + xo + 1024, rb);
  }
  STAMP(kid, 10);
  asm volatile("" : "+v"(t));
  {
    const u32 ro = OFF_XR + ((256 * (t >> 5) + s2) * 256 + (t & 31)) * 4;
    wait_vm0();
    do {
      MEMF();
#pragma unroll
      for (int m = 0; m < 8; ++m) xin[m] = bld1<16>(rs, ro + 128 * m);
    } while (anysent(mx2(mx2(mx2(xin[0], xin[1]), mx2(xin[2], xin[3])), mx2(mx2(xin[4], xin[5]), mx2(xin[6], xin[7])))) && --budget > 0);
    STAMP(kid, 11);
    r1_body<16, 1536, RP>(xin, tw, tx, lds, rs, s2, t);
    STAMP(kid, 12);
  }
  {
    SB();
    const u32 tv = (t >> 2) * TPB + (t & 3) * 16;
    CIn ci;
    c_load(ci, rs, t, kp, qd, special);
    SB();
    wait_vm0();
    __builtin_amdgcn_s_barrier();
    MEMF();
    STAMP(3, 0);
    u4v in[4];
    do {
      MEMF();
#pragma unroll
      for (int m = 0; m < 4; ++m) in[m] = bld4s<16>(rs, tv, ts + m * 64 * TPB);
      const u4v a = (in[0] | in[1]) | (in[2] | in[3]);
      if (__builtin_amdgcn_ballot_w64((((a.x | a.y) | (a.z | a.w)) & 1u) != 0) == 0) break;
    } while (--budget > 0);
    STAMP(3, 1);
    c_body(in, ci, lds, rs, tv, ts, t, kp, special, [&](int) __attribute__((always_inline)) {});
    STAMP(3, 2);
  }
  last = 1;
  }
}

extern "C" __device__ __attribute__((used, noinline)) void z_end() {}

extern "C" void kernel_launch(void* const* d_in, const int* in_sizes, int n_in, void* d_out, int out_size, void* d_ws,
                              size_t ws_size, hipStream_t stream) {
  const unsigned short* x = (const unsigned short*)d_in[0];
  const unsigned short* wr = (const unsigned short*)d_in[1];
  const unsigned short* wi = (const unsigned short*)d_in[2];
  const unsigned short* w1 = (const unsigned short*)d_in[4];
  const unsigned short* b1 = (const unsigned short*)d_in[5];
  const unsigned short* w2 = (const unsigned short*)d_in[6];
  const unsigned short* b2 = (const unsigned short*)d_in[7];
  char* ws = (char*)d_ws;
  (void)hipFuncSetAttribute((const void*)k_m, hipFuncAttributeMaxDynamicSharedMemorySize, LDS_M);
  k_first<<<256, 256, 0, stream>>>((const u32*)x, wr, wi, w1, w2, ws);
  k_m<<<256, 256, LDS_M, stream>>>((const u32*)x, b1, b2, ws, (unsigned short*)d_out);
}
```

```cpp
#include <hip/hip_runtime.h>
#include <hip/hip_bf16.h>
#include <stdint.h>

typedef unsigned int u32;
typedef float f2 __attribute__((ext_vector_type(2)));
typedef float f4 __attribute__((ext_vector_type(4)));
typedef float f16v __attribute__((ext_vector_type(16)));
typedef u32 u2v __attribute__((ext_vector_type(2)));
typedef u32 u4v __attribute__((ext_vector_type(4)));
typedef __bf16 bf2v __attribute__((ext_vector_type(2)));
typedef __bf16 bf8v __attribute__((ext_vector_type(8)));
typedef short s4v __attribute__((ext_vector_type(4)));
typedef short s8v __attribute__((ext_vector_type(8)));
#define LAS __attribute__((address_space(3)))
#define DI __device__ __forceinline__
#define RLX_AGENT __ATOMIC_RELAXED, __HIP_MEMORY_SCOPE_AGENT

#define STAMP(K, I)

constexpr int S_ = 2048, D_ = 512, F_ = 1792, DF_ = 257;
constexpr u32 TPB = 16384 + 256;

constexpr u32 OFF_CNT = 0;
constexpr u32 OFF_T = 65536;
constexpr u32 SZ_T = 256u * TPB;
constexpr u32 OFF_WT = OFF_T + SZ_T;
constexpr u32 SZ_WT = 256u * 256 * 8 * 4;
constexpr u32 OFF_PQ = OFF_WT + SZ_WT;
constexpr u32 SZ_PQ = 256u * 8 * 16;
constexpr u32 OFF_W2F = OFF_PQ + SZ_PQ;
constexpr u32 SZ_W2F = (u32)F_ * D_ * 2;
constexpr u32 OFF_W1F = OFF_W2F + SZ_W2F;
constexpr u32 OFF_XR = OFF_W1F + SZ_W2F;
constexpr u32 SZ_X = (u32)S_ * D_ * 2;
constexpr u32 OFF_X1P = OFF_XR + SZ_X;
constexpr u32 OFF_LNF = OFF_X1P + SZ_X;
constexpr u32 OFF_H = OFF_LNF + SZ_X;
constexpr u32 SZ_H = (u32)S_ * F_ * 2;
constexpr u32 OFF_LNF2 = OFF_H + SZ_H;
constexpr u32 OFF_H2 = OFF_LNF2 + SZ_X;
constexpr u32 WS_USED = OFF_H2 + SZ_H;

struct cxf { float x, y; };
constexpr double kPi = 3.14159265358979323846264338327950288;
constexpr double c_sin(double x) { double x2 = x * x, t = x, s = x; for (int i = 1; i < 11; ++i) { t *= -x2 / double((2 * i) * (2 * i + 1)); s += t; } return s; }
constexpr double c_cos(double x) { double x2 = x * x, t = 1, s = 1; for (int i = 1; i < 11; ++i) { t *= -x2 / double((2 * i - 1) * (2 * i)); s += t; } return s; }
constexpr cxf tw2048(int i) {
  i &= 2047;
  int qd = i >> 9, r = i & 511;
  double c = 0, s = 0;
  if (r <= 256) { double a = 2 * kPi * r / 2048.0; c = c_cos(a); s = c_sin(a); }
  else { double a = 2 * kPi * (512 - r) / 2048.0; c = c_sin(a); s = c_cos(a); }
  double cc = 0, ss = 0;
  if (qd == 0) { cc = c; ss = s; } else if (qd == 1) { cc = -s; ss = c; } else if (qd == 2) { cc = -c; ss = -s; } else { cc = s; ss = -c; }
  return cxf{(float)cc, (float)(-ss)};
}
template <int R, int C> struct Tab { cxf v[R * C]; };
constexpr Tab<32, 8> mk_r1() { Tab<32, 8> t{}; for (int j = 0; j < 32; ++j) for (int q = 0; q < 8; ++q) t.v[j * 8 + q] = tw2048(8 * j * q); return t; }
constexpr Tab<8, 8> mk_r2() { Tab<8, 8> t{}; for (int j = 0; j < 8; ++j) for (int q = 0; q < 8; ++q) t.v[j * 8 + q] = tw2048(64 * j * q); return t; }
constexpr Tab<256, 1> mk_512() { Tab<256, 1> t{}; for (int c = 0; c < 256; ++c) t.v[c] = tw2048(4 * c + 512); return t; }
constexpr Tab<32, 8> mk_512u() { Tab<32, 8> t{}; for (int u = 0; u < 32; ++u) for (int i = 0; i < 8; ++i) t.v[u * 8 + i] = tw2048(4 * (u + 32 * (i >> 2) + 64 * (i & 3)) + 512); return t; }
constexpr Tab<32, 22> mk_r2all() {
  Tab<32, 22> t{};
  for (int j = 0; j < 32; ++j) {
    cxf v[22] = {};
    for (int i = 0; i < 8; ++i) v[i] = tw2048(4 * (j + 32 * (i >> 2) + 64 * (i & 3)) + 512);
    for (int a = 0; a < 2; ++a) for (int i = 1; i < 4; ++i) v[8 + 3 * a + (i - 1)] = tw2048(64 * ((j >> 3) + 4 * a) * i);
    for (int q = 1; q < 8; ++q) v[13 + q] = tw2048(8 * j * q);
    for (int i = 0; i < 22; ++i) t.v[((i >> 1) * 32 + j) * 2 + (i & 1)] = v[i];
  }
  return t;
}
constexpr Tab<32, 14> mk_r1all() {
  Tab<32, 14> t{};
  for (int j = 0; j < 32; ++j)
    for (int i = 0; i < 14; ++i) t.v[((i >> 1) * 32 + j) * 2 + (i & 1)] = i < 7 ? tw2048(8 * j * (i + 1)) : tw2048(64 * (j & 3) * (i - 6));
  return t;
}
constexpr Tab<256, 8> mk_x() { Tab<256, 8> t{}; for (int s = 0; s < 256; ++s) for (int k = 0; k < 8; ++k) t.v[s * 8 + k] = tw2048(s * k); return t; }
constexpr Tab<64, 4> mk_c1() { Tab<64, 4> t{}; for (int j = 0; j < 64; ++j) for (int q = 0; q < 4; ++q) t.v[j * 4 + q] = tw2048(8 * j * q); return t; }
constexpr double kSc = 6.9053396600248785e-04;
constexpr cxf tw2048s(int i) {
  i &= 2047;
  int qd = i >> 9, r = i & 511;
  double c = 0, s = 0;
  if (r <= 256) { double a = 2 * kPi * r / 2048.0; c = c_cos(a); s = c_sin(a); }
  else { double a = 2 * kPi * (512 - r) / 2048.0; c = c_sin(a); s = c_cos(a); }
  double cc = 0, ss = 0;
  if (qd == 0) { cc = c; ss = s; } else if (qd == 1) { cc = -s; ss = c; } else if (qd == 2) { cc = -c; ss = -s; } else { cc = s; ss = -c; }
  return cxf{(float)(cc * kSc), (float)(-ss * kSc)};
}
constexpr Tab<8, 8> mk_c2() { Tab<8, 8> t{}; for (int j = 0; j < 8; ++j) for (int q = 0; q < 8; ++q) t.v[((q >> 1) * 8 + j) * 2 + (q & 1)] = tw2048s(32 * j * q); return t; }

__device__ const __attribute__((aligned(64))) Tab<32, 14> g_r1all = mk_r1all();
__device__ const __attribute__((aligned(64))) Tab<256, 1> g_tw512 = mk_512();
__device__ const __attribute__((aligned(64))) Tab<32, 22> g_r2all = mk_r2all();
__device__ const __attribute__((aligned(64))) Tab<256, 8> g_twx = mk_x();
__device__ const __attribute__((aligned(64))) Tab<64, 4> g_twc1 = mk_c1();
__device__ const __attribute__((aligned(64))) Tab<8, 8> g_twc2 = mk_c2();

DI float bf_lo(u32 v) { return __uint_as_float(v << 16); }
DI float bf_hi(u32 v) { return __uint_as_float(v & 0xffff0000u); }
DI f2 unpk(u32 v) { return f2{bf_lo(v), bf_hi(v)}; }
DI float rbf(float x) { return (float)(__bf16)x; }
DI u32 pack2(f2 a) { bf2v v = __builtin_convertvector(a, bf2v); return __builtin_bit_cast(u32, v); }
DI f2 rbf2(f2 a) { return unpk(pack2(a)); }
DI f2 swp(f2 a) { return __builtin_shufflevector(a, a, 1, 0); }
DI f2 fma2(f2 a, f2 b, f2 c) { return __builtin_elementwise_fma(a, b, c); }
DI f2 xx(f2 a) { return __builtin_shufflevector(a, a, 0, 0); }
DI f2 yy(f2 a) { return __builtin_shufflevector(a, a, 1, 1); }
DI f2 cmul(f2 a, f2 b) {
  f2 t, r;
  asm("v_pk_mul_f32 %0, %1, %2 op_sel:[1,1] op_sel_hi:[1,0]" : "=v"(t) : "v"(a), "v"(b));
  asm("v_pk_fma_f32 %0, %1, %2, %3 op_sel:[0,0,0] op_sel_hi:[0,1,1] neg_lo:[0,0,1]" : "=v"(r) : "v"(a), "v"(b), "v"(t));
  return r;
}
DI f2 cmulc(f2 a, f2 b) {
  f2 t, r;
  asm("v_pk_mul_f32 %0, %1, %2 op_sel:[1,1] op_sel_hi:[1,0]" : "=v"(t) : "v"(a), "v"(b));
  asm("v_pk_fma_f32 %0, %1, %2, %3 op_sel:[0,0,0] op_sel_hi:[0,1,1] neg_hi:[0,1,0]" : "=v"(r) : "v"(a), "v"(b), "v"(t));
  return r;
}
DI f2 cmul_s(f2 a, f2 b) {
  f2 t, r;
  asm("v_pk_mul_f32 %0, %1, %2 op_sel:[1,1] op_sel_hi:[1,0]" : "=v"(t) : "v"(a), "s"(b));
  asm("v_pk_fma_f32 %0, %1, %2, %3 op_sel:[0,0,0] op_sel_hi:[0,1,1] neg_lo:[0,0,1]" : "=v"(r) : "v"(a), "s"(b), "v"(t));
  return r;
}
DI f2 cmulc_s(f2 a, f2 b) {
  f2 t, r;
  asm("v_pk_mul_f32 %0, %1, %2 op_sel:[1,1] op_sel_hi:[1,0]" : "=v"(t) : "v"(a), "s"(b));
  asm("v_pk_fma_f32 %0, %1, %2, %3 op_sel:[0,0,0] op_sel_hi:[0,1,1] neg_hi:[0,1,0]" : "=v"(r) : "v"(a), "s"(b), "v"(t));
  return r;
}
DI f2 cmul_add(f2 a, f2 b, f2 e) {
  f2 t, r;
  asm("v_pk_fma_f32 %0, %1, %2, %3 op_sel:[1,1,0] op_sel_hi:[1,0,1] neg_lo:[0,1,0]" : "=v"(t) : "v"(a), "v"(b), "v"(e));
  asm("v_pk_fma_f32 %0, %1, %2, %3 op_sel:[0,0,0] op_sel_hi:[0,1,1]" : "=v"(r) : "v"(a), "v"(b), "v"(t));
  return r;
}
DI f2 cmulc_add(f2 a, f2 b, f2 e) {
  f2 t, r;
  asm("v_pk_fma_f32 %0, %1, %2, %3 op_sel:[1,1,0] op_sel_hi:[1,0,1]" : "=v"(t) : "v"(a), "v"(b), "v"(e));
  asm("v_pk_fma_f32 %0, %1, %2, %3 op_sel:[0,0,0] op_sel_hi:[0,1,1] neg_hi:[0,1,0]" : "=v"(r) : "v"(a), "v"(b), "v"(t));
  return r;
}
DI f2 ccmul_add(f2 a, f2 b, f2 e) {
  f2 t, r;
  asm("v_pk_fma_f32 %0, %1, %2, %3 op_sel:[1,1,0] op_sel_hi:[1,0,1] neg_hi:[0,1,0]" : "=v"(t) : "v"(a), "v"(b), "v"(e));
  asm("v_pk_fma_f32 %0, %1, %2, %3 op_sel:[0,0,0] op_sel_hi:[0,1,1]" : "=v"(r) : "v"(a), "v"(b), "v"(t));
  return r;
}
template <bool INV> DI f2 addmi(f2 a, f2 d) { return fma2(swp(d), INV ? f2{-1.f, 1.f} : f2{1.f, -1.f}, a); }
template <bool INV> DI f2 submi(f2 a, f2 d) { return fma2(swp(d), INV ? f2{1.f, -1.f} : f2{-1.f, 1.f}, a); }

template <bool INV> DI void fft4(f2& b0, f2& b1, f2& b2, f2& b3) {
  f2 c0 = b0 + b2, c2 = b0 - b2, c1 = b1 + b3, d = b1 - b3;
  b0 = c0 + c1; b2 = c0 - c1; b1 = addmi<INV>(c2, d); b3 = submi<INV>(c2, d);
}
template <bool INV> DI void fft8(f2* v) {
  const float h = 0.70710678118654752440f;
  f2 a0 = v[0] + v[4], a4 = v[0] - v[4];
  f2 a1 = v[1] + v[5], a5 = v[1] - v[5];
  f2 a2 = v[2] + v[6], a6 = v[2] - v[6];
  f2 a3 = v[3] + v[7], a7 = v[3] - v[7];
  f2 c0 = addmi<INV>(a4, a6), c2 = submi<INV>(a4, a6);
  f2 u = addmi<INV>(a5, a7), w = submi<INV>(a5, a7);
  f2 tu = addmi<INV>(u, u), tw = addmi<INV>(w, w);
  fft4<INV>(a0, a1, a2, a3);
  v[0] = a0; v[2] = a1; v[4] = a2; v[6] = a3;
  v[1] = fma2(tu, f2{h, h}, c0);
  v[5] = fma2(tu, f2{-h, -h}, c0);
  v[3] = fma2(swp(tw), INV ? f2{-h, h} : f2{h, -h}, c2);
  v[7] = fma2(swp(tw), INV ? f2{h, -h} : f2{-h, h}, c2);
}
template <int CTRL> DI float dpp_add(float x) {
  return x + __int_as_float(__builtin_amdgcn_update_dpp(0, __float_as_int(x), CTRL, 0xf, 0xf, false));
}
DI float red32(float x) {
  x = dpp_add<0xB1>(x); x = dpp_add<0x4E>(x); x = dpp_add<0x141>(x); x = dpp_add<0x140>(x);
  auto r = __builtin_amdgcn_permlane16_swap(__float_as_uint(x), __float_as_uint(x), false, false);
  return __uint_as_float(r[0]) + __uint_as_float(r[1]);
}
DI void wbar() { __builtin_amdgcn_wave_barrier(); }
DI void bar() {
  asm volatile("s_waitcnt lgkmcnt(0)" ::: "memory");
  __builtin_amdgcn_s_barrier();
  asm volatile("" ::: "memory");
}
DI void wait_vm0() { __builtin_amdgcn_s_waitcnt(0x0F70); asm volatile("" ::: "memory"); }
DI void layernorm8(const f2* v, u32* o) {
  f2 s2 = ((v[0] + v[1]) + (v[2] + v[3])) + ((v[4] + v[5]) + (v[6] + v[7]));
  float mean = red32(s2.x + s2.y) * (1.0f / 512.0f);
  f2 mm = f2{mean, mean};
  f2 q0 = f2{0, 0}, q1 = f2{0, 0};
#pragma unroll
  for (int i = 0; i < 8; i += 2) { f2 d0 = v[i] - mm, d1 = v[i + 1] - mm; q0 = fma2(d0, d0, q0); q1 = fma2(d1, d1, q1); }
  q0 = q0 + q1;
  float q = red32(q0.x + q0.y);
  float m = rbf(mean), vr = rbf(q * (1.0f / 512.0f));
  float sd = rbf(__builtin_amdgcn_sqrtf(rbf(vr + 1e-5f)));
  float ri = __builtin_amdgcn_rcpf(sd);
  f2 m2 = f2{m, m}, r2 = f2{ri, ri};
#pragma unroll
  for (int i = 0; i < 8; ++i) o[i] = pack2(rbf2(v[i] - m2) * r2);
}
DI f2 ldtw(const cxf* p) { return *(const f2*)p; }
typedef __amdgpu_buffer_rsrc_t rsrc_t;
DI rsrc_t mk_rsrc(void* p, u32 bytes) { return __builtin_amdgcn_make_buffer_rsrc(p, 0, bytes, 0x00020000); }
template <int AUX> DI u4v bld4(rsrc_t r, u32 off) { return __builtin_amdgcn_raw_buffer_load_b128(r, off, 0, AUX); }
template <int AUX> DI u32 bld1(rsrc_t r, u32 off) { return __builtin_amdgcn_raw_buffer_load_b32(r, off, 0, AUX); }
template <int AUX> DI void bst4(rsrc_t r, u32 off, u4v v) { __builtin_amdgcn_raw_buffer_store_b128(v, r, off, 0, AUX); }
DI u4v f4u(f2 a, f2 b) { return u4v{__float_as_uint(a.x), __float_as_uint(a.y), __float_as_uint(b.x), __float_as_uint(b.y)}; }

constexpr int RP = 288;

constexpr u32 SENT = 0xFFFFFFFFu;
DI u32 nosent(u32 v) { return v < SENT - 1 ? v : SENT - 1; }
DI u32 mx2(u32 a, u32 b) { return a > b ? a : b; }
DI u32 mx4(u32 m, u4v a) { return mx2(mx2(mx2(mx2(m, a.x), a.y), a.z), a.w); }
DI bool anysent(u32 m) { return __builtin_amdgcn_ballot_w64(m == SENT) != 0; }
struct R1Tw { f2 tw1[8], tw2[8], w512; };
DI void r1_load_tw(R1Tw& tw, int t) {
  const f4* p = (const f4*)&g_r1all + (t & 31);
  f2 v[14];
#pragma unroll
  for (int i = 0; i < 7; ++i) { f4 q = p[32 * i]; v[2 * i] = f2{q[0], q[1]}; v[2 * i + 1] = f2{q[2], q[3]}; }
#pragma unroll
  for (int q = 1; q < 8; ++q) { tw.tw1[q] = v[q - 1]; tw.tw2[q] = v[6 + q]; }
  tw.w512 = ldtw(&g_tw512.v[t]);
}
DI u4v tag0(u4v v) { return v & u4v{~1u, ~1u, ~1u, ~1u}; }
DI u4v tag1(u4v v) { return v | u4v{1u, 1u, 1u, 1u}; }
template <int AUX, int RW, int R0> DI void r1_body(const u32* xin, const R1Tw& tw, const f16v& tx, f2* lds, rsrc_t rs, int s2, int t) {
  const int s1 = t >> 5, j = t & 31;
  f2 z[8];
#pragma unroll
  for (int m = 0; m < 8; ++m) z[m] = unpk(xin[m]);
  u32 o[8];
  layernorm8(z, o);
#pragma unroll
  for (int m = 0; m < 8; ++m) z[m] = unpk(o[m]);
  f2* L = lds + R0 + (s1 >> 1) * RW + (s1 & 1) * RP;
  fft8<false>(z);
#pragma unroll
  for (int q = 1; q < 8; ++q) z[q] = cmul(z[q], tw.tw1[q]);
#pragma unroll
  for (int q = 0; q < 8; ++q) L[q * 36 + j] = z[q];
  wbar();
#pragma unroll
  for (int m = 0; m < 8; ++m) z[m] = L[(j >> 2) * 36 + (j & 3) + 4 * m];
  wbar();
  fft8<false>(z);
#pragma unroll
  for (int q = 1; q < 8; ++q) z[q] = cmul(z[q], tw.tw2[q]);
#pragma unroll
  for (int q = 0; q < 8; ++q) L[(j & 3) * 68 + 8 * q + (j >> 2)] = z[q];
  wbar();
#pragma unroll
  for (int i = 0; i < 8; ++i) z[i] = L[(i & 3) * 68 + 32 * (i >> 2) + j];
  wbar();
  fft4<false>(z[0], z[1], z[2], z[3]);
  fft4<false>(z[4], z[5], z[6], z[7]);
#pragma unroll
  for (int i = 0; i < 8; ++i) L[j + 32 * (i >> 2) + 64 * (i & 3)] = z[i];
  bar();
  f2 X[8];
#pragma unroll
  for (int r = 0; r < 8; ++r) {
    const f2* Lr = lds + R0 + (r >> 1) * RW + (r & 1) * RP;
    f2 A = Lr[t], Bc = Lr[(256 - t) & 255];
    f2 E = fma2(Bc, f2{1.f, -1.f}, A);
    f2 Dm = fma2(Bc, f2{-1.f, 1.f}, A);
    f2 x2 = cmul_add(Dm, tw.w512, E);
    if (t == 0) x2.y = E.x - Dm.y;
    X[r] = x2;
  }
  fft8<false>(X);
#pragma unroll
  for (int k = 1; k < 8; ++k) X[k] = cmul_s(X[k], f2{tx[2 * k], tx[2 * k + 1]});
  const u32 dst = OFF_T + s2 * TPB + t * 16;
  bst4<AUX>(rs, dst, tag0(f4u(X[0], X[4])));
  bst4<AUX>(rs, dst + 4096, tag0(f4u(X[1], X[7])));
  bst4<AUX>(rs, dst + 8192, tag0(f4u(X[2], X[6])));
  bst4<AUX>(rs, dst + 12288, tag0(f4u(X[3], X[5])));
}

DI int f_of(int kap, int g, int j) {
  int u = kap / 7, r = kap % 7;
  if (r < 6) { int ct = 2 * u + (r >= 3), p = r % 3; return 112 * ct + 32 * p + (j < 4 ? 4 * g + j : 16 + 4 * g + (j - 4)); }
  return 112 * (2 * u + (g >> 1)) + 96 + 8 * (g & 1) + j;
}
constexpr int WPIT = 528;
#define ARM_A(PARTS) do { if ((PARTS) && first) { \
    const u4v sv = u4v{SENT, SENT, SENT, SENT}; \
    const int xcd_ = b & 7, ii_ = b >> 3, w_ = __builtin_amdgcn_readfirstlane(t >> 6), ln_ = t & 63, g_ = ln_ >> 4; \
    const int s2_ = 32 * xcd_ + ii_, rt_ = 2 * xcd_ + (ii_ >> 4), ct_ = ii_ & 15, rt2_ = 4 * xcd_ + (ii_ >> 3), ct2_ = ii_ & 7; \
    if ((PARTS) & 1) { \
      const u32 co = OFF_LNF + ((s2_ >> 1) * 16 + 4 * w_) * 1024 + (ln_ >> 3) * 256 + (s2_ & 1) * 128 + (ln_ & 7) * 16; \
      bst4<CAUX>(rs, co, sv); bst4<CAUX>(rs, co + 2048, sv); \
      const u32 xo = OFF_XR + ((256 * (ln_ >> 3) + 8 * rt2_ + 2 * w_) * D_ + 64 * ct2_) * 2 + (ln_ & 7) * 16; \
      bst4<CAUX>(rs, xo, sv); bst4<CAUX>(rs, xo + 1024, sv); } \
    if ((PARTS) & 2) { \
      const int tt0_ = 8 * rt_ + 2 * w_, kb_ = 7 * (ct_ >> 1) + 3 * (ct_ & 1); \
      _Pragma("unroll") for (int h = 0; h < 2; ++h) _Pragma("unroll") for (int p = 0; p < 3; ++p) \
        bst4<CAUX>(rs, OFF_H + ((tt0_ + h) * 56 + kb_ + p) * 1024 + ln_ * 16, sv); \
      bst4<CAUX>(rs, OFF_H + (((tt0_ + (g_ & 1)) * 56 + 7 * (ct_ >> 1) + 6) * 64 + (2 * (ct_ & 1) + (g_ >> 1)) * 16 + (ln_ & 15)) * 16, sv); } \
    __builtin_amdgcn_sched_barrier(0); } } while (0)
#define CAUX 0
#define KF_ARM 3
#define KC_ARM (3 - (KF_ARM))
#define KF_P0 3
#define WAUX 0
#define TAUX 0
__global__ __launch_bounds__(256) void k_first(const u32* __restrict__ x, const unsigned short* __restrict__ wr,
                                               const unsigned short* __restrict__ wi, const unsigned short* __restrict__ w1,
                                               const unsigned short* __restrict__ w2, char* __restrict__ ws) {
  __shared__ __attribute__((aligned(16))) char smem[8 * RP * 8 + 32 * WPIT];
  const int b = blockIdx.x, t = threadIdx.x, w = __builtin_amdgcn_readfirstlane(t >> 6), lane = t & 63;
  STAMP(0, 0);
  asm volatile("" :: "s"(x), "s"(wr), "s"(wi), "s"(w1), "s"(w2), "s"(ws));
  const rsrc_t rs = mk_rsrc(ws, WS_USED);
  const bool first = true;
  const int s2 = 32 * (b & 7) + (b >> 3);
  u32 xin[8];
  const u32* xr = x + (size_t)(256 * (t >> 5) + s2) * 256 + (t & 31);
#pragma unroll
  for (int m = 0; m < 8; ++m) xin[m] = xr[32 * m];
  R1Tw tw;
  r1_load_tw(tw, t);
  const f16v tx = *(const f16v*)&g_twx.v[s2 * 8];
  unsigned short fr[8], fi[8];
#pragma unroll
  for (int i = 0; i < 8; ++i) { fr[i] = wr[(b + 256 * i) * DF_ + t]; fi[i] = wi[(b + 256 * i) * DF_ + t]; }
  u4v wu[4];
  const int isw2 = b >= 112;
  const int ua = isw2 ? (b - 112) >> 1 : b / 7, ub = isw2 ? (b - 112) & 1 : b % 7;
  if (b < 224) {
#pragma unroll
    for (int g = 0; g < 4; ++g) {
      const int i = t >> 5;
      const unsigned short* p;
      if (isw2) p = w2 + (size_t)f_of(ua, g, i) * D_;
      else p = w1 + (size_t)(256 * (g & 1) + 64 * (i >> 1) + 4 * ua + 2 * (g >> 1) + (i & 1)) * F_;
      wu[g] = *(const u4v*)(p + 256 * ub + (t & 31) * 8);
    }
  } else {
#pragma unroll
    for (int g = 0; g < 4; ++g) asm volatile("" : "=v"(wu[g]));
  }
  unsigned short pv[8] = {};
  if (w == 0) {
    const int k = b + 256 * (t & 7), km = (2048 - k) & 2047;
    pv[0] = wr[k * DF_]; pv[1] = wi[k * DF_]; pv[2] = wr[km * DF_]; pv[3] = wi[km * DF_];
    pv[4] = wr[k * DF_ + 256]; pv[5] = wi[k * DF_ + 256]; pv[6] = wr[km * DF_ + 256]; pv[7] = wi[km * DF_ + 256];
  }
  __builtin_amdgcn_sched_barrier(0);
  u32 pf0 = 0;
  if (w == 3 && b < 64) {
    u32 alo, ahi, elo, ehi;
    asm volatile("s_getpc_b64 s[40:41]\n\ts_mov_b32 %0, s40\n\ts_mov_b32 %1, s41" : "=s"(alo), "=s"(ahi) :: "s40", "s41");
    asm volatile("s_getpc_b64 s[40:41]\n\ts_add_u32 %0, s40, k_m@rel32@lo+4\n\ts_addc_u32 %1, s41, k_m@rel32@hi+12" : "=s"(elo), "=s"(ehi) :: "s40", "s41", "scc");
    const unsigned long long a = ((unsigned long long)ahi << 32) | alo, e = ((unsigned long long)ehi << 32) | elo;
    const u32 lim = e > a && e - a < 65536 ? (u32)(e - a) - 4 : 0u;
    const u32 o = lane * 128, oc = (o < lim ? o : lim) & ~3u;
    asm volatile("global_load_dword %0, %1, %2" : "=v"(pf0) : "v"(oc), "s"(a));
  }
  __builtin_amdgcn_sched_barrier(0);
  ARM_A(KF_ARM & KF_P0);
  if (b < 224) {
    char* im = smem + 8 * RP * 8;
#pragma unroll
    for (int g = 0; g < 4; ++g) *(u4v*)(im + (8 * g + (t >> 5)) * WPIT + (t & 31) * 16) = wu[g];
    bar();
    const int g = lane >> 4, q = (lane >> 2) & 3, p = lane & 3;
    const char* a0 = im + (8 * g + q) * WPIT + (isw2 ? 32 * p : 8 * p) + 128 * w;
#pragma unroll
    for (int n = 0; n < 4; ++n) {
      const char* a = a0 + (isw2 ? 8 * n : 32 * n);
      s4v lo = __builtin_amdgcn_ds_read_tr16_b64_v4i16((LAS s4v*)a);
      s4v hi = __builtin_amdgcn_ds_read_tr16_b64_v4i16((LAS s4v*)(a + 4 * WPIT));
      s8v wf = __builtin_shufflevector(lo, hi, 0, 1, 2, 3, 4, 5, 6, 7);
      u32 off;
      if (isw2) off = OFF_W2F + (((4 * ub + w) * 56 + ua) * 4 + n) * 1024;
      else { int ft = 16 * ub + 4 * w + n; off = OFF_W1F + (((ft / 7) * 16 + ua) * 7 + ft % 7) * 1024; }
      bst4<WAUX>(rs, off + lane * 16, __builtin_bit_cast(u4v, wf));
    }
  }
  {
    const int k1 = b & 7, kp = k1 <= 4 ? (k1 & 3) : 8 - k1, e = k1 >= 4;
    const int tl = e * 32 + ((b >> 3) & 3) * 8 + (b >> 5);
    u32 o[8];
#pragma unroll
    for (int i = 0; i < 8; ++i) o[i] = (u32)fr[i] | ((u32)fi[i] << 16);
    const u32 dst = OFF_WT + ((kp * 64 + (t >> 2)) * 512 + (t & 3) * 64 + tl) * 16;
    bst4<TAUX>(rs, dst, u4v{o[0], o[1], o[2], o[3]});
    bst4<TAUX>(rs, dst + 4096, u4v{o[4], o[5], o[6], o[7]});
    auto cv = [](unsigned short v) { return __uint_as_float((u32)v << 16); };
    float h0r = 0.5f * (cv(pv[0]) + cv(pv[2])), h0i = 0.5f * (cv(pv[1]) - cv(pv[3]));
    float h1r = 0.5f * (cv(pv[4]) + cv(pv[6])), h1i = 0.5f * (cv(pv[5]) - cv(pv[7]));
    if (t < 8) ((f4*)(ws + OFF_PQ))[(kp * 64 + tl) * 8 + t] = f4{0.5f * (h0r + h1r), 0.5f * (h0i + h1i), 0.5f * (h0r - h1r), 0.5f * (h0i - h1i)};
  }
  ARM_A(KF_ARM & ~KF_P0);
  STAMP(0, 1);
  u32 pf[7];
#pragma unroll
  for (int i = 0; i < 7; ++i) pf[i] = 0;
  asm volatile("" :: "v"(xin[0]), "v"(xin[1]), "v"(xin[2]), "v"(xin[3]), "v"(xin[4]), "v"(xin[5]), "v"(xin[6]), "v"(xin[7]), "v"(tw.w512),
               "v"(tw.tw1[1]), "v"(tw.tw1[3]), "v"(tw.tw1[5]), "v"(tw.tw1[7]), "v"(tw.tw2[2]), "v"(tw.tw2[4]), "v"(tw.tw2[6]));
  if (w == 3 && b < 64) {
    u32 alo, ahi, elo, ehi;
    asm volatile("s_getpc_b64 s[40:41]\n\ts_add_u32 %0, s40, k_m@rel32@lo+4\n\ts_addc_u32 %1, s41, k_m@rel32@hi+12" : "=s"(alo), "=s"(ahi) :: "s40", "s41", "scc");
    asm volatile("s_getpc_b64 s[40:41]\n\ts_add_u32 %0, s40, z_end@rel32@lo+4\n\ts_addc_u32 %1, s41, z_end@rel32@hi+12" : "=s"(elo), "=s"(ehi) :: "s40", "s41", "scc");
    const unsigned long long a = ((unsigned long long)ahi << 32) | alo, e = ((unsigned long long)ehi << 32) | elo;
    const u32 lim = e > a && e - a < 65536 ? (u32)(e - a) - 4 : 0u;
#pragma unroll
    for (int i = 0; i < 6; ++i) {
      const u32 o = (i * 64 + lane) * 128, oc = (o < lim ? o : lim) & ~3u;
      asm volatile("global_load_dword %0, %1, %2" : "=v"(pf[i]) : "v"(oc), "s"(a));
    }
    const char* tb = lane < 44 ? (const char*)&g_r2all + lane * 128 : lane < 60 ? (const char*)&g_twc1 + (lane - 44) * 128 : (const char*)&g_twc2 + (lane - 60) * 128;
    asm volatile("global_load_dword %0, %1, off" : "=v"(pf[6]) : "v"(tb));
  }
  __builtin_amdgcn_sched_barrier(0);
  r1_body<0, 2 * RP, 0>(xin, tw, tx, (f2*)smem, rs, s2, t);
  asm volatile("s_waitcnt vmcnt(0)" :: "v"(pf[0]), "v"(pf[1]), "v"(pf[2]), "v"(pf[3]), "v"(pf[4]), "v"(pf[5]), "v"(pf[6]), "v"(pf0));
  STAMP(0, 2);
}

constexpr int PK = 292, QS = 72;
DI int idx1(int k1, int q, int j) { return k1 * PK + q * QS + (j ^ ((k1 >> 2) << 2)); }
template <int AUX> DI u4v bld4s(rsrc_t r, u32 off, u32 soff) { return __builtin_amdgcn_raw_buffer_load_b128(r, off, soff, AUX); }
template <int AUX> DI void bst4s(rsrc_t r, u32 off, u32 soff, u4v v) { __builtin_amdgcn_raw_buffer_store_b128(v, r, off + soff, 0, AUX); }
struct CIn { u4v w0, w1; f2 tc1[4], tc2[8]; u4v pq[8]; };
template <class HK> DI void c_body(const u4v* in, const CIn& ci, f2* lds, rsrc_t rs, u32 tv, u32 ts, int t, int kp, bool special, HK hook) {
  const int cc = t & 3, j = t >> 2;
  const f2* tc1 = ci.tc1; const f2* tc2 = ci.tc2; const u4v* pq = ci.pq; const u4v w0 = ci.w0, w1 = ci.w1;
#pragma unroll
  for (int e = 0; e < 2; ++e) {
    f2 a[4];
#pragma unroll
    for (int m = 0; m < 4; ++m) a[m] = e ? f2{__uint_as_float(in[m].z), __uint_as_float(in[m].w)} : f2{__uint_as_float(in[m].x), __uint_as_float(in[m].y)};
    fft4<false>(a[0], a[1], a[2], a[3]);
#pragma unroll
    for (int q = 1; q < 4; ++q) a[q] = cmul(a[q], tc1[q]);
#pragma unroll
    for (int q = 0; q < 4; ++q) lds[idx1(2 * cc + e, q, j)] = a[q];
  }
  hook(1);
  bar();
  const int k1 = t >> 5, q = (t >> 3) & 3, jp = t & 7;
  const float sc = (float)kSc;
  f2 v[8];
#pragma unroll
  for (int m = 0; m < 8; ++m) v[m] = lds[idx1(k1, q, jp + 8 * m)];
  wbar();
  hook(2);
  fft8<false>(v);
  v[0] = v[0] * f2{sc, sc};
#pragma unroll
  for (int i = 1; i < 8; ++i) v[i] = cmul(v[i], tc2[i]);
  f2* L2 = lds + k1 * PK + q * QS;
#pragma unroll
  for (int i = 0; i < 8; ++i) L2[jp * 9 + i] = v[i];
  wbar();
#pragma unroll
  for (int i = 0; i < 8; ++i) v[i] = L2[i * 9 + jp];
  wbar();
  hook(3);
  fft8<false>(v);
  if (!special) {
    u32 w[8] = {w0.x, w0.y, w0.z, w0.w, w1.x, w1.y, w1.z, w1.w};
#pragma unroll
    for (int i = 0; i < 8; ++i) v[i] = cmul(v[i], unpk(w[i]));
  } else {
#pragma unroll
    for (int i = 0; i < 8; ++i) lds[k1 * PK + i * 32 + (t & 31)] = v[i];
    wbar();
    const bool z0 = kp == 0 && k1 == 0;
    const int u = q + 4 * jp, up = z0 ? (32 - u) & 31 : 31 - u;
    const int zi = (z0 && u == 0) ? 8 : 7;
    const f2* src = lds + (kp == 0 ? k1 : k1 ^ 1) * PK + (up & 3) * 8 + (up >> 2);
#pragma unroll
    for (int i = 0; i < 8; ++i) {
      f2 zm = src[((zi - i) & 7) * 32];
      f2 P = f2{__uint_as_float(pq[i].x), __uint_as_float(pq[i].y)}, Q = f2{__uint_as_float(pq[i].z), __uint_as_float(pq[i].w)};
      v[i] = ccmul_add(zm, Q, cmul(v[i], P));
    }
    wbar();
  }
  hook(4);
  fft8<true>(v);
  v[0] = v[0] * f2{sc, sc};
#pragma unroll
  for (int i = 1; i < 8; ++i) v[i] = cmulc(v[i], tc2[i]);
#pragma unroll
  for (int i = 0; i < 8; ++i) L2[i * 9 + jp] = v[i];
  wbar();
#pragma unroll
  for (int i = 0; i < 8; ++i) v[i] = L2[jp * 9 + i];
  wbar();
  hook(5);
  fft8<true>(v);
#pragma unroll
  for (int m = 0; m < 8; ++m) lds[idx1(k1, q, jp + 8 * m)] = v[m];
  hook(6);
  bar();
  f2 o[2][4];
#pragma unroll
  for (int e = 0; e < 2; ++e) {
#pragma unroll
    for (int qq = 0; qq < 4; ++qq) o[e][qq] = lds[idx1(2 * cc + e, qq, j)];
#pragma unroll
    for (int qq = 1; qq < 4; ++qq) o[e][qq] = cmulc(o[e][qq], tc1[qq]);
    fft4<true>(o[e][0], o[e][1], o[e][2], o[e][3]);
  }
#pragma unroll
  for (int m = 0; m < 4; ++m) bst4s<16>(rs, tv, ts + m * 64 * TPB, tag1(f4u(o[0][m], o[1][m])));
}

#define SB() __builtin_amdgcn_sched_barrier(0)
template <int N> DI void actN(const f2* a, const f2* bias, u32* o) {
  const float c0 = -2.0f * 0.7978845608028654f * 1.4426950408889634f, c1 = c0 * 0.044715f;
  f2 x[N], u[N];
#pragma unroll
  for (int i = 0; i < N; ++i) x[i] = rbf2(a[i]);
  SB();
#pragma unroll
  for (int i = 0; i < N; ++i) x[i] = x[i] + bias[i];
  SB();
#pragma unroll
  for (int i = 0; i < N; ++i) x[i] = rbf2(x[i]);
  SB();
#pragma unroll
  for (int i = 0; i < N; ++i) u[i] = x[i] * x[i];
  SB();
#pragma unroll
  for (int i = 0; i < N; ++i) u[i] = fma2(u[i], f2{c1, c1}, f2{c0, c0});
  SB();
#pragma unroll
  for (int i = 0; i < N; ++i) u[i] = u[i] * x[i];
  SB();
#pragma unroll
  for (int i = 0; i < N; ++i) u[i] = f2{__builtin_amdgcn_exp2f(u[i].x), __builtin_amdgcn_exp2f(u[i].y)};
  SB();
#pragma unroll
  for (int i = 0; i < N; ++i) u[i] = u[i] + f2{1.f, 1.f};
  SB();
#pragma unroll
  for (int i = 0; i < N; ++i) u[i] = f2{__builtin_amdgcn_rcpf(u[i].x), __builtin_amdgcn_rcpf(u[i].y)};
  SB();
#pragma unroll
  for (int i = 0; i < N; ++i) u[i] = u[i] * x[i];
  SB();
#pragma unroll
  for (int i = 0; i < N; ++i) o[i] = pack2(u[i]);
  SB();
#pragma unroll
  for (int i = 0; i < N; ++i) o[i] = nosent(o[i]);
  SB();
}

#define ALD4(dst, voff, sbase, imm) asm volatile("global_load_dwordx4 %0, %1, %2 offset:" #imm : "=v"(dst) : "v"(voff), "s"(sbase))
#define ALD4S(dst, voff, sbase) asm volatile("global_load_dwordx4 %0, %1, %2 sc1" : "=v"(dst) : "v"(voff), "s"(sbase) : "memory")
#define ALD1(dst, voff, sbase, imm) asm volatile("global_load_dword %0, %1, %2 offset:" #imm : "=v"(dst) : "v"(voff), "s"(sbase))
#define TSTEP(i)
#define NSLOW()
#define MEMF() asm volatile("" ::: "memory")
#define MX4F(m, a) do { asm volatile("v_max_u32 %0, %1, %2" : "=v"(m) : "v"((a).x), "v"((a).y)); \
    asm volatile("v_max3_u32 %0, %0, %1, %2" : "+v"(m) : "v"((a).z), "v"((a).w)); } while (0)
#define MX4(m, a) do { asm volatile("v_max3_u32 %0, %0, %1, %2" : "+v"(m) : "v"((a).x), "v"((a).y)); \
    asm volatile("v_max3_u32 %0, %0, %1, %2" : "+v"(m) : "v"((a).z), "v"((a).w)); } while (0)
constexpr int LDS_W1 = 114688;
constexpr int LDS_M = LDS_W1 + 49152;
constexpr int W2E = 7;
constexpr int PA = 6;
#define HPOS 3
#define MAUX 16
#define FAUX 0
#define PD 4

extern "C" __global__ __launch_bounds__(256, 1) void k_m(const u32* __restrict__ xres, const unsigned short* __restrict__ b1,
                                              const unsigned short* __restrict__ b2, char* ws, unsigned short* out) {
  extern __shared__ __attribute__((aligned(16))) char smem[];
  const rsrc_t rs = mk_rsrc(ws, WS_USED);
  f2* lds = (f2*)(smem + LDS_W1);
  int budget = 1 << 16;
#define KB(n) ((n) < 12 ? kq[(n) & 3] : ((n) == 12 ? kt0 : kt1))
#define KI(n) ((n) < 12 ? (n) >> 2 : 0)
#define W2LD1(n, i) w2f[n][i] = bld4s<0>(rs, l16 + (i) * 1024, w2s + (KB(n) + KI(n)) * 4096)
#define W2LD(n) do { _Pragma("unroll") for (int i_ = 0; i_ < 4; ++i_) W2LD1(n, i_); } while (0)
  const rsrc_t rx = mk_rsrc((void*)xres, SZ_X);
  int last = 0;
#pragma nounroll
  for (;;) {
  asm volatile("" : "+s"(last));
  int t = threadIdx.x;
  asm volatile("" : "+v"(t));
  int b = blockIdx.x, w = __builtin_amdgcn_readfirstlane(threadIdx.x >> 6);
  asm volatile("" : "+s"(b), "+s"(w));
  const int xcd = b & 7, ii = b >> 3;
  const int s2 = 32 * xcd + ii, rt = 2 * xcd + (ii >> 4), ct = ii & 15, rt2 = 4 * xcd + (ii >> 3), ct2 = ii & 7;
  const int tt0 = 8 * rt + 2 * w, kb = 7 * (ct >> 1) + 3 * (ct & 1);
  const int rot = ct2 & 3;
  int kq[4];
#pragma unroll
  for (int q = 0; q < 4; ++q) { const int pr = (q + rot) & 3; kq[q] = 14 * w + 7 * (pr >> 1) + 3 * (pr & 1); }
  const int kt0 = 14 * w + 6 + 7 * (ct2 & 1), kt1 = 14 * w + 13 - 7 * (ct2 & 1);
  const int qd = 8 * (b & 7) + ((b >> 3) & 7), kp = b >> 6;
  const bool special = qd == 0 && w == 0;
  const u32 ts = OFF_T + kp * 4096 + qd * 64;
  const u32 w2s = OFF_W2F + ct2 * 56 * 4096;
  const f16v tx = *(const f16v*)&g_twx.v[s2 * 8];
  const int lane = t & 63, g = lane >> 4, c = lane & 15;
  const u32 l16 = lane * 16;
  const int rho2 = 64 * rt2 + 16 * w + c;
  const u32 lno = ((s2 >> 1) * 16 + 4 * w) * 1024 + (lane >> 3) * 256 + (s2 & 1) * 128 + (lane & 7) * 16;
  const u32 hst = (tt0 * 56 + kb) * 1024 + l16;
  const u32 htl = (((tt0 + (g & 1)) * 56 + 7 * (ct >> 1) + 6) * 64 + (2 * (ct & 1) + (g >> 1)) * 16 + c) * 16;
  const u32 ol = last ? OFF_LNF2 : OFF_LNF, oh = last ? OFF_H2 : OFF_H;
  const u32 cl = last ? 0x7f000000u : OFF_LNF2, ch = last ? 0x7f000000u : OFF_H2;
  u4v w2f[14][4];
  u32 xin[8];
  {
    const u32 tv = (t >> 2) * TPB + (t & 3) * 16;
    CIn ci;
    u4v in[4], c1a, c1b, c2[4];
    wait_vm0();
    if (last) __builtin_amdgcn_s_barrier();
    MEMF();
    STAMP(1 + 2 * last, 0);
    const char* tb = ws + ts;
    ALD4S(in[0], tv, tb); ALD4S(in[1], tv, tb + 64 * TPB); ALD4S(in[2], tv, tb + 128 * TPB); ALD4S(in[3], tv, tb + 192 * TPB);
    if (special) {
      const u32 po = t * 128;
      const char* pb = ws + OFF_PQ + kp * 8192;
      ALD4(ci.pq[0], po, pb, 0); ALD4(ci.pq[1], po, pb, 16); ALD4(ci.pq[2], po, pb, 32); ALD4(ci.pq[3], po, pb, 48);
      ALD4(ci.pq[4], po, pb, 64); ALD4(ci.pq[5], po, pb, 80); ALD4(ci.pq[6], po, pb, 96); ALD4(ci.pq[7], po, pb, 112);
    } else {
#pragma unroll
      for (int i = 0; i < 8; ++i) asm volatile("" : "=v"(ci.pq[i]));
    }
    {
      const u32 t16 = t * 16, o1 = (t >> 2) * 32, o2 = (t & 7) * 16;
      const char* wb = ws + OFF_WT + (kp * 64 + qd) * 8192;
      ALD4(ci.w0, t16, wb, 0); ALD4(ci.w1, t16, wb + 4096, 0);
      ALD4(c1a, o1, (const char*)&g_twc1, 0); ALD4(c1b, o1, (const char*)&g_twc1, 16);
      ALD4(c2[0], o2, (const char*)&g_twc2, 0); ALD4(c2[1], o2, (const char*)&g_twc2, 128);
      ALD4(c2[2], o2, (const char*)&g_twc2, 256); ALD4(c2[3], o2, (const char*)&g_twc2, 384);
    }
    SB();
    {
      const rsrc_t rr = last ? rs : rx;
      const u32 ro = (last ? OFF_XR : 0u) + ((256 * (t >> 5) + s2) * 256 + (t & 31)) * 4;
#pragma unroll
      for (int m = 0; m < 8; ++m) xin[m] = bld1<16>(rr, ro + 128 * m);
    }
    SB();
    if (!last) {
      const char* gsrc = ws + OFF_W1F + (size_t)ct * LDS_W1 + l16;
#pragma unroll
      for (int p_ = 0; p_ < 28; ++p_) {
        const int sl_ = p_ / 7, pc_ = p_ % 7;
        const char* gp_ = gsrc + ((4 * w + sl_ + ct) & 15) * 7168 + (pc_ & 4) * 1024;
        char* lp_ = smem + (4 * w + sl_) * 7168 + (pc_ & 4) * 1024;
        if ((pc_ & 3) == 0) __builtin_amdgcn_global_load_lds((const void*)gp_, (LAS void*)lp_, 16, 0, 0);
        if ((pc_ & 3) == 1) __builtin_amdgcn_global_load_lds((const void*)gp_, (LAS void*)lp_, 16, 1024, 0);
        if ((pc_ & 3) == 2) __builtin_amdgcn_global_load_lds((const void*)gp_, (LAS void*)lp_, 16, 2048, 0);
        if ((pc_ & 3) == 3) __builtin_amdgcn_global_load_lds((const void*)gp_, (LAS void*)lp_, 16, 3072, 0);
      }
    }
    asm volatile("s_cmp_eq_u32 %20, 0\n\ts_cbranch_scc1 1f\n\ts_waitcnt vmcnt(8)\n1:\n\ts_waitcnt vmcnt(36)"
                 : "+v"(in[0]), "+v"(in[1]), "+v"(in[2]), "+v"(in[3]), "+v"(ci.w0), "+v"(ci.w1), "+v"(c1a), "+v"(c1b), "+v"(c2[0]), "+v"(c2[1]),
                   "+v"(c2[2]), "+v"(c2[3]), "+v"(ci.pq[0]), "+v"(ci.pq[1]), "+v"(ci.pq[2]), "+v"(ci.pq[3]), "+v"(ci.pq[4]), "+v"(ci.pq[5]),
                   "+v"(ci.pq[6]), "+v"(ci.pq[7]) : "s"(last) : "scc");
    auto stale = [&]() { const u4v a = (in[0] | in[1]) | (in[2] | in[3]); return __builtin_amdgcn_ballot_w64((((a.x | a.y) | (a.z | a.w)) & 1u) != 0) != 0; };
    if (__builtin_expect(stale(), 0)) {
      do {
        asm volatile("global_load_dwordx4 %0, %4, %5 sc1\n\tglobal_load_dwordx4 %1, %4, %6 sc1\n\tglobal_load_dwordx4 %2, %4, %7 sc1\n\t"
                     "global_load_dwordx4 %3, %4, %8 sc1\n\ts_waitcnt vmcnt(0)"
                     : "=&v"(in[0]), "=&v"(in[1]), "=&v"(in[2]), "=&v"(in[3])
                     : "v"(tv), "s"(tb), "s"(tb + 64 * TPB), "s"(tb + 128 * TPB), "s"(tb + 192 * TPB) : "memory");
      } while (stale() && --budget > 0);
    }
    STAMP(1 + 2 * last, 1);
    auto lo = [](u4v v) { return f2{__uint_as_float(v.x), __uint_as_float(v.y)}; };
    auto hi = [](u4v v) { return f2{__uint_as_float(v.z), __uint_as_float(v.w)}; };
    ci.tc1[1] = hi(c1a); ci.tc1[2] = lo(c1b); ci.tc1[3] = hi(c1b);
#pragma unroll
    for (int q = 0; q < 4; ++q) { ci.tc2[2 * q] = lo(c2[q]); ci.tc2[2 * q + 1] = hi(c2[q]); }
    c_body(in, ci, lds, rs, tv, ts, t, kp, special, [&](int) __attribute__((always_inline)) {});
    STAMP(1 + 2 * last, 2);
  }
  u2v bias[7];
  {
    const int s1 = t >> 5, j = t & 31;
    u4v in[4], tq[11];
    SB();
#pragma unroll
    for (int i = 0; i < 11; ++i) tq[i] = ((const u4v*)&g_r2all)[32 * i + j];
#pragma unroll
    for (int jt = 0; jt < 7; ++jt) bias[jt] = *(const u2v*)(b1 + ct * 112 + 16 * jt + 4 * g);
    SB();
    wait_vm0();
    __builtin_amdgcn_s_barrier();
    MEMF();
    STAMP(kid, 0);
    do {
      MEMF();
#pragma unroll
      for (int m = 0; m < 4; ++m) in[m] = bld4s<16>(rs, t * 16, OFF_T + s2 * TPB + m * 4096);
      const u4v a = (in[0] & in[1]) & (in[2] & in[3]);
      if (__builtin_amdgcn_ballot_w64(((a.x & a.y) & (a.z & a.w) & 1u) == 0) == 0) break;
    } while (--budget > 0);
    SB();
    {
      const u4v sv = u4v{SENT, SENT, SENT, SENT};
#pragma unroll
      for (int q = 0; q < 6; ++q) bst4<MAUX>(rs, ch + hst + (q & 1) * 57344 + (q >> 1) * 1024, sv);
      bst4<MAUX>(rs, ch + htl, sv);
      bst4<MAUX>(rs, cl + lno, sv);
      bst4<MAUX>(rs, cl + lno + 2048, sv);
      SB();
    }
    STAMP(kid, 1);
    f2 X[8];
    {
      auto lo = [](u4v v) { return f2{__uint_as_float(v.x), __uint_as_float(v.y)}; };
      auto hi = [](u4v v) { return f2{__uint_as_float(v.z), __uint_as_float(v.w)}; };
      X[0] = lo(in[0]); X[4] = hi(in[0]); X[1] = lo(in[1]); X[7] = hi(in[1]);
      X[2] = lo(in[2]); X[6] = hi(in[2]); X[3] = lo(in[3]); X[5] = hi(in[3]);
    }
#pragma unroll
    for (int k = 1; k < 8; ++k) X[k] = cmulc_s(X[k], f2{tx[2 * k], tx[2 * k + 1]});
    fft8<true>(X);
#pragma unroll
    for (int r = 0; r < 8; ++r) lds[r * RP + t] = X[r];
    f2 tw1[8], twu[8], tw2[2][4];
    {
      f2 tv[22];
#pragma unroll
      for (int i = 0; i < 11; ++i) {
        tv[2 * i] = f2{__uint_as_float(tq[i].x), __uint_as_float(tq[i].y)};
        tv[2 * i + 1] = f2{__uint_as_float(tq[i].z), __uint_as_float(tq[i].w)};
      }
#pragma unroll
      for (int i = 0; i < 8; ++i) twu[i] = tv[i];
#pragma unroll
      for (int a = 0; a < 2; ++a)
#pragma unroll
        for (int i = 1; i < 4; ++i) tw2[a][i] = tv[8 + 3 * a + (i - 1)];
#pragma unroll
      for (int q = 1; q < 8; ++q) tw1[q] = tv[13 + q];
    }
    bar();
    f2* L = lds + s1 * RP;
    f2 z[8];
#pragma unroll
    for (int i = 0; i < 8; ++i) {
      int k = j + 32 * (i >> 2) + 64 * (i & 3);
      f2 A = L[k], Bc = L[(256 - k) & 255];
      f2 E = fma2(Bc, f2{1.f, -1.f}, A);
      f2 Dm = fma2(Bc, f2{-1.f, 1.f}, A);
      f2 zz = cmulc_add(Dm, twu[i], E);
      if (i == 0 && j == 0) zz = f2{A.x + A.y, A.x - A.y};
      z[i] = zz;
    }
    wbar();
    fft4<true>(z[0], z[1], z[2], z[3]);
    fft4<true>(z[4], z[5], z[6], z[7]);
#pragma unroll
    for (int a = 0; a < 2; ++a)
#pragma unroll
      for (int i = 1; i < 4; ++i) z[4 * a + i] = cmulc(z[4 * a + i], tw2[a][i]);
#pragma unroll
    for (int i = 0; i < 8; ++i) L[(i & 3) * 72 + 32 * (i >> 2) + j] = z[i];
    wbar();
#pragma unroll
    for (int q = 0; q < 8; ++q) z[q] = L[(j & 3) * 72 + 8 * q + (j >> 2)];
    fft8<true>(z);
#pragma unroll
    for (int m = 0; m < 8; ++m) L[(j >> 2) * 36 + (j & 3) + 4 * m] = z[m];
    wbar();
#pragma unroll
    for (int q = 0; q < 8; ++q) z[q] = L[q * 36 + j];
#pragma unroll
    for (int q = 1; q < 8; ++q) z[q] = cmulc(z[q], tw1[q]);
    fft8<true>(z);
    u32 x1[8], o[8];
#pragma unroll
    for (int m = 0; m < 8; ++m) { x1[m] = pack2(unpk(xin[m]) + rbf2(z[m])); z[m] = unpk(x1[m]); }
    const int rho = 8 * s2 + s1;
    u32* Lw = (u32*)L;
#pragma unroll
    for (int m = 0; m < 8; ++m) Lw[32 * m + j] = x1[m];
    wbar();
    u4v xa = *(const u4v*)(Lw + 4 * j), xb = *(const u4v*)(Lw + 128 + 4 * j);
    layernorm8(z, o);
#pragma unroll
    for (int m = 0; m < 8; ++m) o[m] = nosent(o[m]);
    char* sg = smem + LDS_W1 + 20480;
    *(u4v*)(sg + j * 288 + s1 * 16) = u4v{o[0], o[1], o[2], o[3]};
    *(u4v*)(sg + j * 288 + 144 + s1 * 16) = u4v{o[4], o[5], o[6], o[7]};
    bar();
    const char* Lr = sg + (16 * w + (lane >> 3)) * 144 + (lane & 7) * 16;
    const u4v fa = *(const u4v*)Lr, fb = *(const u4v*)(Lr + 8 * 144);
    const u32 lo = ol + lno;
    SB();
    bst4<16>(rs, lo, fa);
    bst4<16>(rs, lo + 2048, fb);
    SB();
    bst4<16>(rs, OFF_X1P + rho * 1024 + j * 16, xa);
    bst4<16>(rs, OFF_X1P + rho * 1024 + j * 16 + 512, xb);
  }
  STAMP(kid, 2);
  u4v hf[14][4];
  u32 hoff[4];
#pragma unroll
  for (int h = 0; h < 4; ++h) hoff[h] = oh + (4 * rt2 + ((w + h) & 3)) * 56 * 1024;
#define HFLDX(n, A) do { _Pragma("unroll") for (int h_ = 0; h_ < 4; ++h_) hf[n][h_] = bld4s<A>(rs, l16 + KI(n) * 1024, hoff[h_] + KB(n) * 1024); } while (0)
#define HFLD(n) HFLDX(n, 16)
#define HFLDF(n) HFLDX(n, FAUX)
#define HFMX(n) mx4(mx4(mx4(mx4(0, hf[n][0]), hf[n][1]), hf[n][2]), hf[n][3])
#define CMPS(bad, m) asm volatile("v_cmp_eq_u32 %0, -1, %1" : "=s"(bad) : "v"(m))
  {
    u4v av[2][16];
#define AVLDX(ks, A) do { _Pragma("unroll") for (int h_ = 0; h_ < 2; ++h_) av[h_][ks] = bld4s<A>(rs, l16 + (((ks) + ct) & 15) * 1024, ol + (tt0 + h_) * 16384); } while (0)
#define AVLD(ks) AVLDX(ks, 16)
    SB();
    wait_vm0();
    __builtin_amdgcn_s_barrier();
    MEMF();
    STAMP(kid, 3);
    f4 acc[7][2];
    u32 o0[8];
#define GELU8(p, o) do { f2 a_[8], bs_[8]; \
      _Pragma("unroll") for (int h = 0; h < 2; ++h) { \
        f4 a0 = acc[2 * (p)][h], a1 = acc[2 * (p) + 1][h]; \
        a_[4 * h] = f2{a0[0], a0[1]}; a_[4 * h + 1] = f2{a0[2], a0[3]}; a_[4 * h + 2] = f2{a1[0], a1[1]}; a_[4 * h + 3] = f2{a1[2], a1[3]}; \
        bs_[4 * h] = unpk(bias[2 * (p)].x); bs_[4 * h + 1] = unpk(bias[2 * (p)].y); bs_[4 * h + 2] = unpk(bias[2 * (p) + 1].x); bs_[4 * h + 3] = unpk(bias[2 * (p) + 1].y); } \
      SB(); W2LD(W2E + 2 * (p)); W2LD(W2E + 2 * (p) + 1); \
      if ((p) == HPOS) { _Pragma("unroll") for (int n = 0; n < PD; ++n) HFLDF(n); } \
      SB(); actN<8>(a_, bs_, o); } while (0)
#define HST8(p, o) do { _Pragma("unroll") for (int h = 0; h < 2; ++h) \
        bst4<16>(rs, oh + hst + h * 57344 + (p) * 1024, u4v{o[4 * h], o[4 * h + 1], o[4 * h + 2], o[4 * h + 3]}); } while (0)
    for (int redo = 0;; ++redo) {
      do {
        MEMF();
#pragma unroll
        for (int ks = 0; ks < PA; ++ks) AVLD(ks);
        u32 m = 0;
#pragma unroll
        for (int ks = 0; ks < PA; ++ks) { m = mx4(m, av[0][ks]); m = mx4(m, av[1][ks]); }
        if (!anysent(m)) break;
      } while (--budget > 0);
      STAMP(kid, 4);
      TSTEP(31);
      SB();
#pragma unroll
      for (int jt = 0; jt < 7; ++jt) { acc[jt][0] = f4{0, 0, 0, 0}; acc[jt][1] = f4{0, 0, 0, 0}; }
      const char* lb = smem + l16;
      u4v wf[2][7];
#pragma unroll
      for (int jt = 0; jt < 7; ++jt) wf[0][jt] = *(const u4v*)(lb + jt * 1024);
      SB();
#pragma unroll
      for (int ks = 0; ks < 16; ++ks) {
        const int cu = ks & 1, nx = cu ^ 1;
        const bool chk = ks + 1 >= PA && ks < 15;
        u32 m = 0;
        unsigned long long bad = 0;
#pragma unroll
        for (int jt = 0; jt < 7; ++jt) {
          bf8v wfb = __builtin_bit_cast(bf8v, wf[cu][jt]);
          acc[jt][0] = __builtin_amdgcn_mfma_f32_16x16x32_bf16(wfb, __builtin_bit_cast(bf8v, av[0][ks]), acc[jt][0], 0, 0, 0);
          SB();
          if (ks < 15) wf[nx][jt] = *(const u4v*)(lb + ((ks + 1) * 7 + jt) * 1024);
          SB();
          acc[jt][1] = __builtin_amdgcn_mfma_f32_16x16x32_bf16(wfb, __builtin_bit_cast(bf8v, av[1][ks]), acc[jt][1], 0, 0, 0);
          SB();
          if (ks + PA < 16 && jt < 2) av[jt][ks + PA] = bld4s<FAUX>(rs, l16 + ((ks + PA + ct) & 15) * 1024, ol + (tt0 + jt) * 16384);
          if (ks >= 16 - W2E && jt < 4) W2LD1(ks - (16 - W2E), jt);
          if (chk && jt == 4) asm volatile("v_max_u32 %0, %1, %2" : "=v"(m) : "v"(av[0][ks + 1].x), "v"(av[1][ks + 1].x));
          if (chk && jt == 5) CMPS(bad, m);
          SB();
        }
        TSTEP(ks);
        if (chk) {
          MEMF();
          if (__builtin_expect(bad != 0, 0)) {
            NSLOW();
            do {
              MEMF();
#pragma unroll
              for (int k2 = ks + 1; k2 < 16 && k2 <= ks + PA; ++k2) AVLD(k2);
              wait_vm0();
            } while (anysent(mx4(mx4(0, av[0][ks + 1]), av[1][ks + 1])) && --budget > 0);
          }
          SB();
        }
      }
      STAMP(kid, 5);
      const bool nan = __builtin_amdgcn_ballot_w64(__builtin_isunordered(acc[0][0][0], acc[0][1][0])) != 0;
      GELU8(0, o0);
      MEMF();
      if (__builtin_expect(!nan || redo >= 3, 1)) break;
    }
    HST8(0, o0);
    {
      u32 o[8];
      GELU8(1, o);
      HST8(1, o);
      GELU8(2, o);
      HST8(2, o);
    }
    {
      f4 a0 = acc[6][0], a1 = acc[6][1];
      f2 a[4] = {f2{a0[0], a0[1]}, f2{a0[2], a0[3]}, f2{a1[0], a1[1]}, f2{a1[2], a1[3]}};
      f2 bs[4] = {unpk(bias[6].x), unpk(bias[6].y), unpk(bias[6].x), unpk(bias[6].y)};
      u32 o4[4];
      SB();
      W2LD(13);
      if (HPOS == 3) {
#pragma unroll
        for (int n = 0; n < PD; ++n) HFLDF(n);
      }
      SB();
      actN<4>(a, bs, o4);
      auto rx = __builtin_amdgcn_permlane16_swap(o4[0], o4[2], false, false);
      auto ry = __builtin_amdgcn_permlane16_swap(o4[1], o4[3], false, false);
      bst4<16>(rs, oh + htl, u4v{rx[0], ry[0], rx[1], ry[1]});
    }
  }
  STAMP(kid, 6);
  R1Tw tw;
  {
    f4 acc[4][4];
    u4v bb0 = *(const u4v*)(b2 + 64 * ct2 + 16 * g), bb1 = *(const u4v*)(b2 + 64 * ct2 + 16 * g + 8);
    const u32 pvo = oh + (((4 * rt2 + w) * 56 + 7 * (4 * (rt2 & 1) + w) + 3 * (lane >> 5)) * 64 + (lane & 31)) * 16;
    u32 pv = 0;
    u4v x1a, x1b;
    f4* red = (f4*)(smem + LDS_W1);
    for (int redo = 0;; ++redo) {
      SB();
      MEMF();
      if (__builtin_expect(redo != 0, 0)) {
#pragma unroll
        for (int n = 0; n < 14; ++n) W2LD(n);
#pragma unroll
        for (int n = 0; n < PD; ++n) HFLD(n);
        wait_vm0();
      }
      if (anysent(HFMX(0))) {
        do {
          MEMF();
#pragma unroll
          for (int n = 0; n < PD; ++n) HFLD(n);
          wait_vm0();
        } while (anysent(HFMX(0)) && --budget > 0);
      }
#pragma unroll
      for (int i = 0; i < 4; ++i)
#pragma unroll
        for (int h = 0; h < 4; ++h) acc[i][h] = f4{0, 0, 0, 0};
      STAMP(kid, 8);
      TSTEP(30);
      SB();
#pragma unroll
      for (int n = 0; n < 14; ++n) {
        u32 m = 0;
        unsigned long long bad = 0;
#pragma unroll
        for (int q = 0; q < 8; ++q) {
          const int h = q >> 1, i = 2 * (q & 1);
          acc[i][h] = __builtin_amdgcn_mfma_f32_16x16x32_bf16(__builtin_bit_cast(bf8v, w2f[n][i]), __builtin_bit_cast(bf8v, hf[n][h]), acc[i][h], 0, 0, 0);
          SB();
          if (n + PD < 14 && q < 4) hf[n + PD][q] = bld4s<FAUX>(rs, l16 + KI(n + PD) * 1024, hoff[q] + KB(n + PD) * 1024);
          if (n == 4 && q == 4) pv = bld1<16>(rs, pvo);
          if (n < 13 && q == 5) asm volatile("v_max3_u32 %0, %1, %2, %3" : "=v"(m) : "v"(hf[n + 1][0].x), "v"(hf[n + 1][1].x), "v"(hf[n + 1][2].x));
          if (n < 13 && q == 6) CMPS(bad, m);
          SB();
          acc[i + 1][h] = __builtin_amdgcn_mfma_f32_16x16x32_bf16(__builtin_bit_cast(bf8v, w2f[n][i + 1]), __builtin_bit_cast(bf8v, hf[n][h]), acc[i + 1][h], 0, 0, 0);
          SB();
          if (n < 13 && n != 8 && q == 5) asm volatile("v_max_u32 %0, %0, %1" : "+v"(m) : "v"(hf[n + 1][3].x));
          if (n == 8 && q == 5) asm volatile("v_max3_u32 %0, %0, %1, %2" : "+v"(m) : "v"(hf[n + 1][3].x), "v"(pv));
          SB();
        }
        TSTEP(16 + n);
        if (n < 13) {
          MEMF();
          if (__builtin_expect(bad != 0, 0)) {
            NSLOW();
            do {
              MEMF();
#pragma unroll
              for (int n2 = n + 1; n2 < 14 && n2 <= n + PD; ++n2) HFLD(n2);
              if (n == 8) pv = bld1<16>(rs, pvo);
              wait_vm0();
              m = HFMX(n + 1);
              if (n == 8) m = mx2(m, pv);
            } while (anysent(m) && --budget > 0);
          }
          SB();
        }
        if (n == 8) {
          MEMF();
          x1a = bld4<16>(rs, OFF_X1P + rho2 * 1024 + ct2 * 128 + g * 32);
          x1b = bld4<16>(rs, OFF_X1P + rho2 * 1024 + ct2 * 128 + g * 32 + 16);
          SB();
        }
      }
      STAMP(kid, 9);
      r1_load_tw(tw, t);
      SB();
      const bool nan = __builtin_amdgcn_ballot_w64(__builtin_isunordered(acc[0][0][0], acc[0][1][0]) || __builtin_isunordered(acc[0][2][0], acc[0][3][0])) != 0;
#pragma unroll
      for (int h = 1; h < 4; ++h)
#pragma unroll
        for (int i = 0; i < 4; ++i) red[((((w + h) & 3) * 3 + (h - 1)) * 4 + i) * 64 + lane] = acc[i][h];
      MEMF();
      if (__builtin_expect(!nan || redo >= 3, 1)) break;
    }
    bar();
    f2 s[8];
#pragma unroll
    for (int i = 0; i < 4; ++i) {
      f4 a = (acc[i][0] + red[((w * 3 + 0) * 4 + i) * 64 + lane]) + (red[((w * 3 + 1) * 4 + i) * 64 + lane] + red[((w * 3 + 2) * 4 + i) * 64 + lane]);
      s[2 * i] = f2{a[0], a[1]}; s[2 * i + 1] = f2{a[2], a[3]};
    }
    const u32 x1v[8] = {x1a.x, x1a.y, x1a.z, x1a.w, x1b.x, x1b.y, x1b.z, x1b.w};
    u32 bs[8] = {bb0.x, bb0.y, bb0.z, bb0.w, bb1.x, bb1.y, bb1.z, bb1.w};
    u32 o[8];
#pragma unroll
    for (int i = 0; i < 8; ++i) o[i] = pack2(unpk(x1v[i]) + rbf2(rbf2(s[i]) + unpk(bs[i])));
#pragma unroll
    for (int i = 0; i < 8; ++i) o[i] = nosent(o[i]);
    char* st = smem + LDS_W1 + w * 12288;
    wbar();
    *(u4v*)(st + c * 144 + g * 32) = u4v{o[0], o[1], o[2], o[3]};
    *(u4v*)(st + c * 144 + g * 32 + 16) = u4v{o[4], o[5], o[6], o[7]};
    wbar();
    const u4v ra = *(const u4v*)(st + (lane >> 3) * 144 + (lane & 7) * 16), rb = *(const u4v*)(st + ((lane >> 3) + 8) * 144 + (lane & 7) * 16);
    const u32 xo = ((256 * (lane >> 3) + 8 * rt2 + 2 * w) * D_ + 64 * ct2) * 2 + (lane & 7) * 16;
    if (last) {
      *(u4v*)((char*)out + xo) = ra;
      *(u4v*)((char*)out + xo + 1024) = rb;
      STAMP(kid, 10);
      return;
    }
    bst4<16>(rs, OFF_XR + xo, ra);
    bst4<16>(rs, OFF_XR + xo + 1024, rb);
  }
  STAMP(kid, 10);
  asm volatile("" : "+v"(t));
  {
    u32 xin[8];
    const u32 ro = OFF_XR + ((256 * (t >> 5) + s2) * 256 + (t & 31)) * 4;
    wait_vm0();
    do {
      MEMF();
#pragma unroll
      for (int m = 0; m < 8; ++m) xin[m] = bld1<16>(rs, ro + 128 * m);
    } while (anysent(mx2(mx2(mx2(xin[0], xin[1]), mx2(xin[2], xin[3])), mx2(mx2(xin[4], xin[5]), mx2(xin[6], xin[7])))) && --budget > 0);
    STAMP(kid, 11);
    r1_body<16, 1536, RP>(xin, tw, tx, lds, rs, s2, t);
    STAMP(kid, 12);
  }
  last = 1;
  }
}

extern "C" __device__ __attribute__((used, noinline)) void z_end() {}

extern "C" void kernel_launch(void* const* d_in, const int* in_sizes, int n_in, void* d_out, int out_size, void* d_ws,
                              size_t ws_size, hipStream_t stream) {
  const unsigned short* x = (const unsigned short*)d_in[0];
  const unsigned short* wr = (const unsigned short*)d_in[1];
  const unsigned short* wi = (const unsigned short*)d_in[2];
  const unsigned short* w1 = (const unsigned short*)d_in[4];
  const unsigned short* b1 = (const unsigned short*)d_in[5];
  const unsigned short* w2 = (const unsigned short*)d_in[6];
  const unsigned short* b2 = (const unsigned short*)d_in[7];
  char* ws = (char*)d_ws;
  (void)hipFuncSetAttribute((const void*)k_m, hipFuncAttributeMaxDynamicSharedMemorySize, LDS_M);
  k_first<<<256, 256, 0, stream>>>((const u32*)x, wr, wi, w1, w2, ws);
  k_m<<<256, 256, LDS_M, stream>>>((const u32*)x, b1, b2, ws, (unsigned short*)d_out);
}
```

```cpp
#include <hip/hip_runtime.h>
#include <hip/hip_bf16.h>
#include <stdint.h>
#include <type_traits>

typedef unsigned int u32;
typedef float f2 __attribute__((ext_vector_type(2)));
typedef float f4 __attribute__((ext_vector_type(4)));
typedef float f16v __attribute__((ext_vector_type(16)));
typedef u32 u2v __attribute__((ext_vector_type(2)));
typedef u32 u4v __attribute__((ext_vector_type(4)));
typedef __bf16 bf2v __attribute__((ext_vector_type(2)));
typedef __bf16 bf8v __attribute__((ext_vector_type(8)));
typedef short s4v __attribute__((ext_vector_type(4)));
typedef short s8v __attribute__((ext_vector_type(8)));
#define LAS __attribute__((address_space(3)))
#define DI __device__ __forceinline__
#define RLX_AGENT __ATOMIC_RELAXED, __HIP_MEMORY_SCOPE_AGENT

#define STAMP(K, I)

constexpr int S_ = 2048, D_ = 512, F_ = 1792, DF_ = 257;
constexpr u32 TPB = 16384 + 256;

constexpr u32 OFF_T = 65536;
constexpr u32 SZ_T = 256u * TPB;
constexpr u32 OFF_WT = OFF_T + SZ_T;
constexpr u32 SZ_WT = 256u * 256 * 8 * 4;
constexpr u32 OFF_PQ = OFF_WT + SZ_WT;
constexpr u32 SZ_PQ = 256u * 8 * 16;
constexpr u32 OFF_W2F = OFF_PQ + SZ_PQ;
constexpr u32 SZ_W2F = (u32)F_ * D_ * 2;
constexpr u32 OFF_W1F = OFF_W2F + SZ_W2F;
constexpr u32 OFF_XR = OFF_W1F + SZ_W2F;
constexpr u32 SZ_X = (u32)S_ * D_ * 2;
constexpr u32 OFF_X1P = OFF_XR + SZ_X;
constexpr u32 OFF_LNF = OFF_X1P + SZ_X;
constexpr u32 OFF_H = OFF_LNF + SZ_X;
constexpr u32 SZ_H = (u32)S_ * F_ * 2;
constexpr u32 OFF_LNF2 = OFF_H + SZ_H;
constexpr u32 OFF_H2 = OFF_LNF2 + SZ_X;
constexpr u32 WS_USED = OFF_H2 + SZ_H;

struct cxf { float x, y; };
constexpr double kPi = 3.14159265358979323846264338327950288;
constexpr double c_sin(double x) { double x2 = x * x, t = x, s = x; for (int i = 1; i < 11; ++i) { t *= -x2 / double((2 * i) * (2 * i + 1)); s += t; } return s; }
constexpr double c_cos(double x) { double x2 = x * x, t = 1, s = 1; for (int i = 1; i < 11; ++i) { t *= -x2 / double((2 * i - 1) * (2 * i)); s += t; } return s; }
constexpr cxf tw2048(int i) {
  i &= 2047;
  int qd = i >> 9, r = i & 511;
  double c = 0, s = 0;
  if (r <= 256) { double a = 2 * kPi * r / 2048.0; c = c_cos(a); s = c_sin(a); }
  else { double a = 2 * kPi * (512 - r) / 2048.0; c = c_sin(a); s = c_cos(a); }
  double cc = 0, ss = 0;
  if (qd == 0) { cc = c; ss = s; } else if (qd == 1) { cc = -s; ss = c; } else if (qd == 2) { cc = -c; ss = -s; } else { cc = s; ss = -c; }
  return cxf{(float)cc, (float)(-ss)};
}
template <int R, int C> struct Tab { cxf v[R * C]; };
constexpr Tab<32, 8> mk_r1() { Tab<32, 8> t{}; for (int j = 0; j < 32; ++j) for (int q = 0; q < 8; ++q) t.v[j * 8 + q] = tw2048(8 * j * q); return t; }
constexpr Tab<8, 8> mk_r2() { Tab<8, 8> t{}; for (int j = 0; j < 8; ++j) for (int q = 0; q < 8; ++q) t.v[j * 8 + q] = tw2048(64 * j * q); return t; }
constexpr Tab<256, 1> mk_512() { Tab<256, 1> t{}; for (int c = 0; c < 256; ++c) t.v[c] = tw2048(4 * c + 512); return t; }
constexpr Tab<32, 8> mk_512u() { Tab<32, 8> t{}; for (int u = 0; u < 32; ++u) for (int i = 0; i < 8; ++i) t.v[u * 8 + i] = tw2048(4 * (u + 32 * (i >> 2) + 64 * (i & 3)) + 512); return t; }
constexpr Tab<32, 22> mk_r2all() {
  Tab<32, 22> t{};
  for (int j = 0; j < 32; ++j) {
    cxf v[22] = {};
    for (int i = 0; i < 8; ++i) v[i] = tw2048(4 * (j + 32 * (i >> 2) + 64 * (i & 3)) + 512);
    for (int a = 0; a < 2; ++a) for (int i = 1; i < 4; ++i) v[8 + 3 * a + (i - 1)] = tw2048(64 * ((j >> 3) + 4 * a) * i);
    for (int q = 1; q < 8; ++q) v[13 + q] = tw2048(8 * j * q);
    for (int i = 0; i < 22; ++i) t.v[((i >> 1) * 32 + j) * 2 + (i & 1)] = v[i];
  }
  return t;
}
constexpr Tab<32, 14> mk_r1all() {
  Tab<32, 14> t{};
  for (int j = 0; j < 32; ++j)
    for (int i = 0; i < 14; ++i) t.v[((i >> 1) * 32 + j) * 2 + (i & 1)] = i < 7 ? tw2048(8 * j * (i + 1)) : tw2048(64 * (j & 3) * (i - 6));
  return t;
}
constexpr Tab<256, 8> mk_x() { Tab<256, 8> t{}; for (int s = 0; s < 256; ++s) for (int k = 0; k < 8; ++k) t.v[s * 8 + k] = tw2048(s * k); return t; }
constexpr Tab<64, 4> mk_c1() { Tab<64, 4> t{}; for (int j = 0; j < 64; ++j) for (int q = 0; q < 4; ++q) t.v[j * 4 + q] = tw2048(8 * j * q); return t; }
constexpr double kSc = 6.9053396600248785e-04;
constexpr cxf tw2048s(int i) {
  i &= 2047;
  int qd = i >> 9, r = i & 511;
  double c = 0, s = 0;
  if (r <= 256) { double a = 2 * kPi * r / 2048.0; c = c_cos(a); s = c_sin(a); }
  else { double a = 2 * kPi * (512 - r) / 2048.0; c = c_sin(a); s = c_cos(a); }
  double cc = 0, ss = 0;
  if (qd == 0) { cc = c; ss = s; } else if (qd == 1) { cc = -s; ss = c; } else if (qd == 2) { cc = -c; ss = -s; } else { cc = s; ss = -c; }
  return cxf{(float)(cc * kSc), (float)(-ss * kSc)};
}
constexpr Tab<8, 8> mk_c2() { Tab<8, 8> t{}; for (int j = 0; j < 8; ++j) for (int q = 0; q < 8; ++q) t.v[((q >> 1) * 8 + j) * 2 + (q & 1)] = tw2048s(32 * j * q); return t; }

__device__ const __attribute__((aligned(64))) Tab<32, 14> g_r1all = mk_r1all();
__device__ const __attribute__((aligned(64))) Tab<256, 1> g_tw512 = mk_512();
__device__ const __attribute__((aligned(64))) Tab<32, 22> g_r2all = mk_r2all();
__device__ const __attribute__((aligned(64))) Tab<256, 8> g_twx = mk_x();
__device__ const __attribute__((aligned(64))) Tab<64, 4> g_twc1 = mk_c1();
__device__ const __attribute__((aligned(64))) Tab<8, 8> g_twc2 = mk_c2();

DI float bf_lo(u32 v) { return __uint_as_float(v << 16); }
DI float bf_hi(u32 v) { return __uint_as_float(v & 0xffff0000u); }
DI f2 unpk(u32 v) { return f2{bf_lo(v), bf_hi(v)}; }
DI float rbf(float x) { return (float)(__bf16)x; }
DI u32 pack2(f2 a) { bf2v v = __builtin_convertvector(a, bf2v); return __builtin_bit_cast(u32, v); }
DI f2 rbf2(f2 a) { return unpk(pack2(a)); }
DI f2 swp(f2 a) { return __builtin_shufflevector(a, a, 1, 0); }
DI f2 fma2(f2 a, f2 b, f2 c) { return __builtin_elementwise_fma(a, b, c); }
DI f2 xx(f2 a) { return __builtin_shufflevector(a, a, 0, 0); }
DI f2 yy(f2 a) { return __builtin_shufflevector(a, a, 1, 1); }
DI f2 cmul(f2 a, f2 b) {
  f2 t, r;
  asm("v_pk_mul_f32 %0, %1, %2 op_sel:[1,1] op_sel_hi:[1,0]" : "=v"(t) : "v"(a), "v"(b));
  asm("v_pk_fma_f32 %0, %1, %2, %3 op_sel:[0,0,0] op_sel_hi:[0,1,1] neg_lo:[0,0,1]" : "=v"(r) : "v"(a), "v"(b), "v"(t));
  return r;
}
DI f2 cmulc(f2 a, f2 b) {
  f2 t, r;
  asm("v_pk_mul_f32 %0, %1, %2 op_sel:[1,1] op_sel_hi:[1,0]" : "=v"(t) : "v"(a), "v"(b));
  asm("v_pk_fma_f32 %0, %1, %2, %3 op_sel:[0,0,0] op_sel_hi:[0,1,1] neg_hi:[0,1,0]" : "=v"(r) : "v"(a), "v"(b), "v"(t));
  return r;
}
DI f2 cmul_s(f2 a, f2 b) {
  f2 t, r;
  asm("v_pk_mul_f32 %0, %1, %2 op_sel:[1,1] op_sel_hi:[1,0]" : "=v"(t) : "v"(a), "s"(b));
  asm("v_pk_fma_f32 %0, %1, %2, %3 op_sel:[0,0,0] op_sel_hi:[0,1,1] neg_lo:[0,0,1]" : "=v"(r) : "v"(a), "s"(b), "v"(t));
  return r;
}
DI f2 cmulc_s(f2 a, f2 b) {
  f2 t, r;
  asm("v_pk_mul_f32 %0, %1, %2 op_sel:[1,1] op_sel_hi:[1,0]" : "=v"(t) : "v"(a), "s"(b));
  asm("v_pk_fma_f32 %0, %1, %2, %3 op_sel:[0,0,0] op_sel_hi:[0,1,1] neg_hi:[0,1,0]" : "=v"(r) : "v"(a), "s"(b), "v"(t));
  return r;
}
DI f2 cmul_add(f2 a, f2 b, f2 e) {
  f2 t, r;
  asm("v_pk_fma_f32 %0, %1, %2, %3 op_sel:[1,1,0] op_sel_hi:[1,0,1] neg_lo:[0,1,0]" : "=v"(t) : "v"(a), "v"(b), "v"(e));
  asm("v_pk_fma_f32 %0, %1, %2, %3 op_sel:[0,0,0] op_sel_hi:[0,1,1]" : "=v"(r) : "v"(a), "v"(b), "v"(t));
  return r;
}
DI f2 cmulc_add(f2 a, f2 b, f2 e) {
  f2 t, r;
  asm("v_pk_fma_f32 %0, %1, %2, %3 op_sel:[1,1,0] op_sel_hi:[1,0,1]" : "=v"(t) : "v"(a), "v"(b), "v"(e));
  asm("v_pk_fma_f32 %0, %1, %2, %3 op_sel:[0,0,0] op_sel_hi:[0,1,1] neg_hi:[0,1,0]" : "=v"(r) : "v"(a), "v"(b), "v"(t));
  return r;
}
DI f2 ccmul_add(f2 a, f2 b, f2 e) {
  f2 t, r;
  asm("v_pk_fma_f32 %0, %1, %2, %3 op_sel:[1,1,0] op_sel_hi:[1,0,1] neg_hi:[0,1,0]" : "=v"(t) : "v"(a), "v"(b), "v"(e));
  asm("v_pk_fma_f32 %0, %1, %2, %3 op_sel:[0,0,0] op_sel_hi:[0,1,1]" : "=v"(r) : "v"(a), "v"(b), "v"(t));
  return r;
}
template <bool INV> DI f2 addmi(f2 a, f2 d) { return fma2(swp(d), INV ? f2{-1.f, 1.f} : f2{1.f, -1.f}, a); }
template <bool INV> DI f2 submi(f2 a, f2 d) { return fma2(swp(d), INV ? f2{1.f, -1.f} : f2{-1.f, 1.f}, a); }

template <bool INV> DI void fft4(f2& b0, f2& b1, f2& b2, f2& b3) {
  f2 c0 = b0 + b2, c2 = b0 - b2, c1 = b1 + b3, d = b1 - b3;
  b0 = c0 + c1; b2 = c0 - c1; b1 = addmi<INV>(c2, d); b3 = submi<INV>(c2, d);
}
template <bool INV> DI void fft8(f2* v) {
  const float h = 0.70710678118654752440f;
  f2 a0 = v[0] + v[4], a4 = v[0] - v[4];
  f2 a1 = v[1] + v[5], a5 = v[1] - v[5];
  f2 a2 = v[2] + v[6], a6 = v[2] - v[6];
  f2 a3 = v[3] + v[7], a7 = v[3] - v[7];
  f2 c0 = addmi<INV>(a4, a6), c2 = submi<INV>(a4, a6);
  f2 u = addmi<INV>(a5, a7), w = submi<INV>(a5, a7);
  f2 tu = addmi<INV>(u, u), tw = addmi<INV>(w, w);
  fft4<INV>(a0, a1, a2, a3);
  v[0] = a0; v[2] = a1; v[4] = a2; v[6] = a3;
  v[1] = fma2(tu, f2{h, h}, c0);
  v[5] = fma2(tu, f2{-h, -h}, c0);
  v[3] = fma2(swp(tw), INV ? f2{-h, h} : f2{h, -h}, c2);
  v[7] = fma2(swp(tw), INV ? f2{h, -h} : f2{-h, h}, c2);
}
template <int CTRL> DI float dpp_add(float x) {
  return x + __int_as_float(__builtin_amdgcn_update_dpp(0, __float_as_int(x), CTRL, 0xf, 0xf, false));
}
DI float red32(float x) {
  x = dpp_add<0xB1>(x); x = dpp_add<0x4E>(x); x = dpp_add<0x141>(x); x = dpp_add<0x140>(x);
  auto r = __builtin_amdgcn_permlane16_swap(__float_as_uint(x), __float_as_uint(x), false, false);
  return __uint_as_float(r[0]) + __uint_as_float(r[1]);
}
DI void wbar() { __builtin_amdgcn_wave_barrier(); }
DI void bar() {
  asm volatile("s_waitcnt lgkmcnt(0)" ::: "memory");
  __builtin_amdgcn_s_barrier();
  asm volatile("" ::: "memory");
}
DI void wait_vm0() { __builtin_amdgcn_s_waitcnt(0x0F70); asm volatile("" ::: "memory"); }
DI void layernorm8(const f2* v, u32* o) {
  f2 s2 = ((v[0] + v[1]) + (v[2] + v[3])) + ((v[4] + v[5]) + (v[6] + v[7]));
  float mean = red32(s2.x + s2.y) * (1.0f / 512.0f);
  f2 mm = f2{mean, mean};
  f2 q0 = f2{0, 0}, q1 = f2{0, 0};
#pragma unroll
  for (int i = 0; i < 8; i += 2) { f2 d0 = v[i] - mm, d1 = v[i + 1] - mm; q0 = fma2(d0, d0, q0); q1 = fma2(d1, d1, q1); }
  q0 = q0 + q1;
  float q = red32(q0.x + q0.y);
  float m = rbf(mean), vr = rbf(q * (1.0f / 512.0f));
  float sd = rbf(__builtin_amdgcn_sqrtf(rbf(vr + 1e-5f)));
  float ri = __builtin_amdgcn_rcpf(sd);
  f2 m2 = f2{m, m}, r2 = f2{ri, ri};
#pragma unroll
  for (int i = 0; i < 8; ++i) o[i] = pack2(rbf2(v[i] - m2) * r2);
}
DI f2 ldtw(const cxf* p) { return *(const f2*)p; }
typedef __amdgpu_buffer_rsrc_t rsrc_t;
DI rsrc_t mk_rsrc(void* p, u32 bytes) { return __builtin_amdgcn_make_buffer_rsrc(p, 0, bytes, 0x00020000); }
template <int AUX> DI u4v bld4(rsrc_t r, u32 off) { return __builtin_amdgcn_raw_buffer_load_b128(r, off, 0, AUX); }
template <int AUX> DI u32 bld1(rsrc_t r, u32 off) { return __builtin_amdgcn_raw_buffer_load_b32(r, off, 0, AUX); }
template <int AUX> DI void bst4(rsrc_t r, u32 off, u4v v) { __builtin_amdgcn_raw_buffer_store_b128(v, r, off, 0, AUX); }
DI u4v f4u(f2 a, f2 b) { return u4v{__float_as_uint(a.x), __float_as_uint(a.y), __float_as_uint(b.x), __float_as_uint(b.y)}; }

constexpr int RP = 288;

constexpr u32 SENT = 0xFFFFFFFFu;
DI u32 nosent(u32 v) { return v < SENT - 1 ? v : SENT - 1; }
DI u32 mx2(u32 a, u32 b) { return a > b ? a : b; }
DI u32 mx4(u32 m, u4v a) { return mx2(mx2(mx2(mx2(m, a.x), a.y), a.z), a.w); }
DI bool anysent(u32 m) { return __builtin_amdgcn_ballot_w64(m == SENT) != 0; }
struct R1Tw { f2 tw1[8], tw2[8], w512; };
DI void r1_load_tw(R1Tw& tw, int t) {
  const f4* p = (const f4*)&g_r1all + (t & 31);
  f2 v[14];
#pragma unroll
  for (int i = 0; i < 7; ++i) { f4 q = p[32 * i]; v[2 * i] = f2{q[0], q[1]}; v[2 * i + 1] = f2{q[2], q[3]}; }
#pragma unroll
  for (int q = 1; q < 8; ++q) { tw.tw1[q] = v[q - 1]; tw.tw2[q] = v[6 + q]; }
  tw.w512 = ldtw(&g_tw512.v[t]);
}
DI u4v tag0(u4v v) { return v & u4v{~1u, ~1u, ~1u, ~1u}; }
DI u4v tag1(u4v v) { return v | u4v{1u, 1u, 1u, 1u}; }
template <int AUX, int RW, int R0> DI void r1_body(const u32* xin, const R1Tw& tw, const f16v& tx, f2* lds, rsrc_t rs, int s2, int t) {
  const int s1 = t >> 5, j = t & 31;
  f2 z[8];
#pragma unroll
  for (int m = 0; m < 8; ++m) z[m] = unpk(xin[m]);
  u32 o[8];
  layernorm8(z, o);
#pragma unroll
  for (int m = 0; m < 8; ++m) z[m] = unpk(o[m]);
  f2* L = lds + R0 + (s1 >> 1) * RW + (s1 & 1) * RP;
  fft8<false>(z);
#pragma unroll
  for (int q = 1; q < 8; ++q) z[q] = cmul(z[q], tw.tw1[q]);
#pragma unroll
  for (int q = 0; q < 8; ++q) L[q * 36 + j] = z[q];
  wbar();
#pragma unroll
  for (int m = 0; m < 8; ++m) z[m] = L[(j >> 2) * 36 + (j & 3) + 4 * m];
  wbar();
  fft8<false>(z);
#pragma unroll
  for (int q = 1; q < 8; ++q) z[q] = cmul(z[q], tw.tw2[q]);
#pragma unroll
  for (int q = 0; q < 8; ++q) L[(j & 3) * 68 + 8 * q + (j >> 2)] = z[q];
  wbar();
#pragma unroll
  for (int i = 0; i < 8; ++i) z[i] = L[(i & 3) * 68 + 32 * (i >> 2) + j];
  wbar();
  fft4<false>(z[0], z[1], z[2], z[3]);
  fft4<false>(z[4], z[5], z[6], z[7]);
#pragma unroll
  for (int i = 0; i < 8; ++i) L[j + 32 * (i >> 2) + 64 * (i & 3)] = z[i];
  bar();
  f2 X[8];
#pragma unroll
  for (int r = 0; r < 8; ++r) {
    const f2* Lr = lds + R0 + (r >> 1) * RW + (r & 1) * RP;
    f2 A = Lr[t], Bc = Lr[(256 - t) & 255];
    f2 E = fma2(Bc, f2{1.f, -1.f}, A);
    f2 Dm = fma2(Bc, f2{-1.f, 1.f}, A);
    f2 x2 = cmul_add(Dm, tw.w512, E);
    if (t == 0) x2.y = E.x - Dm.y;
    X[r] = x2;
  }
  fft8<false>(X);
#pragma unroll
  for (int k = 1; k < 8; ++k) X[k] = cmul_s(X[k], f2{tx[2 * k], tx[2 * k + 1]});
  const u32 dst = OFF_T + s2 * TPB + t * 16;
  bst4<AUX>(rs, dst, tag0(f4u(X[0], X[4])));
  bst4<AUX>(rs, dst + 4096, tag0(f4u(X[1], X[7])));
  bst4<AUX>(rs, dst + 8192, tag0(f4u(X[2], X[6])));
  bst4<AUX>(rs, dst + 12288, tag0(f4u(X[3], X[5])));
}

DI int f_of(int kap, int g, int j) {
  int u = kap / 7, r = kap % 7;
  if (r < 6) { int ct = 2 * u + (r >= 3), p = r % 3; return 112 * ct + 32 * p + (j < 4 ? 4 * g + j : 16 + 4 * g + (j - 4)); }
  return 112 * (2 * u + (g >> 1)) + 96 + 8 * (g & 1) + j;
}
constexpr int WPIT = 528;
#define WAUX 0
#define TAUX 0
__global__ __launch_bounds__(256) void k_first(const u32* __restrict__ x, const unsigned short* __restrict__ wr,
                                               const unsigned short* __restrict__ wi, const unsigned short* __restrict__ w1,
                                               const unsigned short* __restrict__ w2, char* __restrict__ ws) {
  __shared__ __attribute__((aligned(16))) char smem[8 * RP * 8 + 32 * WPIT];
  const int b = blockIdx.x & 255, t = threadIdx.x, w = __builtin_amdgcn_readfirstlane(t >> 6), lane = t & 63;
  STAMP(0, 0);
  asm volatile("" :: "s"(x), "s"(wr), "s"(wi), "s"(w1), "s"(w2), "s"(ws));
  const rsrc_t rs = mk_rsrc(ws, WS_USED);
  if (blockIdx.x < 256) {
  const int s2 = 32 * (b & 7) + (b >> 3);
  u32 xin[8];
  const u32* xr = x + (size_t)(256 * (t >> 5) + s2) * 256 + (t & 31);
#pragma unroll
  for (int m = 0; m < 8; ++m) xin[m] = xr[32 * m];
  R1Tw tw;
  r1_load_tw(tw, t);
  const f16v tx = *(const f16v*)&g_twx.v[s2 * 8];
  __builtin_amdgcn_sched_barrier(0);
  u32 pf0 = 0;
  if (w == 3 && b < 64) {
    u32 alo, ahi, elo, ehi;
    asm volatile("s_getpc_b64 s[40:41]\n\ts_mov_b32 %0, s40\n\ts_mov_b32 %1, s41" : "=s"(alo), "=s"(ahi) :: "s40", "s41");
    asm volatile("s_getpc_b64 s[40:41]\n\ts_add_u32 %0, s40, k_m@rel32@lo+4\n\ts_addc_u32 %1, s41, k_m@rel32@hi+12" : "=s"(elo), "=s"(ehi) :: "s40", "s41", "scc");
    const unsigned long long a = ((unsigned long long)ahi << 32) | alo, e = ((unsigned long long)ehi << 32) | elo;
    const u32 lim = e > a && e - a < 65536 ? (u32)(e - a) - 4 : 0u;
    const u32 o = lane * 128, oc = (o < lim ? o : lim) & ~3u;
    asm volatile("global_load_dword %0, %1, %2" : "=v"(pf0) : "v"(oc), "s"(a));
  }
  __builtin_amdgcn_sched_barrier(0);
  r1_body<0, 2 * RP, 0>(xin, tw, tx, (f2*)smem, rs, s2, t);
  asm volatile("s_waitcnt vmcnt(0)" :: "v"(pf0));
  STAMP(0, 2);
  return;
  }
  u32 pf[7];
#pragma unroll
  for (int i = 0; i < 7; ++i) pf[i] = 0;
  if (w == 3 && b >= 224) {
    u32 alo, ahi, elo, ehi;
    asm volatile("s_getpc_b64 s[40:41]\n\ts_add_u32 %0, s40, k_m@rel32@lo+4\n\ts_addc_u32 %1, s41, k_m@rel32@hi+12" : "=s"(alo), "=s"(ahi) :: "s40", "s41", "scc");
    asm volatile("s_getpc_b64 s[40:41]\n\ts_add_u32 %0, s40, z_end@rel32@lo+4\n\ts_addc_u32 %1, s41, z_end@rel32@hi+12" : "=s"(elo), "=s"(ehi) :: "s40", "s41", "scc");
    const unsigned long long a = ((unsigned long long)ahi << 32) | alo, e = ((unsigned long long)ehi << 32) | elo;
    const u32 lim = e > a && e - a < 65536 ? (u32)(e - a) - 4 : 0u;
#pragma unroll
    for (int i = 0; i < 6; ++i) {
      const u32 o = (i * 64 + lane) * 128, oc = (o < lim ? o : lim) & ~3u;
      asm volatile("global_load_dword %0, %1, %2" : "=v"(pf[i]) : "v"(oc), "s"(a));
    }
    const char* tb = lane < 44 ? (const char*)&g_r2all + lane * 128 : lane < 60 ? (const char*)&g_twc1 + (lane - 44) * 128 : (const char*)&g_twc2 + (lane - 60) * 128;
    asm volatile("global_load_dword %0, %1, off" : "=v"(pf[6]) : "v"(tb));
  }
  __builtin_amdgcn_sched_barrier(0);
  unsigned short fr[8], fi[8];
#pragma unroll
  for (int i = 0; i < 8; ++i) { fr[i] = wr[(b + 256 * i) * DF_ + t]; fi[i] = wi[(b + 256 * i) * DF_ + t]; }
  u4v wu[4];
  const int isw2 = b >= 112;
  const int ua = isw2 ? (b - 112) >> 1 : b / 7, ub = isw2 ? (b - 112) & 1 : b % 7;
  if (b < 224) {
#pragma unroll
    for (int g = 0; g < 4; ++g) {
      const int i = t >> 5;
      const unsigned short* p;
      if (isw2) p = w2 + (size_t)f_of(ua, g, i) * D_;
      else p = w1 + (size_t)(256 * (g & 1) + 64 * (i >> 1) + 4 * ua + 2 * (g >> 1) + (i & 1)) * F_;
      wu[g] = *(const u4v*)(p + 256 * ub + (t & 31) * 8);
    }
  } else {
#pragma unroll
    for (int g = 0; g < 4; ++g) asm volatile("" : "=v"(wu[g]));
  }
  unsigned short pv[8] = {};
  if (w == 0) {
    const int k = b + 256 * (t & 7), km = (2048 - k) & 2047;
    pv[0] = wr[k * DF_]; pv[1] = wi[k * DF_]; pv[2] = wr[km * DF_]; pv[3] = wi[km * DF_];
    pv[4] = wr[k * DF_ + 256]; pv[5] = wi[k * DF_ + 256]; pv[6] = wr[km * DF_ + 256]; pv[7] = wi[km * DF_ + 256];
  }
  __builtin_amdgcn_sched_barrier(0);
  if (b < 224) {
    char* im = smem + 8 * RP * 8;
#pragma unroll
    for (int g = 0; g < 4; ++g) *(u4v*)(im + (8 * g + (t >> 5)) * WPIT + (t & 31) * 16) = wu[g];
    bar();
    const int g = lane >> 4, q = (lane >> 2) & 3, p = lane & 3;
    const char* a0 = im + (8 * g + q) * WPIT + (isw2 ? 32 * p : 8 * p) + 128 * w;
#pragma unroll
    for (int n = 0; n < 4; ++n) {
      const char* a = a0 + (isw2 ? 8 * n : 32 * n);
      s4v lo = __builtin_amdgcn_ds_read_tr16_b64_v4i16((LAS s4v*)a);
      s4v hi = __builtin_amdgcn_ds_read_tr16_b64_v4i16((LAS s4v*)(a + 4 * WPIT));
      s8v wf = __builtin_shufflevector(lo, hi, 0, 1, 2, 3, 4, 5, 6, 7);
      u32 off;
      if (isw2) off = OFF_W2F + (((4 * ub + w) * 56 + ua) * 4 + n) * 1024;
      else { int ft = 16 * ub + 4 * w + n; off = OFF_W1F + (((ft / 7) * 16 + ua) * 7 + ft % 7) * 1024; }
      bst4<WAUX>(rs, off + lane * 16, __builtin_bit_cast(u4v, wf));
    }
  }
  {
    const int k1 = b & 7, kp = k1 <= 4 ? (k1 & 3) : 8 - k1, e = k1 >= 4;
    const int tl = e * 32 + ((b >> 3) & 3) * 8 + (b >> 5);
    u32 o[8];
#pragma unroll
    for (int i = 0; i < 8; ++i) o[i] = (u32)fr[i] | ((u32)fi[i] << 16);
    const u32 dst = OFF_WT + ((kp * 64 + (t >> 2)) * 512 + (t & 3) * 64 + tl) * 16;
    bst4<TAUX>(rs, dst, u4v{o[0], o[1], o[2], o[3]});
    bst4<TAUX>(rs, dst + 4096, u4v{o[4], o[5], o[6], o[7]});
    auto cv = [](unsigned short v) { return __uint_as_float((u32)v << 16); };
    float h0r = 0.5f * (cv(pv[0]) + cv(pv[2])), h0i = 0.5f * (cv(pv[1]) - cv(pv[3]));
    float h1r = 0.5f * (cv(pv[4]) + cv(pv[6])), h1i = 0.5f * (cv(pv[5]) - cv(pv[7]));
    if (t < 8) ((f4*)(ws + OFF_PQ))[(kp * 64 + tl) * 8 + t] = f4{0.5f * (h0r + h1r), 0.5f * (h0i + h1i), 0.5f * (h0r - h1r), 0.5f * (h0i - h1i)};
  }
  asm volatile("s_waitcnt vmcnt(0)" :: "v"(pf[0]), "v"(pf[1]), "v"(pf[2]), "v"(pf[3]), "v"(pf[4]), "v"(pf[5]), "v"(pf[6]));
}

constexpr int PK = 292, QS = 72;
DI int idx1(int k1, int q, int j) { return k1 * PK + q * QS + (j ^ ((k1 >> 2) << 2)); }
template <int AUX> DI u4v bld4s(rsrc_t r, u32 off, u32 soff) { return __builtin_amdgcn_raw_buffer_load_b128(r, off, soff, AUX); }
template <int AUX> DI void bst4s(rsrc_t r, u32 off, u32 soff, u4v v) { __builtin_amdgcn_raw_buffer_store_b128(v, r, off + soff, 0, AUX); }
struct CIn { u4v w0, w1; f2 tc1[4], tc2[8]; u4v pq[8]; };
template <class HK> DI void c_body(const u4v* in, const CIn& ci, f2* lds, rsrc_t rs, u32 tv, u32 ts, int t, int kp, bool special, HK hook) {
  const int cc = t & 3, j = t >> 2;
  const f2* tc1 = ci.tc1; const f2* tc2 = ci.tc2; const u4v* pq = ci.pq; const u4v w0 = ci.w0, w1 = ci.w1;
#pragma unroll
  for (int e = 0; e < 2; ++e) {
    f2 a[4];
#pragma unroll
    for (int m = 0; m < 4; ++m) a[m] = e ? f2{__uint_as_float(in[m].z), __uint_as_float(in[m].w)} : f2{__uint_as_float(in[m].x), __uint_as_float(in[m].y)};
    fft4<false>(a[0], a[1], a[2], a[3]);
#pragma unroll
    for (int q = 1; q < 4; ++q) a[q] = cmul(a[q], tc1[q]);
#pragma unroll
    for (int q = 0; q < 4; ++q) lds[idx1(2 * cc + e, q, j)] = a[q];
  }
  hook(1);
  bar();
  const int k1 = t >> 5, q = (t >> 3) & 3, jp = t & 7;
  const float sc = (float)kSc;
  f2 v[8];
#pragma unroll
  for (int m = 0; m < 8; ++m) v[m] = lds[idx1(k1, q, jp + 8 * m)];
  wbar();
  hook(2);
  fft8<false>(v);
  v[0] = v[0] * f2{sc, sc};
#pragma unroll
  for (int i = 1; i < 8; ++i) v[i] = cmul(v[i], tc2[i]);
  f2* L2 = lds + k1 * PK + q * QS;
#pragma unroll
  for (int i = 0; i < 8; ++i) L2[jp * 9 + i] = v[i];
  wbar();
#pragma unroll
  for (int i = 0; i < 8; ++i) v[i] = L2[i * 9 + jp];
  wbar();
  hook(3);
  fft8<false>(v);
  if (!special) {
    u32 w[8] = {w0.x, w0.y, w0.z, w0.w, w1.x, w1.y, w1.z, w1.w};
#pragma unroll
    for (int i = 0; i < 8; ++i) v[i] = cmul(v[i], unpk(w[i]));
  } else {
#pragma unroll
    for (int i = 0; i < 8; ++i) lds[k1 * PK + i * 32 + (t & 31)] = v[i];
    wbar();
    const bool z0 = kp == 0 && k1 == 0;
    const int u = q + 4 * jp, up = z0 ? (32 - u) & 31 : 31 - u;
    const int zi = (z0 && u == 0) ? 8 : 7;
    const f2* src = lds + (kp == 0 ? k1 : k1 ^ 1) * PK + (up & 3) * 8 + (up >> 2);
#pragma unroll
    for (int i = 0; i < 8; ++i) {
      f2 zm = src[((zi - i) & 7) * 32];
      f2 P = f2{__uint_as_float(pq[i].x), __uint_as_float(pq[i].y)}, Q = f2{__uint_as_float(pq[i].z), __uint_as_float(pq[i].w)};
      v[i] = ccmul_add(zm, Q, cmul(v[i], P));
    }
    wbar();
  }
  hook(4);
  fft8<true>(v);
  v[0] = v[0] * f2{sc, sc};
#pragma unroll
  for (int i = 1; i < 8; ++i) v[i] = cmulc(v[i], tc2[i]);
#pragma unroll
  for (int i = 0; i < 8; ++i) L2[i * 9 + jp] = v[i];
  wbar();
#pragma unroll
  for (int i = 0; i < 8; ++i) v[i] = L2[jp * 9 + i];
  wbar();
  hook(5);
  fft8<true>(v);
#pragma unroll
  for (int m = 0; m < 8; ++m) lds[idx1(k1, q, jp + 8 * m)] = v[m];
  hook(6);
  bar();
  f2 o[2][4];
#pragma unroll
  for (int e = 0; e < 2; ++e) {
#pragma unroll
    for (int qq = 0; qq < 4; ++qq) o[e][qq] = lds[idx1(2 * cc + e, qq, j)];
#pragma unroll
    for (int qq = 1; qq < 4; ++qq) o[e][qq] = cmulc(o[e][qq], tc1[qq]);
    fft4<true>(o[e][0], o[e][1], o[e][2], o[e][3]);
  }
#pragma unroll
  for (int m = 0; m < 4; ++m) bst4s<16>(rs, tv, ts + m * 64 * TPB, tag1(f4u(o[0][m], o[1][m])));
}

#define SB() __builtin_amdgcn_sched_barrier(0)
template <int N> DI void actN(const f2* a, const f2* bias, u32* o) {
  const float c0 = -2.0f * 0.7978845608028654f * 1.4426950408889634f, c1 = c0 * 0.044715f;
  f2 x[N], u[N];
#pragma unroll
  for (int i = 0; i < N; ++i) x[i] = rbf2(a[i]);
  SB();
#pragma unroll
  for (int i = 0; i < N; ++i) x[i] = x[i] + bias[i];
  SB();
#pragma unroll
  for (int i = 0; i < N; ++i) x[i] = rbf2(x[i]);
  SB();
#pragma unroll
  for (int i = 0; i < N; ++i) u[i] = x[i] * x[i];
  SB();
#pragma unroll
  for (int i = 0; i < N; ++i) u[i] = fma2(u[i], f2{c1, c1}, f2{c0, c0});
  SB();
#pragma unroll
  for (int i = 0; i < N; ++i) u[i] = u[i] * x[i];
  SB();
#pragma unroll
  for (int i = 0; i < N; ++i) u[i] = f2{__builtin_amdgcn_exp2f(u[i].x), __builtin_amdgcn_exp2f(u[i].y)};
  SB();
#pragma unroll
  for (int i = 0; i < N; ++i) u[i] = u[i] + f2{1.f, 1.f};
  SB();
#pragma unroll
  for (int i = 0; i < N; ++i) u[i] = f2{__builtin_amdgcn_rcpf(u[i].x), __builtin_amdgcn_rcpf(u[i].y)};
  SB();
#pragma unroll
  for (int i = 0; i < N; ++i) u[i] = u[i] * x[i];
  SB();
#pragma unroll
  for (int i = 0; i < N; ++i) o[i] = pack2(u[i]);
  SB();
#pragma unroll
  for (int i = 0; i < N; ++i) o[i] = nosent(o[i]);
  SB();
}

#define ALD4(dst, voff, sbase, imm) asm volatile("global_load_dwordx4 %0, %1, %2 offset:" #imm : "=v"(dst) : "v"(voff), "s"(sbase))
#define ALD4S(dst, voff, sbase) asm volatile("global_load_dwordx4 %0, %1, %2 sc1" : "=v"(dst) : "v"(voff), "s"(sbase) : "memory")
#define ALD1(dst, voff, sbase, imm) asm volatile("global_load_dword %0, %1, %2 offset:" #imm : "=v"(dst) : "v"(voff), "s"(sbase))
#define TSTEP(i)
#define NSLOW()
#define MEMF() asm volatile("" ::: "memory")
#define MX4F(m, a) do { asm volatile("v_max_u32 %0, %1, %2" : "=v"(m) : "v"((a).x), "v"((a).y)); \
    asm volatile("v_max3_u32 %0, %0, %1, %2" : "+v"(m) : "v"((a).z), "v"((a).w)); } while (0)
#define MX4(m, a) do { asm volatile("v_max3_u32 %0, %0, %1, %2" : "+v"(m) : "v"((a).x), "v"((a).y)); \
    asm volatile("v_max3_u32 %0, %0, %1, %2" : "+v"(m) : "v"((a).z), "v"((a).w)); } while (0)
constexpr int LDS_W1 = 114688;
constexpr int LDS_M = LDS_W1 + 49152;
constexpr int W2E = 7;
constexpr int PA = 6;
#define HPOS 3
#define FAUX 0
#define PD 4

extern "C" __global__ __launch_bounds__(256, 1) void k_m(const u32* __restrict__ xres, const unsigned short* __restrict__ b1,
                                              const unsigned short* __restrict__ b2, char* ws, unsigned short* out) {
  extern __shared__ __attribute__((aligned(16))) char smem[];
  const rsrc_t rs = mk_rsrc(ws, WS_USED);
  f2* lds = (f2*)(smem + LDS_W1);
  int budget = 1 << 16;
#define KB(n) ((n) < 12 ? kq[(n) & 3] : ((n) == 12 ? kt0 : kt1))
#define KI(n) ((n) < 12 ? (n) >> 2 : 0)
#define W2LD1(n, i) w2f[n][i] = bld4s<0>(rs, l16 + (i) * 1024, w2s + (KB(n) + KI(n)) * 4096)
#define W2LD(n) do { _Pragma("unroll") for (int i_ = 0; i_ < 4; ++i_) W2LD1(n, i_); } while (0)
  int last = 0;
#pragma nounroll
  for (;;) {
  asm volatile("" : "+s"(last));
  int t = threadIdx.x;
  asm volatile("" : "+v"(t));
  int b = blockIdx.x, w = __builtin_amdgcn_readfirstlane(threadIdx.x >> 6);
  asm volatile("" : "+s"(b), "+s"(w));
  const int xcd = b & 7, ii = b >> 3;
  const int s2 = 32 * xcd + ii, rt = 2 * xcd + (ii >> 4), ct = ii & 15, rt2 = 4 * xcd + (ii >> 3), ct2 = ii & 7;
  const int tt0 = 8 * rt + 2 * w, kb = 7 * (ct >> 1) + 3 * (ct & 1);
  const int rot = ct2 & 3;
  int kq[4];
#pragma unroll
  for (int q = 0; q < 4; ++q) { const int pr = (q + rot) & 3; kq[q] = 14 * w + 7 * (pr >> 1) + 3 * (pr & 1); }
  const int kt0 = 14 * w + 6 + 7 * (ct2 & 1), kt1 = 14 * w + 13 - 7 * (ct2 & 1);
  const int qd = 8 * (b & 7) + ((b >> 3) & 7), kp = b >> 6;
  const bool special = qd == 0 && w == 0;
  const u32 ts = OFF_T + kp * 4096 + qd * 64;
  const u32 w2s = OFF_W2F + ct2 * 56 * 4096;
  const f16v tx = *(const f16v*)&g_twx.v[s2 * 8];
  const int lane = t & 63, g = lane >> 4, c = lane & 15;
  const u32 l16 = lane * 16;
  const int rho2 = 64 * rt2 + 16 * w + c;
  const u32 lno = ((s2 >> 1) * 16 + 4 * w) * 1024 + (lane >> 3) * 256 + (s2 & 1) * 128 + (lane & 7) * 16;
  const u32 hst = (tt0 * 56 + kb) * 1024 + l16;
  const u32 htl = (((tt0 + (g & 1)) * 56 + 7 * (ct >> 1) + 6) * 64 + (2 * (ct & 1) + (g >> 1)) * 16 + c) * 16;
  const u32 ol = last ? OFF_LNF2 : OFF_LNF, oh = last ? OFF_H2 : OFF_H;
  const u32 drop = last ? 0x7f000000u : 0u;
  u4v w2f[14][4];
  u32 xin[8];
  const char* gsrc = ws + OFF_W1F + (size_t)ct * LDS_W1 + l16;
#define W1DMA(pa, pb) do { SB(); if (!last) { _Pragma("unroll") for (int p_ = (pa); p_ < (pb); ++p_) { \
      const int sl_ = p_ / 7, pc_ = p_ % 7; \
      const char* gp_ = gsrc + ((4 * w + sl_ + ct) & 15) * 7168 + (pc_ & 4) * 1024; \
      char* lp_ = smem + (4 * w + sl_) * 7168 + (pc_ & 4) * 1024; \
      if ((pc_ & 3) == 0) __builtin_amdgcn_global_load_lds((const void*)gp_, (LAS void*)lp_, 16, 0, 0); \
      if ((pc_ & 3) == 1) __builtin_amdgcn_global_load_lds((const void*)gp_, (LAS void*)lp_, 16, 1024, 0); \
      if ((pc_ & 3) == 2) __builtin_amdgcn_global_load_lds((const void*)gp_, (LAS void*)lp_, 16, 2048, 0); \
      if ((pc_ & 3) == 3) __builtin_amdgcn_global_load_lds((const void*)gp_, (LAS void*)lp_, 16, 3072, 0); } } SB(); } while (0)
  {
    const u32 tv = (t >> 2) * TPB + (t & 3) * 16;
    CIn ci;
    u4v in[4], c1a, c1b, c2[4];
    wait_vm0();
    if (last) __builtin_amdgcn_s_barrier();
    MEMF();
    STAMP(1 + 2 * last, 0);
    const char* tb = ws + ts;
    ALD4S(in[0], tv, tb); ALD4S(in[1], tv, tb + 64 * TPB); ALD4S(in[2], tv, tb + 128 * TPB); ALD4S(in[3], tv, tb + 192 * TPB);
    if (special) {
      const u32 po = t * 128;
      const char* pb = ws + OFF_PQ + kp * 8192;
      ALD4(ci.pq[0], po, pb, 0); ALD4(ci.pq[1], po, pb, 16); ALD4(ci.pq[2], po, pb, 32); ALD4(ci.pq[3], po, pb, 48);
      ALD4(ci.pq[4], po, pb, 64); ALD4(ci.pq[5], po, pb, 80); ALD4(ci.pq[6], po, pb, 96); ALD4(ci.pq[7], po, pb, 112);
    } else {
#pragma unroll
      for (int i = 0; i < 8; ++i) asm volatile("" : "=v"(ci.pq[i]));
    }
    {
      const u32 t16 = t * 16, o1 = (t >> 2) * 32, o2 = (t & 7) * 16;
      const char* wb = ws + OFF_WT + (kp * 64 + qd) * 8192;
      ALD4(ci.w0, t16, wb, 0); ALD4(ci.w1, t16, wb + 4096, 0);
      ALD4(c1a, o1, (const char*)&g_twc1, 0); ALD4(c1b, o1, (const char*)&g_twc1, 16);
      ALD4(c2[0], o2, (const char*)&g_twc2, 0); ALD4(c2[1], o2, (const char*)&g_twc2, 128);
      ALD4(c2[2], o2, (const char*)&g_twc2, 256); ALD4(c2[3], o2, (const char*)&g_twc2, 384);
    }
    SB();
    {
      const char* xb = last ? ws + OFF_XR : (const char*)xres;
      const u32 ro = ((256 * (t >> 5) + s2) * 256 + (t & 31)) * 4;
#define ALD1S(m) asm volatile("global_load_dword %0, %1, %2 offset:" #m "*128 sc1" : "=v"(xin[m]) : "v"(ro), "s"(xb))
      ALD1S(0); ALD1S(1); ALD1S(2); ALD1S(3); ALD1S(4); ALD1S(5); ALD1S(6); ALD1S(7);
    }
    SB();
    W1DMA(0, 4);
    asm volatile("s_cmp_eq_u32 %20, 0\n\ts_cbranch_scc1 1f\n\ts_waitcnt vmcnt(8)\n1:\n\ts_waitcnt vmcnt(12)"
                 : "+v"(in[0]), "+v"(in[1]), "+v"(in[2]), "+v"(in[3]), "+v"(ci.w0), "+v"(ci.w1), "+v"(c1a), "+v"(c1b), "+v"(c2[0]), "+v"(c2[1]),
                   "+v"(c2[2]), "+v"(c2[3]), "+v"(ci.pq[0]), "+v"(ci.pq[1]), "+v"(ci.pq[2]), "+v"(ci.pq[3]), "+v"(ci.pq[4]), "+v"(ci.pq[5]),
                   "+v"(ci.pq[6]), "+v"(ci.pq[7]) : "s"(last) : "scc");
    auto stale = [&]() { const u4v a = (in[0] | in[1]) | (in[2] | in[3]); return __builtin_amdgcn_ballot_w64((((a.x | a.y) | (a.z | a.w)) & 1u) != 0) != 0; };
    if (__builtin_expect(stale(), 0)) {
      do {
        asm volatile("global_load_dwordx4 %0, %4, %5 sc1\n\tglobal_load_dwordx4 %1, %4, %6 sc1\n\tglobal_load_dwordx4 %2, %4, %7 sc1\n\t"
                     "global_load_dwordx4 %3, %4, %8 sc1\n\ts_waitcnt vmcnt(0)"
                     : "=&v"(in[0]), "=&v"(in[1]), "=&v"(in[2]), "=&v"(in[3])
                     : "v"(tv), "s"(tb), "s"(tb + 64 * TPB), "s"(tb + 128 * TPB), "s"(tb + 192 * TPB) : "memory");
      } while (stale() && --budget > 0);
    }
    SB();
    bst4<16>(rs, ol + lno, u4v{SENT, SENT, SENT, SENT});
    bst4<16>(rs, ol + lno + 2048, u4v{SENT, SENT, SENT, SENT});
    SB();
    STAMP(1 + 2 * last, 1);
    auto lo = [](u4v v) { return f2{__uint_as_float(v.x), __uint_as_float(v.y)}; };
    auto hi = [](u4v v) { return f2{__uint_as_float(v.z), __uint_as_float(v.w)}; };
    ci.tc1[1] = hi(c1a); ci.tc1[2] = lo(c1b); ci.tc1[3] = hi(c1b);
#pragma unroll
    for (int q = 0; q < 4; ++q) { ci.tc2[2 * q] = lo(c2[q]); ci.tc2[2 * q + 1] = hi(c2[q]); }
    c_body(in, ci, lds, rs, tv, ts, t, kp, special, [&](int h) __attribute__((always_inline)) {
      if (h == 6) {
        SB();
        if (last) __builtin_amdgcn_s_waitcnt(0x0F70); else __builtin_amdgcn_s_waitcnt(0x0F7A);
        MEMF();
      }
      W1DMA(2 + 2 * h, 4 + 2 * h); {
      } });
    STAMP(1 + 2 * last, 2);
  }
  u2v bias[7];
  {
    const int s1 = t >> 5, j = t & 31;
    u4v in[4], tq[11];
    SB();
    {
      const u32 jo = j * 16, bo = g * 8;
      const char* tp = (const char*)&g_r2all;
      ALD4(tq[0], jo, tp, 0); ALD4(tq[1], jo, tp, 512); ALD4(tq[2], jo, tp, 1024); ALD4(tq[3], jo, tp, 1536);
      ALD4(tq[4], jo, tp, 2048); ALD4(tq[5], jo, tp, 2560); ALD4(tq[6], jo, tp, 3072); ALD4(tq[7], jo, tp, 3584);
      ALD4(tq[8], jo, tp + 4096, 0); ALD4(tq[9], jo, tp + 4096, 512); ALD4(tq[10], jo, tp + 4096, 1024);
      const unsigned short* bp = b1 + ct * 112;
#define ALD2(m) asm volatile("global_load_dwordx2 %0, %1, %2 offset:" #m "*32" : "=v"(bias[m]) : "v"(bo), "s"(bp))
      ALD2(0); ALD2(1); ALD2(2); ALD2(3); ALD2(4); ALD2(5); ALD2(6);
    }
    SB();
    __builtin_amdgcn_s_waitcnt(0x4F72);
    MEMF();
    __builtin_amdgcn_s_barrier();
    MEMF();
    STAMP(kid, 0);
    const char* tb = ws + OFF_T + s2 * TPB;
    const u32 t16 = t * 16;
    ALD4S(in[0], t16, tb); ALD4S(in[1], t16, tb + 4096); ALD4S(in[2], t16, tb + 8192); ALD4S(in[3], t16, tb + 12288);
    SB();
    asm volatile("s_waitcnt vmcnt(0)" : "+v"(in[0]), "+v"(in[1]), "+v"(in[2]), "+v"(in[3]), "+v"(tq[0]), "+v"(tq[1]), "+v"(tq[2]), "+v"(tq[3]),
                 "+v"(tq[4]), "+v"(tq[5]), "+v"(tq[6]), "+v"(tq[7]), "+v"(tq[8]), "+v"(tq[9]), "+v"(tq[10]), "+v"(bias[0]), "+v"(bias[1]),
                 "+v"(bias[2]), "+v"(bias[3]), "+v"(bias[4]), "+v"(bias[5]), "+v"(bias[6]), "+v"(xin[0]), "+v"(xin[1]), "+v"(xin[2]),
                 "+v"(xin[3]), "+v"(xin[4]), "+v"(xin[5]), "+v"(xin[6]), "+v"(xin[7]));
    auto stale = [&]() { const u4v a = (in[0] & in[1]) & (in[2] & in[3]); return __builtin_amdgcn_ballot_w64(((a.x & a.y) & (a.z & a.w) & 1u) == 0) != 0; };
    if (stale()) {
      do {
        asm volatile("global_load_dwordx4 %0, %4, %5 sc1\n\tglobal_load_dwordx4 %1, %4, %6 sc1\n\tglobal_load_dwordx4 %2, %4, %7 sc1\n\t"
                     "global_load_dwordx4 %3, %4, %8 sc1\n\ts_waitcnt vmcnt(0)"
                     : "=&v"(in[0]), "=&v"(in[1]), "=&v"(in[2]), "=&v"(in[3])
                     : "v"(t16), "s"(tb), "s"(tb + 4096), "s"(tb + 8192), "s"(tb + 12288) : "memory");
      } while (stale() && --budget > 0);
    }
    const u4v sv = u4v{SENT, SENT, SENT, SENT};
    const u32 xo = drop + OFF_XR + ((256 * (lane >> 3) + 8 * rt2 + 2 * w) * D_ + 64 * ct2) * 2 + (lane & 7) * 16;
    SB();
#pragma unroll
    for (int q = 0; q < 3; ++q) bst4<16>(rs, oh + hst + q * 1024, sv);
    W1DMA(16, 19);
    STAMP(kid, 1);
    f2 X[8];
    {
      auto lo = [](u4v v) { return f2{__uint_as_float(v.x), __uint_as_float(v.y)}; };
      auto hi = [](u4v v) { return f2{__uint_as_float(v.z), __uint_as_float(v.w)}; };
      X[0] = lo(in[0]); X[4] = hi(in[0]); X[1] = lo(in[1]); X[7] = hi(in[1]);
      X[2] = lo(in[2]); X[6] = hi(in[2]); X[3] = lo(in[3]); X[5] = hi(in[3]);
    }
#pragma unroll
    for (int k = 1; k < 8; ++k) X[k] = cmulc_s(X[k], f2{tx[2 * k], tx[2 * k + 1]});
    fft8<true>(X);
#pragma unroll
    for (int r = 0; r < 8; ++r) lds[r * RP + t] = X[r];
    SB();
#pragma unroll
    for (int q = 0; q < 3; ++q) bst4<16>(rs, oh + hst + 57344 + q * 1024, sv);
    W1DMA(19, 22);
    f2 tw1[8], twu[8], tw2[2][4];
    {
      f2 tv[22];
#pragma unroll
      for (int i = 0; i < 11; ++i) {
        tv[2 * i] = f2{__uint_as_float(tq[i].x), __uint_as_float(tq[i].y)};
        tv[2 * i + 1] = f2{__uint_as_float(tq[i].z), __uint_as_float(tq[i].w)};
      }
#pragma unroll
      for (int i = 0; i < 8; ++i) twu[i] = tv[i];
#pragma unroll
      for (int a = 0; a < 2; ++a)
#pragma unroll
        for (int i = 1; i < 4; ++i) tw2[a][i] = tv[8 + 3 * a + (i - 1)];
#pragma unroll
      for (int q = 1; q < 8; ++q) tw1[q] = tv[13 + q];
    }
    bar();
    f2* L = lds + s1 * RP;
    f2 z[8];
#pragma unroll
    for (int i = 0; i < 8; ++i) {
      int k = j + 32 * (i >> 2) + 64 * (i & 3);
      f2 A = L[k], Bc = L[(256 - k) & 255];
      f2 E = fma2(Bc, f2{1.f, -1.f}, A);
      f2 Dm = fma2(Bc, f2{-1.f, 1.f}, A);
      f2 zz = cmulc_add(Dm, twu[i], E);
      if (i == 0 && j == 0) zz = f2{A.x + A.y, A.x - A.y};
      z[i] = zz;
    }
    wbar();
    SB();
    bst4<16>(rs, oh + htl, sv);
    bst4<16>(rs, xo, sv);
    bst4<16>(rs, xo + 1024, sv);
    W1DMA(22, 25);
    fft4<true>(z[0], z[1], z[2], z[3]);
    fft4<true>(z[4], z[5], z[6], z[7]);
#pragma unroll
    for (int a = 0; a < 2; ++a)
#pragma unroll
      for (int i = 1; i < 4; ++i) z[4 * a + i] = cmulc(z[4 * a + i], tw2[a][i]);
#pragma unroll
    for (int i = 0; i < 8; ++i) L[(i & 3) * 72 + 32 * (i >> 2) + j] = z[i];
    wbar();
#pragma unroll
    for (int q = 0; q < 8; ++q) z[q] = L[(j & 3) * 72 + 8 * q + (j >> 2)];
    W1DMA(25, 28);
    fft8<true>(z);
#pragma unroll
    for (int m = 0; m < 8; ++m) L[(j >> 2) * 36 + (j & 3) + 4 * m] = z[m];
    wbar();
#pragma unroll
    for (int q = 0; q < 8; ++q) z[q] = L[q * 36 + j];
#pragma unroll
    for (int q = 1; q < 8; ++q) z[q] = cmulc(z[q], tw1[q]);
    fft8<true>(z);
    u32 x1[8], o[8];
#pragma unroll
    for (int m = 0; m < 8; ++m) { x1[m] = pack2(unpk(xin[m]) + rbf2(z[m])); z[m] = unpk(x1[m]); }
    const int rho = 8 * s2 + s1;
    u32* Lw = (u32*)L;
#pragma unroll
    for (int m = 0; m < 8; ++m) Lw[32 * m + j] = x1[m];
    wbar();
    u4v xa = *(const u4v*)(Lw + 4 * j), xb = *(const u4v*)(Lw + 128 + 4 * j);
    layernorm8(z, o);
#pragma unroll
    for (int m = 0; m < 8; ++m) o[m] = nosent(o[m]);
    char* sg = smem + LDS_W1 + 20480;
    *(u4v*)(sg + j * 288 + s1 * 16) = u4v{o[0], o[1], o[2], o[3]};
    *(u4v*)(sg + j * 288 + 144 + s1 * 16) = u4v{o[4], o[5], o[6], o[7]};
    wait_vm0();
    bar();
    const char* Lr = sg + (16 * w + (lane >> 3)) * 144 + (lane & 7) * 16;
    const u4v fa = *(const u4v*)Lr, fb = *(const u4v*)(Lr + 8 * 144);
    const u32 lo = ol + lno;
    SB();
    bst4<16>(rs, lo, fa);
    bst4<16>(rs, lo + 2048, fb);
    SB();
    bst4<16>(rs, OFF_X1P + rho * 1024 + j * 16, xa);
    bst4<16>(rs, OFF_X1P + rho * 1024 + j * 16 + 512, xb);
  }
  STAMP(kid, 2);
  u4v hf[14][4];
  u32 hoff[4];
#pragma unroll
  for (int h = 0; h < 4; ++h) hoff[h] = oh + (4 * rt2 + ((w + h) & 3)) * 56 * 1024;
#define HFLDX(n, A) do { _Pragma("unroll") for (int h_ = 0; h_ < 4; ++h_) hf[n][h_] = bld4s<A>(rs, l16 + KI(n) * 1024, hoff[h_] + KB(n) * 1024); } while (0)
#define HFLD(n) HFLDX(n, 16)
#define HFLDF(n) HFLDX(n, FAUX)
#define HFMX(n) mx4(mx4(mx4(mx4(0, hf[n][0]), hf[n][1]), hf[n][2]), hf[n][3])
#define CMPS(bad, m) asm volatile("v_cmp_eq_u32 %0, -1, %1" : "=s"(bad) : "v"(m))
  {
    u4v av[2][16];
#define AVLDX(ks, A) do { _Pragma("unroll") for (int h_ = 0; h_ < 2; ++h_) av[h_][ks] = bld4s<A>(rs, l16, avo[h_][ks]); } while (0)
#define AVLD(ks) AVLDX(ks, 16)
    u32 avo[2][16];
#pragma unroll
    for (int h = 0; h < 2; ++h) {
#pragma unroll
      for (int ks = 0; ks < 16; ++ks) avo[h][ks] = ol + (tt0 + h) * 16384 + ((ks + ct) & 15) * 1024;
      asm volatile("" : "+s"(avo[h][0]), "+s"(avo[h][1]), "+s"(avo[h][2]), "+s"(avo[h][3]), "+s"(avo[h][4]), "+s"(avo[h][5]), "+s"(avo[h][6]), "+s"(avo[h][7]),
                   "+s"(avo[h][8]), "+s"(avo[h][9]), "+s"(avo[h][10]), "+s"(avo[h][11]), "+s"(avo[h][12]), "+s"(avo[h][13]), "+s"(avo[h][14]), "+s"(avo[h][15]) : "v"(l16));
    }
    SB();
    wait_vm0();
    __builtin_amdgcn_s_barrier();
    MEMF();
    STAMP(kid, 3);
    f4 acc[7][2];
    u32 o0[8];
#define GELU8(p, o) do { f2 a_[8], bs_[8]; \
      _Pragma("unroll") for (int h = 0; h < 2; ++h) { \
        f4 a0 = acc[2 * (p)][h], a1 = acc[2 * (p) + 1][h]; \
        a_[4 * h] = f2{a0[0], a0[1]}; a_[4 * h + 1] = f2{a0[2], a0[3]}; a_[4 * h + 2] = f2{a1[0], a1[1]}; a_[4 * h + 3] = f2{a1[2], a1[3]}; \
        bs_[4 * h] = unpk(bias[2 * (p)].x); bs_[4 * h + 1] = unpk(bias[2 * (p)].y); bs_[4 * h + 2] = unpk(bias[2 * (p) + 1].x); bs_[4 * h + 3] = unpk(bias[2 * (p) + 1].y); } \
      SB(); W2LD(W2E + 2 * (p)); W2LD(W2E + 2 * (p) + 1); \
      if ((p) == HPOS) { _Pragma("unroll") for (int n = 0; n < PD; ++n) HFLDF(n); } \
      SB(); actN<8>(a_, bs_, o); } while (0)
#define HST8(p, o) do { _Pragma("unroll") for (int h = 0; h < 2; ++h) \
        bst4<16>(rs, oh + hst + h * 57344 + (p) * 1024, u4v{o[4 * h], o[4 * h + 1], o[4 * h + 2], o[4 * h + 3]}); } while (0)
    for (int redo = 0;; ++redo) {
      do {
        MEMF();
#pragma unroll
        for (int ks = 0; ks < PA; ++ks) AVLD(ks);
        u32 m = 0;
#pragma unroll
        for (int ks = 0; ks < PA; ++ks) { m = mx4(m, av[0][ks]); m = mx4(m, av[1][ks]); }
        if (!anysent(m)) break;
      } while (--budget > 0);
      STAMP(kid, 4);
      TSTEP(31);
      SB();
#pragma unroll
      for (int jt = 0; jt < 7; ++jt) { acc[jt][0] = f4{0, 0, 0, 0}; acc[jt][1] = f4{0, 0, 0, 0}; }
      const char* lb = smem + l16;
      u4v wf[2][7];
#pragma unroll
      for (int jt = 0; jt < 7; ++jt) wf[0][jt] = *(const u4v*)(lb + jt * 1024);
      SB();
#pragma unroll
      for (int ks = 0; ks < 16; ++ks) {
        const int cu = ks & 1, nx = cu ^ 1;
        const bool chk = ks + 1 >= PA && ks < 15;
        u32 m = 0;
        unsigned long long bad = 0;
#pragma unroll
        for (int jt = 0; jt < 7; ++jt) {
          bf8v wfb = __builtin_bit_cast(bf8v, wf[cu][jt]);
          acc[jt][0] = __builtin_amdgcn_mfma_f32_16x16x32_bf16(wfb, __builtin_bit_cast(bf8v, av[0][ks]), acc[jt][0], 0, 0, 0);
          SB();
          if (ks < 15) wf[nx][jt] = *(const u4v*)(lb + ((ks + 1) * 7 + jt) * 1024);
          SB();
          acc[jt][1] = __builtin_amdgcn_mfma_f32_16x16x32_bf16(wfb, __builtin_bit_cast(bf8v, av[1][ks]), acc[jt][1], 0, 0, 0);
          SB();
          if (ks + PA < 16 && jt < 2) av[jt][ks + PA] = bld4s<FAUX>(rs, l16, avo[jt][ks + PA]);
          if (ks >= 16 - W2E && jt < 4) W2LD1(ks - (16 - W2E), jt);
          if (chk && jt == 4) asm volatile("v_max_u32 %0, %1, %2" : "=v"(m) : "v"(av[0][ks + 1].x), "v"(av[1][ks + 1].x));
          if (chk && jt == 5) CMPS(bad, m);
          SB();
        }
        TSTEP(ks);
        if (chk) {
          MEMF();
          if (__builtin_expect(bad != 0, 0)) {
            NSLOW();
            do {
              MEMF();
#pragma unroll
              for (int k2 = ks + 1; k2 < 16 && k2 <= ks + PA; ++k2) AVLD(k2);
              wait_vm0();
            } while (anysent(mx4(mx4(0, av[0][ks + 1]), av[1][ks + 1])) && --budget > 0);
          }
          SB();
        }
      }
      STAMP(kid, 5);
      const bool nan = __builtin_amdgcn_ballot_w64(__builtin_isunordered(acc[0][0][0], acc[0][1][0])) != 0;
      MEMF();
      if (__builtin_expect(!nan || redo >= 3, 1)) break;
    }
    GELU8(0, o0);
    HST8(0, o0);
    {
      u32 o[8];
      GELU8(1, o);
      HST8(1, o);
      GELU8(2, o);
      HST8(2, o);
    }
    {
      f4 a0 = acc[6][0], a1 = acc[6][1];
      f2 a[4] = {f2{a0[0], a0[1]}, f2{a0[2], a0[3]}, f2{a1[0], a1[1]}, f2{a1[2], a1[3]}};
      f2 bs[4] = {unpk(bias[6].x), unpk(bias[6].y), unpk(bias[6].x), unpk(bias[6].y)};
      u32 o4[4];
      SB();
      W2LD(13);
      __builtin_amdgcn_s_barrier();
      MEMF();
      if (HPOS == 3) {
#pragma unroll
        for (int n = 0; n < PD; ++n) HFLDF(n);
      }
      SB();
      actN<4>(a, bs, o4);
      auto rx = __builtin_amdgcn_permlane16_swap(o4[0], o4[2], false, false);
      auto ry = __builtin_amdgcn_permlane16_swap(o4[1], o4[3], false, false);
      bst4<16>(rs, oh + htl, u4v{rx[0], ry[0], rx[1], ry[1]});
    }
  }
  STAMP(kid, 6);
  R1Tw tw;
  {
    f4 acc[4][4];
    u4v bb0 = *(const u4v*)(b2 + 64 * ct2 + 16 * g), bb1 = *(const u4v*)(b2 + 64 * ct2 + 16 * g + 8);
    const u32 pvo = oh + (((4 * rt2 + w) * 56 + 7 * (4 * (rt2 & 1) + w) + 3 * (lane >> 5)) * 64 + (lane & 31)) * 16;
    u32 pv = 0;
    u4v x1a, x1b;
    f4* red = (f4*)(smem + LDS_W1);
    for (int redo = 0;; ++redo) {
      SB();
      MEMF();
      if (__builtin_expect(redo != 0, 0)) {
#pragma unroll
        for (int n = 0; n < 14; ++n) W2LD(n);
#pragma unroll
        for (int n = 0; n < PD; ++n) HFLD(n);
        wait_vm0();
      }
      if (anysent(HFMX(0))) {
        do {
          MEMF();
#pragma unroll
          for (int n = 0; n < PD; ++n) HFLD(n);
          wait_vm0();
        } while (anysent(HFMX(0)) && --budget > 0);
      }
#pragma unroll
      for (int i = 0; i < 4; ++i)
#pragma unroll
        for (int h = 0; h < 4; ++h) acc[i][h] = f4{0, 0, 0, 0};
      STAMP(kid, 8);
      TSTEP(30);
      SB();
#pragma unroll
      for (int n = 0; n < 14; ++n) {
        u32 m = 0;
        unsigned long long bad = 0;
#pragma unroll
        for (int q = 0; q < 8; ++q) {
          const int h = q >> 1, i = 2 * (q & 1);
          acc[i][h] = __builtin_amdgcn_mfma_f32_16x16x32_bf16(__builtin_bit_cast(bf8v, w2f[n][i]), __builtin_bit_cast(bf8v, hf[n][h]), acc[i][h], 0, 0, 0);
          SB();
          if (n + PD < 14 && q < 4) hf[n + PD][q] = bld4s<FAUX>(rs, l16 + KI(n + PD) * 1024, hoff[q] + KB(n + PD) * 1024);
          if (n == 4 && q == 4) pv = bld1<16>(rs, pvo);
          if (n < 13 && q == 5) asm volatile("v_max3_u32 %0, %1, %2, %3" : "=v"(m) : "v"(hf[n + 1][0].x), "v"(hf[n + 1][1].x), "v"(hf[n + 1][2].x));
          if (n < 13 && q == 6) CMPS(bad, m);
          SB();
          acc[i + 1][h] = __builtin_amdgcn_mfma_f32_16x16x32_bf16(__builtin_bit_cast(bf8v, w2f[n][i + 1]), __builtin_bit_cast(bf8v, hf[n][h]), acc[i + 1][h], 0, 0, 0);
          SB();
          if (n < 13 && n != 8 && q == 5) asm volatile("v_max_u32 %0, %0, %1" : "+v"(m) : "v"(hf[n + 1][3].x));
          if (n == 8 && q == 5) asm volatile("v_max3_u32 %0, %0, %1, %2" : "+v"(m) : "v"(hf[n + 1][3].x), "v"(pv));
          SB();
        }
        TSTEP(16 + n);
        if (n < 13) {
          MEMF();
          if (__builtin_expect(bad != 0, 0)) {
            NSLOW();
            do {
              MEMF();
#pragma unroll
              for (int n2 = n + 1; n2 < 14 && n2 <= n + PD; ++n2) HFLD(n2);
              if (n == 8) pv = bld1<16>(rs, pvo);
              wait_vm0();
              m = HFMX(n + 1);
              if (n == 8) m = mx2(m, pv);
            } while (anysent(m) && --budget > 0);
          }
          SB();
        }
        if (n == 8) {
          MEMF();
          x1a = bld4<16>(rs, OFF_X1P + rho2 * 1024 + ct2 * 128 + g * 32);
          x1b = bld4<16>(rs, OFF_X1P + rho2 * 1024 + ct2 * 128 + g * 32 + 16);
          SB();
        }
      }
      STAMP(kid, 9);
      r1_load_tw(tw, t);
      SB();
      const bool nan = __builtin_amdgcn_ballot_w64(__builtin_isunordered(acc[0][0][0], acc[0][1][0]) || __builtin_isunordered(acc[0][2][0], acc[0][3][0])) != 0;
#pragma unroll
      for (int h = 1; h < 4; ++h)
#pragma unroll
        for (int i = 0; i < 4; ++i) red[((((w + h) & 3) * 3 + (h - 1)) * 4 + i) * 64 + lane] = acc[i][h];
      MEMF();
      if (__builtin_expect(!nan || redo >= 3, 1)) break;
    }
    bar();
    f2 s[8];
#pragma unroll
    for (int i = 0; i < 4; ++i) {
      f4 a = (acc[i][0] + red[((w * 3 + 0) * 4 + i) * 64 + lane]) + (red[((w * 3 + 1) * 4 + i) * 64 + lane] + red[((w * 3 + 2) * 4 + i) * 64 + lane]);
      s[2 * i] = f2{a[0], a[1]}; s[2 * i + 1] = f2{a[2], a[3]};
    }
    const u32 x1v[8] = {x1a.x, x1a.y, x1a.z, x1a.w, x1b.x, x1b.y, x1b.z, x1b.w};
    u32 bs[8] = {bb0.x, bb0.y, bb0.z, bb0.w, bb1.x, bb1.y, bb1.z, bb1.w};
    u32 o[8];
#pragma unroll
    for (int i = 0; i < 8; ++i) o[i] = pack2(unpk(x1v[i]) + rbf2(rbf2(s[i]) + unpk(bs[i])));
#pragma unroll
    for (int i = 0; i < 8; ++i) o[i] = nosent(o[i]);
    char* st = smem + LDS_W1 + w * 12288;
    wbar();
    *(u4v*)(st + c * 144 + g * 32) = u4v{o[0], o[1], o[2], o[3]};
    *(u4v*)(st + c * 144 + g * 32 + 16) = u4v{o[4], o[5], o[6], o[7]};
    wbar();
    const u4v ra = *(const u4v*)(st + (lane >> 3) * 144 + (lane & 7) * 16), rb = *(const u4v*)(st + ((lane >> 3) + 8) * 144 + (lane & 7) * 16);
    const u32 xo = ((256 * (lane >> 3) + 8 * rt2 + 2 * w) * D_ + 64 * ct2) * 2 + (lane & 7) * 16;
    if (last) {
      *(u4v*)((char*)out + xo) = ra;
      *(u4v*)((char*)out + xo + 1024) = rb;
      STAMP(kid, 10);
      return;
    }
    bst4<16>(rs, OFF_XR + xo, ra);
    bst4<16>(rs, OFF_XR + xo + 1024, rb);
  }
  STAMP(kid, 10);
  asm volatile("" : "+v"(t));
  {
    u32 xin[8];
    const u32 ro = OFF_XR + ((256 * (t >> 5) + s2) * 256 + (t & 31)) * 4;
    wait_vm0();
    do {
      MEMF();
#pragma unroll
      for (int m = 0; m < 8; ++m) xin[m] = bld1<16>(rs, ro + 128 * m);
    } while (anysent(mx2(mx2(mx2(xin[0], xin[1]), mx2(xin[2], xin[3])), mx2(mx2(xin[4], xin[5]), mx2(xin[6], xin[7])))) && --budget > 0);
    STAMP(kid, 11);
    r1_body<16, 1536, RP>(xin, tw, tx, lds, rs, s2, t);
    STAMP(kid, 12);
  }
  last = 1;
  }
}

extern "C" __device__ __attribute__((used, noinline)) void z_end() {}

extern "C" void kernel_launch(void* const* d_in, const int* in_sizes, int n_in, void* d_out, int out_size, void* d_ws,
                              size_t ws_size, hipStream_t stream) {
  const unsigned short* x = (const unsigned short*)d_in[0];
  const unsigned short* wr = (const unsigned short*)d_in[1];
  const unsigned short* wi = (const unsigned short*)d_in[2];
  const unsigned short* w1 = (const unsigned short*)d_in[4];
  const unsigned short* b1 = (const unsigned short*)d_in[5];
  const unsigned short* w2 = (const unsigned short*)d_in[6];
  const unsigned short* b2 = (const unsigned short*)d_in[7];
  char* ws = (char*)d_ws;
  (void)hipFuncSetAttribute((const void*)k_m, hipFuncAttributeMaxDynamicSharedMemorySize, LDS_M);
  k_first<<<512, 256, 0, stream>>>((const u32*)x, wr, wi, w1, w2, ws);
  k_m<<<256, 256, LDS_M, stream>>>((const u32*)x, b1, b2, ws, (unsigned short*)d_out);
}
```
